```python
import torch

def _softmax(x, axis=-1):
    return torch.softmax(x, dim=axis)
def _gelu(x):
    return torch.nn.functional.gelu(x, approximate="tanh")
def _silu(x):
    return torch.nn.functional.silu(x)
def _sigmoid(x):
    return torch.sigmoid(x)
def _layernorm(x, axis=-1, eps=1e-5):
    m = x.mean(dim=axis, keepdim=True)
    v = x.var(dim=axis, keepdim=True, unbiased=False)
    return (x - m) / torch.sqrt(v + eps)
def _scatter_add(tgt, idx, src, axis=0):
    return tgt.clone().index_add_(axis, idx, src)
def _segment_sum(src, seg, n):
    out = torch.zeros((n,) + src.shape[1:], dtype=src.dtype)
    return out.index_add_(0, seg, src)
def _pad(x, pw):
    flat = tuple(v for pair in reversed(pw) for v in pair)
    return torch.nn.functional.pad(x, flat)

kernel_name = 'recurrent_scan__ssm_conv_s4d__moe_arch_mod__prompt'

def setup_inputs():

    _w = (lambda a: (lambda t: t.to(torch.bfloat16) if t.is_floating_point() else t)(torch.as_tensor(a)))
    def _A_setup():
        Bsz, L, D, N = 1, 2048, 1024, 128
        return {
            "u": _w((lambda *s: torch.randn(*s, dtype=torch.bfloat16) * 0.05)(Bsz, L, D)),
            "log_A_real": _w((lambda *s: torch.randn(*s, dtype=torch.bfloat16) * 6.4)(D, N) * 0.5),
            "A_imag": _w((lambda *s: torch.randn(*s, dtype=torch.bfloat16) * 0.05)(D, N) * 3.14),
            "C_real": _w((lambda *s: torch.randn(*s, dtype=torch.bfloat16) * 0.05)(D, N) / N ** 0.5),
            "C_imag": _w((lambda *s: torch.randn(*s, dtype=torch.bfloat16) * 0.05)(D, N) / N ** 0.5),
            "log_dt": _w((lambda *s: torch.randn(*s, dtype=torch.bfloat16) * 0.05)(D) * 0.5 - 4.0),
            "D_skip": _w((lambda *s: torch.randn(*s, dtype=torch.bfloat16) * 0.05)(D)),
        }
    _A_in = _A_setup()

    def _B_setup():
        B, S, D, F, C = 1, 2048, 1024, 3584, 2
        return {
            "x": _w((lambda *s: torch.randn(*s, dtype=torch.bfloat16) * 0.05)(B, S, D)),
            "w_r": _w((lambda *s: torch.randn(*s, dtype=torch.bfloat16) * 0.05)(D)),
            "w_gate": _w((lambda *s: torch.randn(*s, dtype=torch.bfloat16) * 0.05)(D, F)),
            "w_up": _w((lambda *s: torch.randn(*s, dtype=torch.bfloat16) * 0.05)(D, F)),
            "w_down": _w((lambda *s: torch.randn(*s, dtype=torch.bfloat16) * 0.05)(F, D)),
        }
    _B_in = _B_setup()

    return {
        "A_u": _A_in["u"],
        "A_log_A_real": _A_in["log_A_real"],
        "A_A_imag": _A_in["A_imag"],
        "A_C_real": _A_in["C_real"],
        "A_C_imag": _A_in["C_imag"],
        "A_log_dt": _A_in["log_dt"],
        "A_D_skip": _A_in["D_skip"],
        "B_x": _B_in["x"],
        "B_w_r": _B_in["w_r"],
        "B_w_gate": _B_in["w_gate"],
        "B_w_up": _B_in["w_up"],
        "B_w_down": _B_in["w_down"],
    }

def reference(A_u, A_log_A_real, A_A_imag, A_C_real, A_C_imag, A_log_dt, A_D_skip, B_x, B_w_r, B_w_gate, B_w_up, B_w_down):
    def _A_ref(u, log_A_real, A_imag, C_real, C_imag, log_dt, D_skip):
        L = u.shape[1]
        dt = torch.exp(log_dt)
        A = -torch.exp(log_A_real) + 1j * A_imag
        C = (C_real + 1j * C_imag) * dt[:, None]
        pos = (lambda x, dt: x.to(dt))(torch.arange(L), log_dt.dtype)
        P = torch.exp((A * dt[:, None])[:, :, None] * pos[None, None, :])
        K = 2.0 * torch.einsum("dn,dnl->dl", C, P).real
        L2 = 2 * L
        u_t = (lambda x, *d: x.permute(*d))(u, 0, 2, 1)
        Kf = (lambda x, *a, **k: torch.fft.rfft(x.float(), *a, **k))(K, L2)
        Uf = (lambda x, *a, **k: torch.fft.rfft(x.float(), *a, **k))(u_t, L2)
        y = (lambda x, *a, **k: torch.fft.irfft(x, *a, **k).to(torch.bfloat16))(Uf * Kf[None, :, :], L2)[:, :, :L]
        y = (lambda x, *d: x.permute(*d))(y, 0, 2, 1)
        return y + u * D_skip[None, None, :]

    def _B_ref(x, w_r, w_gate, w_up, w_down):
        B, S, D = x.shape
        C = 2
        r = x @ w_r
        idx = torch.argsort(-r, axis=-1)[:, :C]
        idx_e = (lambda x, n, axis: torch.repeat_interleave(x, n, dim=axis))(idx.reshape(B, C, 1), D, axis=2)
        x_sel = (lambda x, i, axis: torch.take_along_dim(x, i, dim=axis))(x, idx_e, axis=1)
        g = _silu(x_sel @ w_gate)
        u = x_sel @ w_up
        y = (g * u) @ w_down
        r_sel = _sigmoid((lambda x, i, axis: torch.take_along_dim(x, i, dim=axis))(r, idx, axis=1))
        y = y * r_sel.reshape(B, C, 1)
        b_off = (torch.arange(B) * S).reshape(B, 1)
        flat_idx = (idx + b_off).reshape(B * C)
        zero = (x * 0.0).reshape(B * S, D)
        upd = _scatter_add(zero, flat_idx, y.reshape(B * C, D), axis=0)
        return x + upd.reshape(B, S, D)

    x = A_u
    xn = _layernorm(x, axis=-1)
    s = _A_ref(xn, A_log_A_real, A_A_imag, A_C_real, A_C_imag, A_log_dt, A_D_skip)
    g = _silu(_B_ref(xn, B_w_r, B_w_gate, B_w_up, B_w_down))
    return x + s * g
```

```cpp
#include <hip/hip_runtime.h>
#include <stdint.h>
#include <type_traits>

typedef uint16_t u16;
typedef uint32_t u32;
typedef _Float16 f16;
typedef __attribute__((ext_vector_type(8))) _Float16 h8;
typedef __attribute__((ext_vector_type(2))) _Float16 h2;
typedef __attribute__((ext_vector_type(2))) __bf16 b2;
typedef __attribute__((ext_vector_type(8))) __bf16 b8;
typedef __attribute__((ext_vector_type(4))) float f4;
typedef __attribute__((ext_vector_type(2))) float f2;
typedef __attribute__((ext_vector_type(4))) u32 u4;
typedef __attribute__((ext_vector_type(2))) u32 u2;

constexpr int SL = 2048, SD = 1024, SN = 128, SF = 3584;
constexpr int GU_WORDS = 128 * 4 * 16 * 8 * 4;
constexpr int DN_WORDS = 128 * 8 * 64 * 4;
#define SPIN_LOG 20
constexpr int SPIN_MAX = 1 << SPIN_LOG;

#define DEV __device__ __forceinline__
#define STAMP_DECL
#define STAMP(k)
#define STAMP_FLUSH(kn)
#define STAMP_ARG
#define STAMP_PAR
#define WAVE_SYNC() do { __builtin_amdgcn_fence(__ATOMIC_ACQ_REL, "wavefront"); __builtin_amdgcn_wave_barrier(); } while (0)
#define PIN(v) asm volatile("" : "+v"(v))
#define SCHED_FENCE() __builtin_amdgcn_sched_barrier(0)
#define MEM_FENCE() asm volatile("" ::: "memory")

DEV float bflo(u32 v) { return __uint_as_float(v << 16); }
DEV float bfhi(u32 v) { return __uint_as_float(v & 0xffff0000u); }
DEV f2 bfup(u32 v) { f2 r; r.x = bflo(v); r.y = bfhi(v); return r; }
DEV u32 pkbf(f2 v) { b2 t = __builtin_convertvector(v, b2); return __builtin_bit_cast(u32, t); }
DEV u32 pkbf(float a, float b) { f2 v; v.x = a; v.y = b; return pkbf(v); }
DEV u32 pkh(f2 v) { h2 t = __builtin_convertvector(v, h2); return __builtin_bit_cast(u32, t); }
DEV u32 pkh(float a, float b) { f2 v; v.x = a; v.y = b; return pkh(v); }
DEV float rbf(float a) { return __uint_as_float(pkbf(0.0f, a)); }
DEV f2 rbf2(f2 a) { f2 r; r.x = rbf(a.x); r.y = rbf(a.y); return r; }
#define KNOB(name, value) constexpr int name = value
KNOB(NREP, 16);
KNOB(WS_OFF, -10);
KNOB(CODE_KB, 24);
KNOB(W_DELAY, 0);
KNOB(W_A, 4);
KNOB(W_B, 2);
KNOB(W_E, 6);
KNOB(DN_T, 0);
KNOB(KG_SW, 3);
KNOB(E_GP, 0);
KNOB(GU_L, 1);
KNOB(WD_DELAY, 0);
KNOB(B_GAP, 0);
KNOB(KX, 0);
KNOB(K_PRE, 40);
KNOB(K_DELAY, 16);
KNOB(WD_1, 4);
KNOB(CP_S, 2);
KNOB(CP_0, 2);
KNOB(CP_1, 1);
KNOB(CP_2, 1);
KNOB(E_ORD, 1);
KNOB(E_EP, 3);
KNOB(P_LIGHT, 3);
KNOB(US_AUX, 16);
KNOB(W_AUX, 2);
KNOB(W_GATE, 0);
KNOB(CV_P0, 2);
KNOB(CV_P1, 1);
KNOB(W_PACE, 6);
KNOB(W_PACE2, 4);
KNOB(D_PACE, 0);
KNOB(U_AUX, 16);
KNOB(A_DELAY, 12);
KNOB(U_DELAY, 22);
KNOB(U_POS, 1);
KNOB(WD_POS, 1);
KNOB(G_DELAY, 16);
KNOB(G_SLEEP, 2);
KNOB(A_SLEEP, 4);
KNOB(B_DELAY, 20);
KNOB(B_SLEEP, 2);
KNOB(PACE, 3);
KNOB(XT_PRE, 2);
KNOB(XT_MID, 2);
KNOB(E_UP, 2);
KNOB(E_SLP, 1);
KNOB(F_SLP, 2);
KNOB(CV_SW, -1);
KNOB(KMAP, 1);
KNOB(X_NT, 0);
KNOB(E_FIRST, 0);
KNOB(BOX_POS, 0);
KNOB(X_SLEEP, 2);
KNOB(E_PRIO, 2);
constexpr float LOG2E = 1.4426950408889634f;
DEV float silu_fast(float a) { return a * __builtin_amdgcn_rcpf(1.0f + __builtin_amdgcn_exp2f(-1.4426950408889634f * a)); }

template <int CTRL, int RMASK>
DEV float dpp_add(float v) {
    int t = __builtin_amdgcn_update_dpp(0, __float_as_int(v), CTRL, RMASK, 0xf, false);
    return v + __int_as_float(t);
}
DEV float wave_sum(float v) {
    v = dpp_add<0x111, 0xf>(v);
    v = dpp_add<0x112, 0xf>(v);
    v = dpp_add<0x114, 0xf>(v);
    v = dpp_add<0x118, 0xf>(v);
    v = dpp_add<0x142, 0xa>(v);
    v = dpp_add<0x143, 0xc>(v);
    return __int_as_float(__builtin_amdgcn_readlane(__float_as_int(v), 63));
}
DEV float fold4(float a, float b, float c, float d) {
    const auto s0 = __builtin_amdgcn_permlane32_swap(__float_as_uint(a), __float_as_uint(b), false, false);
    const auto s1 = __builtin_amdgcn_permlane32_swap(__float_as_uint(c), __float_as_uint(d), false, false);
    const u32 w0 = __float_as_uint(__uint_as_float(s0[0]) + __uint_as_float(s0[1]));
    const u32 w1 = __float_as_uint(__uint_as_float(s1[0]) + __uint_as_float(s1[1]));
    const auto s2 = __builtin_amdgcn_permlane16_swap(w0, w1, false, false);
    return __uint_as_float(s2[0]) + __uint_as_float(s2[1]);
}
DEV float row_sum(float v) {
    v = dpp_add<0x128, 0xf>(v);
    v = dpp_add<0x124, 0xf>(v);
    v = dpp_add<0x122, 0xf>(v);
    return dpp_add<0x121, 0xf>(v);
}
#define TOP2(ctl) "v_max_u32_dpp %2, %1, %1 " ctl "\n\tv_min_u32_dpp %3, %0, %0 " ctl "\n\tv_max_u32_dpp %0, %0, %0 " ctl "\n\tv_max_u32 %1, %2, %3\n\ts_nop 1\n\t"
DEV void wave_top2(u32& m1, u32& m2) {
    u32 t, s;
    asm volatile("s_nop 1\n\t"
                 TOP2("row_shr:1 row_mask:0xf bank_mask:0xf bound_ctrl:1") TOP2("row_shr:2 row_mask:0xf bank_mask:0xf bound_ctrl:1")
                 TOP2("row_shr:4 row_mask:0xf bank_mask:0xf bound_ctrl:1") TOP2("row_shr:8 row_mask:0xf bank_mask:0xf bound_ctrl:1")
                 TOP2("row_bcast:15 row_mask:0xa bank_mask:0xf") TOP2("row_bcast:31 row_mask:0xc bank_mask:0xf")
                 : "+v"(m1), "+v"(m2), "=&v"(t), "=&v"(s));
}
DEV void top2_of8(u32& m1, u32& m2) {
    u32 t, s;
    asm volatile("s_nop 1\n\t"
                 TOP2("row_shr:1 row_mask:0xf bank_mask:0xf bound_ctrl:1") TOP2("row_shr:2 row_mask:0xf bank_mask:0xf bound_ctrl:1")
                 TOP2("row_shr:4 row_mask:0xf bank_mask:0xf bound_ctrl:1")
                 : "+v"(m1), "+v"(m2), "=&v"(t), "=&v"(s));
}
DEV u32 med3u(u32 a, u32 b, u32 c) { return max(min(a, b), min(max(a, b), c)); }
DEV float dot2(u32 a, u32 b, float c) { return __builtin_amdgcn_fdot2_f32_bf16(__builtin_bit_cast(b2, a), __builtin_bit_cast(b2, b), c, false); }

template <int CTRL> DEV float dpp_mov(float v) { return __int_as_float(__builtin_amdgcn_mov_dpp(__float_as_int(v), CTRL, 0xf, 0xf, true)); }
DEV u32 bfi(u32 m, u32 a, u32 b) { u32 r; asm("v_bfi_b32 %0, %1, %2, %3" : "=v"(r) : "v"(m), "v"(a), "v"(b)); return r; }
DEV void gate4(u32 (&g)[4], u32 u0, u32 u1, u32 u2, u32 u3) {
#define LO "dst_sel:WORD_0 dst_unused:UNUSED_PRESERVE src0_sel:WORD_0\n\t"
#define HI "dst_sel:WORD_1 dst_unused:UNUSED_PRESERVE src0_sel:WORD_1\n\t"
    asm volatile("v_exp_f16_sdwa %0, -%4 " LO "v_exp_f16_sdwa %1, -%5 " LO "v_exp_f16_sdwa %2, -%6 " LO "v_exp_f16_sdwa %3, -%7 " LO
                 "v_exp_f16_sdwa %0, -%4 " HI "v_exp_f16_sdwa %1, -%5 " HI "v_exp_f16_sdwa %2, -%6 " HI "v_exp_f16_sdwa %3, -%7 " HI
                 "v_pk_add_f16 %0, %0, 1.0 op_sel_hi:[1,0]\n\tv_pk_add_f16 %1, %1, 1.0 op_sel_hi:[1,0]\n\tv_pk_add_f16 %2, %2, 1.0 op_sel_hi:[1,0]\n\tv_pk_add_f16 %3, %3, 1.0 op_sel_hi:[1,0]\n\t"
                 "v_rcp_f16_sdwa %0, %0 " LO "v_rcp_f16_sdwa %1, %1 " LO "v_rcp_f16_sdwa %2, %2 " LO "v_rcp_f16_sdwa %3, %3 " LO
                 "v_rcp_f16_sdwa %0, %0 " HI "v_rcp_f16_sdwa %1, %1 " HI "v_rcp_f16_sdwa %2, %2 " HI "v_rcp_f16_sdwa %3, %3 " HI
                 "v_pk_mul_f16 %0, %0, %4\n\tv_pk_mul_f16 %1, %1, %5\n\tv_pk_mul_f16 %2, %2, %6\n\tv_pk_mul_f16 %3, %3, %7"
                 : "=&v"(g[0]), "=&v"(g[1]), "=&v"(g[2]), "=&v"(g[3]) : "v"(u0), "v"(u1), "v"(u2), "v"(u3));
#undef LO
#undef HI
}
struct cplx { f2 r, i; };
DEV cplx cmul(cplx a, cplx b) { cplx z; z.r = a.r * b.r - a.i * b.i; z.i = a.r * b.i + a.i * b.r; return z; }
DEV cplx cmul_conj(cplx g, cplx e) { cplx z; z.r = g.r * e.r + g.i * e.i; z.i = g.i * e.r - g.r * e.i; return z; }
DEV cplx cexp2(f2 ar, f2 ai, float p) {
    cplx z;
    const f2 am = ar * p, rv = ai * p;
    f2 mag, c, s;
#pragma unroll
    for (int k = 0; k < 2; ++k) {
        mag[k] = __builtin_amdgcn_exp2f(am[k]);
        const float rev = __builtin_amdgcn_fractf(rv[k]);
        c[k] = __builtin_amdgcn_cosf(rev);
        s[k] = __builtin_amdgcn_sinf(rev);
    }
    z.r = mag * c; z.i = mag * s;
    return z;
}
DEV float exp_acc(float x) {
    x = __builtin_amdgcn_fmed3f(x, -150.0f, 150.0f);
    const float t = x * 1.4426950408889634f;
    const float r = __builtin_fmaf(x, 1.925963033500011e-08f, __builtin_fmaf(x, 1.4426950408889634f, -t));
    return __builtin_amdgcn_exp2f(t) * __builtin_fmaf(r, 0.6931471805599453f, 1.0f);
}
DEV h8 lds_h8(const char* p) { return *reinterpret_cast<const h8*>(p); }
DEV f4 mfma16(h8 a, h8 b, f4 c) { return __builtin_amdgcn_mfma_f32_16x16x32_f16(a, b, c, 0, 0, 0); }
DEV f4 mfma16b(u4 a, u4 b, f4 c) {
    return __builtin_amdgcn_mfma_f32_16x16x32_bf16(__builtin_bit_cast(b8, a), __builtin_bit_cast(b8, b), c, 0, 0, 0);
}
typedef __attribute__((ext_vector_type(16))) __bf16 b16v;
DEV f4 smfma(u4 a, u4 b0, u4 b1, f4 c, u32 idx) {
    return __builtin_amdgcn_smfmac_f32_16x16x64_bf16(__builtin_bit_cast(b8, a), __builtin_bit_cast(b16v, __builtin_shufflevector(b0, b1, 0, 1, 2, 3, 4, 5, 6, 7)), c, (int)idx, 0, 0);
}
DEV void lds_add1(u32* p) {
    const u32 a = (u32)(uintptr_t)(__attribute__((address_space(3))) u32*)p, one = 1u;
    asm volatile("s_mov_b64 exec, 1\n\tds_add_u32 %0, %1\n\ts_mov_b64 exec, -1" :: "v"(a), "v"(one) : "memory");
}
template <int SLP = 1>
DEV void lds_wait(u32* cnt, u32 n) {
    while (__hip_atomic_load(cnt, __ATOMIC_RELAXED, __HIP_MEMORY_SCOPE_WORKGROUP) < n) __builtin_amdgcn_s_sleep(SLP);
    __builtin_amdgcn_fence(__ATOMIC_ACQUIRE, "wavefront");
    __builtin_amdgcn_wave_barrier();
}

constexpr int KREC = 1024;
constexpr int R_KA = 0, ZA = 543, R_K2 = 560, R_K3 = 812, R_KP = 972;
constexpr int STG_ROW = 544;
constexpr int STG_BUF = 37 * 256;
constexpr int REC_B = KREC * 2 + 64;
constexpr int REC_SZ = 2 * KREC * 2 + 128;
constexpr int UPAD = 256;
constexpr int C_U = REC_SZ;
constexpr int CH_SZ = C_U + (UPAD + SL) * 2 + 16;
static_assert(CH_SZ % 32 == 16, "bank offset of the channel regions");
constexpr int L_STG = 0;
constexpr int L_CH = L_STG + 8 * STG_BUF;
constexpr int L_BOX = L_CH + 8 * CH_SZ;
constexpr int XN_S = 512 + 64, H_S = 256 + 64;
constexpr int L_Z = L_BOX + 5 * 1024;
constexpr int L_XN = L_Z, L_H = L_XN + 4 * XN_S, Z_SZ = 4 * XN_S + 4 * H_S;
constexpr int L_CNT = L_Z + Z_SZ;
constexpr int L_P12 = L_CNT + 128, P12_S = 80;
constexpr int L_P3 = L_P12 + 8 * P12_S, P3_S = 48;
constexpr int L_NUL = L_P3 + 8 * P3_S;
constexpr int L_TOTAL = L_NUL + 256;
static_assert(L_TOTAL <= 160 * 1024 && Z_SZ % 16 == 0 && Z_SZ <= 8 * 1024, "LDS");

DEV h8 box_frag(int al, int q, int off, int hw) {
    const u32 m = (u32)((((2ull << (2 * hw)) - 1ull) << (al - 8 * q + off - hw + 32)) >> 32) & 0xffu;
    const u32 t = m | (m << 15);
    u4 r;
#pragma unroll
    for (int i = 0; i < 4; ++i) r[i] = ((t >> (2 * i)) & 0x10001u) * 0x3c00u;
    return __builtin_bit_cast(h8, r);
}
DEV u4 tag4(f4 v, u32 vtag) {
    u4 b = __builtin_bit_cast(u4, v);
#pragma unroll
    for (int i = 0; i < 4; ++i) b[i] = __builtin_amdgcn_perm(b[i], vtag, 0x07060500u + i);
    return b;
}
DEV f4 fold_halves(f4 a0, f4 a1) {
    f4 o;
#pragma unroll
    for (int i = 0; i < 4; ++i) {
        const f4 a = (i < 2) ? a0 : a1;
        float t = a[i & 1] + __int_as_float(__builtin_amdgcn_update_dpp(0, __float_as_int(a[2 + (i & 1)]), 0x128, 0xf, 0xf, true));
        PIN(t);
        o[i] = t;
    }
    return o;
}
template <int O0, int ST>
DEV void lds_wait_read4(u32 a_cnt, u32 n, u32 a, u4 (&v)[4]) {
    for (;;) {
        u32 c;
        asm volatile("ds_read_b32 %0, %5\n\tds_read_b128 %1, %6 offset:%7\n\tds_read_b128 %2, %6 offset:%8\n\tds_read_b128 %3, %6 offset:%9\n\t"
                     "ds_read_b128 %4, %6 offset:%10\n\ts_waitcnt lgkmcnt(0)"
                     : "=&v"(c), "=&v"(v[0]), "=&v"(v[1]), "=&v"(v[2]), "=&v"(v[3])
                     : "v"(a_cnt), "v"(a), "n"(O0), "n"(O0 + ST), "n"(O0 + 2 * ST), "n"(O0 + 3 * ST) : "memory");
        if (__builtin_amdgcn_readfirstlane(c) >= n) break;
        __builtin_amdgcn_s_sleep(1);
    }
}
DEV u32 tagof(u4 v) { return __builtin_amdgcn_perm(v.y, v.x, 0x0c0c0400u) | __builtin_amdgcn_perm(v.w, v.z, 0x04000c0cu); }
DEV u4 untag4(u4 v) { return v & 0xffffff00u; }
template <int N, int SLEEP, class LD, class BAD, class HOOK>
DEV void poll_pieces(u4 (&v)[N], LD ld, BAD bad, HOOK hook) {
#pragma unroll
    for (int p = 0; p < N; ++p) v[p] = ld(p);
    MEM_FENCE(); SCHED_FENCE();
    hook();
    MEM_FENCE(); SCHED_FENCE();
    {
        bool any = false;
#pragma unroll
        for (int p = 0; p < N; ++p) any |= bad(v[p]);
        if (__builtin_expect(!__any(any), 1)) return;
    }
    u32 miss = 0;
#pragma unroll
    for (int p = 0; p < N; ++p) if (!__all(!bad(v[p]))) miss |= 1u << p;
#pragma unroll 1
    for (int spin = 0; miss != 0 && spin < SPIN_MAX; ++spin) {
        MEM_FENCE();
        __builtin_amdgcn_s_sleep(SLEEP);
#pragma unroll
        for (int p = 0; p < N; ++p) if ((miss >> p) & 1u) v[p] = ld(p);
#pragma unroll
        for (int p = 0; p < N; ++p) if (((miss >> p) & 1u) && __all(!bad(v[p]))) miss &= ~(1u << p);
        __builtin_amdgcn_s_waitcnt(0x0f70);
    }
}
struct hcx { h2 r, i; };
DEV hcx toh(cplx v) { hcx z; z.r = __builtin_convertvector(v.r, h2); z.i = __builtin_convertvector(v.i, h2); return z; }
DEV hcx hmul(hcx a, hcx b) { hcx z; z.r = __builtin_elementwise_fma(a.r, b.r, -(a.i * b.i)); z.i = __builtin_elementwise_fma(a.r, b.i, a.i * b.r); return z; }
DEV hcx hmul_conj(hcx g, hcx e) { hcx z; z.r = __builtin_elementwise_fma(g.r, e.r, g.i * e.i); z.i = __builtin_elementwise_fma(g.i, e.r, -(g.r * e.i)); return z; }
DEV cplx csq(cplx a) { cplx z; z.r = a.r * a.r - a.i * a.i; z.i = (a.r + a.r) * a.i; return z; }
template <int ROW>
DEV void stg_put(hcx v) {
    asm volatile("ds_write_addtid_b32 %0 offset:%2\n\tds_write_addtid_b32 %1 offset:%3"
                 :: "v"(v.r), "v"(v.i), "n"(ROW * STG_ROW), "n"(ROW * STG_ROW + 256) : "memory");
}
#define ST2(a, b, o) "ds_write_addtid_b32 %" #a " offset:%8+" #o "*544\n\tds_write_addtid_b32 %" #b " offset:%8+" #o "*544+256\n\t"
template <int ROW>
DEV void stg_put4(const hcx (&v)[4]) {
    static_assert(STG_ROW == 544, "row stride");
    asm volatile(ST2(0, 1, 0) ST2(2, 3, 1) ST2(4, 5, 2) ST2(6, 7, 3)
                 :: "v"(v[0].r), "v"(v[0].i), "v"(v[1].r), "v"(v[1].i), "v"(v[2].r), "v"(v[2].i), "v"(v[3].r), "v"(v[3].i), "n"(ROW * STG_ROW) : "memory");
}
template <int R0, int N, class F, class G> DEV void stg_rows(F f, G between) {
    hcx v[2][4];
#pragma unroll
    for (int i = 0; i < 4; ++i) v[0][i] = f(i);
    if constexpr (N > 1) {
#pragma unroll
        for (int i = 0; i < 4; ++i) { v[1][i] = f(4 + i); PIN(v[1][i].r); PIN(v[1][i].i); }
    }
    stg_put4<R0>(v[0]); between(0);
    if constexpr (N > 2) {
#pragma unroll
        for (int i = 0; i < 4; ++i) { v[0][i] = f(8 + i); PIN(v[0][i].r); PIN(v[0][i].i); }
    }
    if constexpr (N > 1) { stg_put4<R0 + 4>(v[1]); between(1); }
    if constexpr (N > 3) {
#pragma unroll
        for (int i = 0; i < 4; ++i) { v[1][i] = f(12 + i); PIN(v[1][i].r); PIN(v[1][i].i); }
    }
    if constexpr (N > 2) { stg_put4<R0 + 8>(v[0]); between(2); }
    if constexpr (N > 3) { stg_put4<R0 + 12>(v[1]); between(3); }
}

typedef __attribute__((address_space(3))) const char* lptr;
DEV void lds_add1a(u32 a) {
    const u32 one = 1u;
    asm volatile("s_mov_b64 exec, 1\n\tds_add_u32 %0, %1\n\ts_mov_b64 exec, -1" :: "v"(a), "v"(one) : "memory");
}
#define CONV_ASM_0 \
    "v_add_u32 %7, -64, %0\n\tds_read2_b32 v[8:9], %0 offset0:128 offset1:129\n\tds_read2_b32 v[10:11], %0 offset0:130 offset1:131\n\tds_read2_b32 v[12:13], %0 offset0:0 offset1:1\n\t" \
    "ds_read2_b32 v[14:15], %0 offset0:2 offset1:3\n\tds_read2_b32 v[16:17], %0 offset0:144 offset1:145\n\tds_read2_b32 v[18:19], %0 offset0:146 offset1:147\n\t" \
    "ds_read2_b32 v[20:21], %0 offset0:16 offset1:17\n\tds_read2_b32 v[22:23], %0 offset0:18 offset1:19\n\tds_read2_b32 v[24:25], %0 offset0:160 offset1:161\n\t" \
    "ds_read2_b32 v[26:27], %0 offset0:162 offset1:163\n\tds_read2_b32 v[28:29], %0 offset0:32 offset1:33\n\tds_read2_b32 v[30:31], %0 offset0:34 offset1:35\n\t" \
    "ds_read2_b32 v[32:33], %0 offset0:176 offset1:177\n\tds_read2_b32 v[34:35], %0 offset0:178 offset1:179\n\tds_read2_b32 v[36:37], %0 offset0:48 offset1:49\n\t" \
    "ds_read2_b32 v[38:39], %0 offset0:50 offset1:51\n\tds_read2_b32 v[40:41], %0 offset0:192 offset1:193\n\tds_read2_b32 v[42:43], %0 offset0:194 offset1:195\n\t" \
    "ds_read2_b32 v[44:45], %0 offset0:64 offset1:65\n\tds_read2_b32 v[46:47], %0 offset0:66 offset1:67\n\tds_read2_b32 v[48:49], %0 offset0:208 offset1:209\n\t" \
    "ds_read2_b32 v[50:51], %0 offset0:210 offset1:211\n\tds_read2_b32 v[52:53], %0 offset0:80 offset1:81\n\tds_read2_b32 v[54:55], %0 offset0:82 offset1:83\n\t" \
    "ds_read2_b32 v[56:57], %0 offset0:224 offset1:225\n\tds_read2_b32 v[58:59], %0 offset0:226 offset1:227\n\tds_read2_b32 v[60:61], %0 offset0:96 offset1:97\n\t" \
    "ds_read2_b32 v[62:63], %0 offset0:98 offset1:99\n\tds_read2_b32 v[64:65], %0 offset0:240 offset1:241\n\tds_read2_b32 v[66:67], %0 offset0:242 offset1:243\n\t" \
    "ds_read2_b32 v[68:69], %0 offset0:112 offset1:113\n\tds_read2_b32 v[70:71], %0 offset0:114 offset1:115\n\tds_read2_b32 v[72:73], %7 offset0:0 offset1:1\n\t" \
    "ds_read2_b32 v[74:75], %7 offset0:2 offset1:3\n\t" \
    CONV_WAIT \
    "ds_read_b128 v[76:79], %1 offset:32\n\tds_read_b128 v[80:83], %1 offset:96\n\tds_read_b128 v[84:87], %1 offset:160\n\tds_read_b128 v[88:91], %1 offset:224\n\t" \
    "s_waitcnt lgkmcnt(3)\n\tv_mfma_f32_16x16x32_f16 v[92:95], v[8:11], v[76:79], 0\n\tds_read_b128 v[96:99], %1 offset:288\n\tv_mfma_f32_16x16x32_f16 v[100:103], v[12:15], v[76:79], 0\n\t" \
    "s_waitcnt lgkmcnt(3)\n\tv_mfma_f32_16x16x32_f16 v[92:95], v[16:19], v[80:83], v[92:95]\n\tds_read_b128 v[104:107], %1 offset:352\n\tv_mfma_f32_16x16x32_f16 v[100:103], v[20:23], v[80:83], v[100:103]\n\t" \
    "s_waitcnt lgkmcnt(2)\n\tv_mfma_f32_16x16x32_f16 v[92:95], v[24:27], v[84:87], v[92:95]\n\tds_read_b128 v[108:111], %1 offset:416\n\tv_mfma_f32_16x16x32_f16 v[100:103], v[28:31], v[84:87], v[100:103]\n\t" \
    "v_mfma_f32_16x16x32_f16 v[92:95], v[32:35], v[88:91], v[92:95]\n\tds_read_b128 v[112:115], %1 offset:480\n\tv_mfma_f32_16x16x32_f16 v[100:103], v[36:39], v[88:91], v[100:103]\n\t" \
    "s_waitcnt lgkmcnt(2)\n\tv_mfma_f32_16x16x32_f16 v[92:95], v[40:43], v[96:99], v[92:95]\n\tds_read_b128 v[116:119], %1 offset:544\n\tv_mfma_f32_16x16x32_f16 v[100:103], v[44:47], v[96:99], v[100:103]\n\t" \
    "ds_read_b128 v[120:123], %1 offset:608\n\tv_mfma_f32_16x16x32_f16 v[92:95], v[48:51], v[104:107], v[92:95]\n\tds_read_b128 v[124:127], %1 offset:2528\n\t" \
    "v_mfma_f32_16x16x32_f16 v[100:103], v[52:55], v[104:107], v[100:103]\n\tds_read_b128 v[76:79], %1 offset:672\n\ts_waitcnt lgkmcnt(4)\n\tv_mfma_f32_16x16x32_f16 v[92:95], v[56:59], v[108:111], v[92:95]\n\t" \
    "ds_read_b128 v[80:83], %1 offset:2592\n\tv_mfma_f32_16x16x32_f16 v[100:103], v[60:63], v[108:111], v[100:103]\n\tds_read_b128 v[84:87], %1 offset:736\n\t" \
    "v_mfma_f32_16x16x32_f16 v[92:95], v[64:67], v[112:115], v[92:95]\n\tds_read_b128 v[88:91], %1 offset:2656\n\tv_mfma_f32_16x16x32_f16 v[100:103], v[68:71], v[112:115], v[100:103]\n\t" \
    "ds_read_b128 v[96:99], %1 offset:800\n\tds_read_b128 v[104:107], %1 offset:2784\n\ts_waitcnt lgkmcnt(5)\n\tv_mfma_f32_16x16x32_f16 v[100:103], v[8:11], v[116:119], v[100:103]\n\t" \
    "ds_read_b128 v[108:111], %1 offset:2720\n\tds_read_b128 v[112:115], %1 offset:864\n\tv_mfma_f32_16x16x32_f16 v[100:103], v[16:19], v[120:123], v[100:103]\n\t" \
    "v_mul_f32 v92, %8, v92\n\tv_mul_f32 v93, %8, v93\n\tv_mfma_f32_16x16x32_f16 v[116:119], v[72:75], v[124:127], 0\n\tv_mul_f32 v94, %8, v94\n\tv_mul_f32 v95, %8, v95\n\t" \
    "v_mfma_f32_16x16x32_f16 v[100:103], v[24:27], v[76:79], v[100:103]\n\tds_write_b128 %2, v[92:95] offset:0\n\tds_write_b8 %6, %6 offset:0\n\t" \
    CONV_P0 \
    "ds_read_b128 v[120:123], %1 offset:928\n\ts_waitcnt lgkmcnt(6)\n\tv_mfma_f32_16x16x32_f16 v[116:119], v[12:15], v[80:83], v[116:119]\n\tds_read_b128 v[124:127], %1 offset:2848\n\t" \
    "v_mfma_f32_16x16x32_f16 v[100:103], v[32:35], v[84:87], v[100:103]\n\tds_read_b128 v[76:79], %1 offset:992\n\tv_mfma_f32_16x16x32_f16 v[116:119], v[20:23], v[88:91], v[116:119]\n\t" \
    "ds_read_b128 v[80:83], %1 offset:2912\n\tv_mfma_f32_16x16x32_f16 v[100:103], v[40:43], v[96:99], v[100:103]\n\tds_read_b128 v[92:95], %1 offset:2976\n\t" \
    "s_waitcnt lgkmcnt(4)\n\tv_mfma_f32_16x16x32_f16 v[116:119], v[28:31], v[108:111], v[116:119]\n\tds_read_b128 v[84:87], %1 offset:3040\n\tv_mfma_f32_16x16x32_f16 v[100:103], v[48:51], v[112:115], v[100:103]\n\t" \
    "v_mfma_f32_16x16x32_f16 v[116:119], v[36:39], v[104:107], v[116:119]\n\tds_read_b128 v[88:91], %1 offset:3104\n\tv_mfma_f32_16x16x32_f16 v[100:103], v[56:59], v[120:123], v[100:103]\n\t" \
    "s_waitcnt lgkmcnt(2)\n\tv_mfma_f32_16x16x32_f16 v[116:119], v[44:47], v[124:127], v[116:119]\n\tds_read_b128 v[96:99], %1 offset:3168\n\tv_mfma_f32_16x16x32_f16 v[100:103], v[64:67], v[76:79], v[100:103]\n\t" \
    "v_mfma_f32_16x16x32_f16 v[116:119], v[52:55], v[80:83], v[116:119]\n\tds_read_b128 v[108:111], %1 offset:3232\n\tv_mfma_f32_16x16x32_f16 v[116:119], v[60:63], v[92:95], v[116:119]\n\t" \
    "ds_read_b128 v[104:107], %1 offset:3296\n\tds_read_b128 v[112:115], %1 offset:3360\n\ts_waitcnt lgkmcnt(4)\n\tv_mfma_f32_16x16x32_f16 v[116:119], v[68:71], v[84:87], v[116:119]\n\t" \
    "s_nop 0\n\tv_mul_f32 v100, %8, v100\n\tv_mul_f32 v101, %8, v101\n\tv_mfma_f32_16x16x32_f16 v[120:123], v[72:75], v[84:87], 0\n\tv_mul_f32 v102, %8, v102\n\t" \
    "v_mul_f32 v103, %8, v103\n\tv_mfma_f32_16x16x32_f16 v[116:119], v[8:11], v[88:91], v[116:119]\n\tds_write_b128 %2, v[100:103] offset:1024\n\tds_write_b8 %6, %6 offset:8\n\t" \
    CONV_P1 \
    "v_mfma_f32_16x16x32_f16 v[120:123], v[12:15], v[88:91], v[120:123]\n\ts_waitcnt lgkmcnt(4)\n\tv_mfma_f32_16x16x32_f16 v[116:119], v[16:19], v[96:99], v[116:119]\n\t" \
    "ds_read_b128 v[124:127], %1 offset:3424\n\tv_mfma_f32_16x16x32_f16 v[120:123], v[20:23], v[96:99], v[120:123]\n\tv_mfma_f32_16x16x32_f16 v[116:119], v[24:27], v[108:111], v[116:119]\n\t" \
    "ds_read_b128 v[76:79], %1 offset:3488\n\tv_mfma_f32_16x16x32_f16 v[120:123], v[28:31], v[108:111], v[120:123]\n\ts_waitcnt lgkmcnt(4)\n\tv_mfma_f32_16x16x32_f16 v[116:119], v[32:35], v[104:107], v[116:119]\n\t" \
    "ds_read_b128 v[80:83], %1 offset:3552\n\tv_mfma_f32_16x16x32_f16 v[120:123], v[36:39], v[104:107], v[120:123]\n\tv_mfma_f32_16x16x32_f16 v[116:119], v[40:43], v[112:115], v[116:119]\n\t" \
    "ds_read_b128 v[92:95], %1 offset:3616\n\tv_mfma_f32_16x16x32_f16 v[120:123], v[44:47], v[112:115], v[120:123]\n\tds_read_b128 v[12:15], %1 offset:3680\n\t" \
    "s_waitcnt lgkmcnt(3)\n\tv_mfma_f32_16x16x32_f16 v[116:119], v[48:51], v[124:127], v[116:119]\n\tds_read_b128 v[72:75], %1 offset:3744\n\tv_mfma_f32_16x16x32_f16 v[120:123], v[52:55], v[124:127], v[120:123]\n\t" \
    "ds_read_b128 v[100:103], %1 offset:3808\n\tv_mfma_f32_16x16x32_f16 v[116:119], v[56:59], v[76:79], v[116:119]\n\tds_read_b128 v[84:87], %1 offset:464\n\t" \
    "ds_read_b128 v[88:91], %3 offset:1024\n\tv_mfma_f32_16x16x32_f16 v[120:123], v[60:63], v[76:79], v[120:123]\n\tds_read_b128 v[20:23], %1 offset:480\n\t" \
    "ds_read_b128 v[96:99], %3 offset:4096\n\ts_waitcnt lgkmcnt(6)\n\tv_mfma_f32_16x16x32_f16 v[116:119], v[64:67], v[80:83], v[116:119]\n\tds_read_b128 v[28:31], %1 offset:3872\n\t" \
    "v_mfma_f32_16x16x32_f16 v[120:123], v[68:71], v[80:83], v[120:123]\n\tds_read_b128 v[108:111], %1 offset:976\n\tv_mfma_f32_16x16x32_f16 v[120:123], v[8:11], v[92:95], v[120:123]\n\t" \
    "ds_read_b128 v[36:39], %1 offset:992\n\tv_mfma_f32_16x16x32_f16 v[120:123], v[16:19], v[12:15], v[120:123]\n\tds_read_b128 v[104:107], %1 offset:3936\n\t" \
    "s_waitcnt lgkmcnt(4)\n\tv_mfma_f32_16x16x32_f16 v[120:123], v[24:27], v[72:75], v[120:123]\n\tds_read_b128 v[44:47], %1 offset:1488\n\tv_mfma_f32_16x16x32_f16 v[120:123], v[32:35], v[100:103], v[120:123]\n\t" \
    "ds_read_b128 v[112:115], %1 offset:1504\n\tv_mfma_f32_16x16x32_f16 v[52:55], v[84:87], v[88:91], 0\n\tds_read_b128 v[124:127], %1 offset:4000\n\tv_mfma_f32_16x16x32_f16 v[60:63], v[20:23], v[96:99], 0\n\t" \
    "ds_read_b128 v[76:79], %3 offset:0\n\ts_waitcnt lgkmcnt(4)\n\tv_mfma_f32_16x16x32_f16 v[120:123], v[40:43], v[28:31], v[120:123]\n\tds_read_b128 v[68:71], %3 offset:2048\n\t" \
    "v_mfma_f32_16x16x32_f16 v[80:83], v[108:111], v[88:91], 0\n\tds_read_b128 v[8:11], %3 offset:3072\n\tds_read_b128 v[92:95], %1 offset:4064\n\tv_mfma_f32_16x16x32_f16 v[16:19], v[36:39], v[96:99], 0\n\t" \
    "v_cvt_pk_f16_f32 v12, v52, v60\n\tv_cvt_pk_f16_f32 v13, v53, v61\n\tv_mfma_f32_16x16x32_f16 v[120:123], v[48:51], v[104:107], v[120:123]\n\tv_cvt_pk_f16_f32 v14, v54, v62\n\t" \
    "v_cvt_pk_f16_f32 v15, v55, v63\n\ts_waitcnt lgkmcnt(3)\n\tv_mfma_f32_16x16x32_f16 v[24:27], v[44:47], v[88:91], 0\n\tds_read2_b32 v[72:73], %5 offset0:16 offset1:17\n\t" \
    "ds_read2_b32 v[74:75], %5 offset0:18 offset1:19\n\tv_mfma_f32_16x16x32_f16 v[32:35], v[112:115], v[96:99], 0\n\tv_cvt_pk_f16_f32 v100, v80, v16\n\tv_cvt_pk_f16_f32 v101, v81, v17\n\t" \
    "ds_read2_b32 v[84:85], %5 offset0:0 offset1:1\n\tds_read2_b32 v[86:87], %5 offset0:2 offset1:3\n\tv_mfma_f32_16x16x32_f16 v[120:123], v[56:59], v[124:127], v[120:123]\n\t" \
    "v_cvt_pk_f16_f32 v102, v82, v18\n\tv_cvt_pk_f16_f32 v103, v83, v19\n\tv_add_u32 %7, 1880, %0\n\tds_read2_b32 v[20:21], %7 offset0:0 offset1:1\n\tds_read2_b32 v[22:23], %7 offset0:2 offset1:3\n\t" \
    "v_mfma_f32_16x16x32_f16 v[40:43], v[44:47], v[76:79], 0\n\tds_read_b128 v[28:31], %1 offset:2000\n\tds_read_b128 v[108:111], %1 offset:2016\n\ts_waitcnt lgkmcnt(8)\n\t" \
    "v_mfma_f32_16x16x32_f16 v[36:39], v[112:115], v[68:71], 0\n\tv_cvt_pk_f16_f32 v48, v24, v32\n\tv_cvt_pk_f16_f32 v49, v25, v33\n\tv_mfma_f32_16x16x32_f16 v[104:107], v[112:115], v[76:79], 0\n\t" \
    "v_cvt_pk_f16_f32 v50, v26, v34\n\tv_cvt_pk_f16_f32 v51, v27, v35\n\tv_mfma_f32_16x16x32_f16 v[52:55], v[112:115], v[8:11], 0\n\tds_read2_b32 v[60:61], %5 offset0:32 offset1:33\n\t" \
    "ds_read2_b32 v[62:63], %5 offset0:34 offset1:35\n\tv_mfma_f32_16x16x32_f16 v[120:123], v[64:67], v[92:95], v[120:123]\n\tv_cvt_pk_f16_f32 v56, v40, v36\n\t" \
    "v_cvt_pk_f16_f32 v58, v41, v37\n\ts_waitcnt lgkmcnt(3)\n\tv_mfma_f32_16x16x32_f16 v[116:119], v[12:15], v[72:75], v[116:119]\n\tv_cvt_pk_f16_f32 v124, v42, v38\n\t" \
    "v_cvt_pk_f16_f32 v126, v43, v39\n\tv_mfma_f32_16x16x32_f16 v[120:123], v[12:15], v[84:87], v[120:123]\n\tv_cvt_pk_f16_f32 v57, v104, v52\n\tv_cvt_pk_f16_f32 v59, v105, v53\n\t" \
    "ds_read2_b32 v[80:81], %5 offset0:48 offset1:49\n\tds_read2_b32 v[82:83], %5 offset0:50 offset1:51\n\tv_mfma_f32_16x16x32_f16 v[116:119], v[20:23], v[112:115], v[116:119]\n\t" \
    "v_cvt_pk_f16_f32 v125, v106, v54\n\tv_cvt_pk_f16_f32 v127, v107, v55\n\tv_mfma_f32_16x16x32_f16 v[16:19], v[28:31], v[76:79], 0\n\tds_read_b128 v[44:47], %1 offset:2512\n\t" \
    "s_waitcnt lgkmcnt(3)\n\tv_mfma_f32_16x16x32_f16 v[24:27], v[108:111], v[68:71], 0\n\tds_read_b128 v[32:35], %1 offset:2528\n\tv_mfma_f32_16x16x32_f16 v[64:67], v[108:111], v[76:79], 0\n\t" \
    "v_mfma_f32_16x16x32_f16 v[92:95], v[108:111], v[8:11], 0\n\tds_read2_b64 v[84:87], %4 offset0:0 offset1:1\n\tds_read2_b64 v[40:43], %4 offset0:2 offset1:3\n\t" \
    "v_mfma_f32_16x16x32_f16 v[116:119], v[100:103], v[60:63], v[116:119]\n\ts_nop 1\n\tv_cvt_pk_f16_f32 v36, v16, v24\n\tv_cvt_pk_f16_f32 v38, v17, v25\n\t" \
    "v_mfma_f32_16x16x32_f16 v[120:123], v[100:103], v[72:75], v[120:123]\n\tv_cvt_pk_f16_f32 v112, v18, v26\n\tv_cvt_pk_f16_f32 v114, v19, v27\n\ts_waitcnt lgkmcnt(2)\n\t" \
    "v_mfma_f32_16x16x32_f16 v[116:119], v[48:51], v[80:83], v[116:119]\n\tv_cvt_pk_f16_f32 v37, v64, v92\n\tv_cvt_pk_f16_f32 v39, v65, v93\n\tv_mfma_f32_16x16x32_f16 v[120:123], v[48:51], v[60:63], v[120:123]\n\t" \
    "v_cvt_pk_f16_f32 v113, v66, v94\n\tv_cvt_pk_f16_f32 v115, v67, v95\n\tv_mfma_f32_16x16x32_f16 v[12:15], v[44:47], v[76:79], 0\n\tv_mfma_f32_16x16x32_f16 v[104:107], v[32:35], v[68:71], 0\n\t" \
    "ds_read2_b64 v[52:55], %4 offset0:16 offset1:17\n\tv_mfma_f32_16x16x32_f16 v[72:75], v[32:35], v[76:79], 0\n\tds_read2_b64 v[100:103], %4 offset0:18 offset1:19\n\t" \
    "v_mfma_f32_16x16x32_f16 v[60:63], v[32:35], v[8:11], 0\n\tds_read_b128 v[16:19], %1 offset:3024\n\tds_read_b128 v[24:27], %1 offset:3040\n\ts_waitcnt lgkmcnt(4)\n\t" \
    "v_mfma_f32_16x16x32_f16 v[116:119], v[56:59], v[84:87], v[116:119]\n\tv_cvt_pk_f16_f32 v64, v12, v104\n\tv_cvt_pk_f16_f32 v66, v13, v105\n\tv_mfma_f32_16x16x32_f16 v[116:119], v[124:127], v[40:43], v[116:119]\n\t" \
    "v_cvt_pk_f16_f32 v92, v14, v106\n\tv_cvt_pk_f16_f32 v94, v15, v107\n\tv_mfma_f32_16x16x32_f16 v[48:51], v[28:31], v[88:91], 0\n\tv_cvt_pk_f16_f32 v65, v72, v60\n\t" \
    "v_cvt_pk_f16_f32 v67, v73, v61\n\tv_mfma_f32_16x16x32_f16 v[44:47], v[108:111], v[96:99], 0\n\tv_cvt_pk_f16_f32 v93, v74, v62\n\tv_cvt_pk_f16_f32 v95, v75, v63\n\t" \
    "ds_read2_b64 v[32:35], %4 offset0:32 offset1:33\n\tv_mfma_f32_16x16x32_f16 v[120:123], v[20:23], v[108:111], v[120:123]\n\tds_read2_b64 v[56:59], %4 offset0:34 offset1:35\n\t" \
    "s_waitcnt lgkmcnt(2)\n\tv_mfma_f32_16x16x32_f16 v[116:119], v[36:39], v[52:55], v[116:119]\n\tv_mfma_f32_16x16x32_f16 v[116:119], v[112:115], v[100:103], v[116:119]\n\t" \
    "v_cvt_pk_f16_f32 v12, v48, v44\n\tv_cvt_pk_f16_f32 v13, v49, v45\n\tv_mfma_f32_16x16x32_f16 v[104:107], v[16:19], v[76:79], 0\n\tv_cvt_pk_f16_f32 v14, v50, v46\n\t" \
    "v_cvt_pk_f16_f32 v15, v51, v47\n\tv_mfma_f32_16x16x32_f16 v[88:91], v[24:27], v[68:71], 0\n\tv_mfma_f32_16x16x32_f16 v[28:31], v[24:27], v[76:79], 0\n\t" \
    "v_mfma_f32_16x16x32_f16 v[124:127], v[24:27], v[8:11], 0\n\ts_waitcnt lgkmcnt(0)\n\tv_mfma_f32_16x16x32_f16 v[116:119], v[64:67], v[32:35], v[116:119]\n\t" \
    "s_nop 3\n\tv_cvt_pk_f16_f32 v96, v104, v88\n\tv_cvt_pk_f16_f32 v98, v105, v89\n\tv_mfma_f32_16x16x32_f16 v[116:119], v[92:95], v[56:59], v[116:119]\n\t" \
    "v_cvt_pk_f16_f32 v20, v106, v90\n\tv_cvt_pk_f16_f32 v22, v107, v91\n\tv_mfma_f32_16x16x32_f16 v[120:123], v[36:39], v[84:87], v[120:123]\n\tv_cvt_pk_f16_f32 v97, v28, v124\n\t" \
    "v_cvt_pk_f16_f32 v99, v29, v125\n\tv_mfma_f32_16x16x32_f16 v[120:123], v[112:115], v[40:43], v[120:123]\n\tv_cvt_pk_f16_f32 v21, v30, v126\n\tv_cvt_pk_f16_f32 v23, v31, v127\n\t" \
    "v_mfma_f32_16x16x32_f16 v[120:123], v[12:15], v[80:83], v[120:123]\n\tv_mul_f32 v116, %8, v116\n\tv_mul_f32 v117, %8, v117\n\tv_mfma_f32_16x16x32_f16 v[120:123], v[64:67], v[52:55], v[120:123]\n\t" \
    "v_mul_f32 v118, %8, v118\n\tv_mul_f32 v119, %8, v119\n\tv_mfma_f32_16x16x32_f16 v[120:123], v[92:95], v[100:103], v[120:123]\n\tds_write_b128 %2, v[116:119] offset:2048\n\t" \
    "ds_write_b8 %6, %6 offset:16\n\t" \
    CONV_P2 \
    "v_mfma_f32_16x16x32_f16 v[120:123], v[96:99], v[32:35], v[120:123]\n\tv_mfma_f32_16x16x32_f16 v[120:123], v[20:23], v[56:59], v[120:123]\n\ts_nop 7\n\t" \
    "v_mul_f32 v120, %8, v120\n\tv_mul_f32 v121, %8, v121\n\tv_mul_f32 v122, %8, v122\n\tv_mul_f32 v123, %8, v123\n\tds_write_b128 %2, v[120:123] offset:3072\n\t" \
    "ds_write_b8 %6, %6 offset:24\n\t"
#define CONV_ASM_1 \
    "v_add_u32 %7, -64, %0\n\tds_read_b128 v[8:11], %3 offset:0\n\tds_read_b128 v[12:15], %3 offset:2048\n\tds_read_b128 v[16:19], %3 offset:3072\n\tds_read2_b64 v[24:27], %4 offset0:32 offset1:33\n\t" \
    "ds_read2_b64 v[36:39], %4 offset0:34 offset1:35\n\tds_read2_b64 v[28:31], %4 offset0:16 offset1:17\n\tds_read2_b64 v[40:43], %4 offset0:18 offset1:19\n\t" \
    "ds_read2_b64 v[32:35], %4 offset0:0 offset1:1\n\tds_read2_b64 v[44:47], %4 offset0:2 offset1:3\n\tds_read2_b32 v[20:21], %7 offset0:0 offset1:1\n\tds_read2_b32 v[22:23], %7 offset0:2 offset1:3\n\t" \
    "ds_read2_b32 v[48:49], %0 offset0:0 offset1:1\n\tds_read2_b32 v[50:51], %0 offset0:2 offset1:3\n\tds_read2_b32 v[52:53], %0 offset0:16 offset1:17\n\tds_read2_b32 v[54:55], %0 offset0:18 offset1:19\n\t" \
    "ds_read2_b32 v[56:57], %0 offset0:32 offset1:33\n\tds_read2_b32 v[58:59], %0 offset0:34 offset1:35\n\tds_read2_b32 v[60:61], %0 offset0:48 offset1:49\n\t" \
    "ds_read2_b32 v[62:63], %0 offset0:50 offset1:51\n\tds_read2_b32 v[64:65], %0 offset0:64 offset1:65\n\tds_read2_b32 v[66:67], %0 offset0:66 offset1:67\n\t" \
    "ds_read2_b32 v[68:69], %0 offset0:80 offset1:81\n\tds_read2_b32 v[70:71], %0 offset0:82 offset1:83\n\tds_read2_b32 v[72:73], %0 offset0:96 offset1:97\n\t" \
    "ds_read2_b32 v[74:75], %0 offset0:98 offset1:99\n\tds_read2_b32 v[76:77], %0 offset0:112 offset1:113\n\tds_read2_b32 v[78:79], %0 offset0:114 offset1:115\n\t" \
    "ds_read2_b32 v[80:81], %0 offset0:128 offset1:129\n\tds_read2_b32 v[82:83], %0 offset0:130 offset1:131\n\tds_read2_b32 v[84:85], %0 offset0:144 offset1:145\n\t" \
    "ds_read2_b32 v[86:87], %0 offset0:146 offset1:147\n\tds_read2_b32 v[88:89], %0 offset0:160 offset1:161\n\tds_read2_b32 v[90:91], %0 offset0:162 offset1:163\n\t" \
    CONV_WAIT \
    "ds_read_b128 v[92:95], %1 offset:464\n\tds_read_b128 v[96:99], %1 offset:480\n\tds_read_b128 v[100:103], %1 offset:976\n\tds_read_b128 v[104:107], %1 offset:992\n\t" \
    "s_waitcnt lgkmcnt(3)\n\tv_mfma_f32_16x16x32_f16 v[108:111], v[92:95], v[8:11], 0\n\ts_waitcnt lgkmcnt(2)\n\tv_mfma_f32_16x16x32_f16 v[112:115], v[96:99], v[12:15], 0\n\t" \
    "v_mfma_f32_16x16x32_f16 v[116:119], v[96:99], v[8:11], 0\n\tv_mfma_f32_16x16x32_f16 v[120:123], v[96:99], v[16:19], 0\n\ts_waitcnt lgkmcnt(0)\n\tv_mfma_f32_16x16x32_f16 v[124:127], v[100:103], v[8:11], 0\n\t" \
    "s_nop 3\n\tv_cvt_pk_f16_f32 v92, v108, v112\n\tv_cvt_pk_f16_f32 v94, v109, v113\n\tv_mfma_f32_16x16x32_f16 v[100:103], v[104:107], v[12:15], 0\n\tv_cvt_pk_f16_f32 v12, v110, v114\n\t" \
    "v_cvt_pk_f16_f32 v14, v111, v115\n\tv_mfma_f32_16x16x32_f16 v[108:111], v[104:107], v[8:11], 0\n\tv_cvt_pk_f16_f32 v93, v116, v120\n\tv_cvt_pk_f16_f32 v95, v117, v121\n\t" \
    "v_mfma_f32_16x16x32_f16 v[112:115], v[104:107], v[16:19], 0\n\tv_cvt_pk_f16_f32 v13, v118, v122\n\tv_cvt_pk_f16_f32 v15, v119, v123\n\tv_mfma_f32_16x16x32_f16 v[8:11], v[20:23], v[96:99], 0\n\t" \
    "v_cvt_pk_f16_f32 v16, v124, v100\n\tv_cvt_pk_f16_f32 v18, v125, v101\n\tv_mfma_f32_16x16x32_f16 v[116:119], v[20:23], v[104:107], 0\n\tv_cvt_pk_f16_f32 v120, v126, v102\n\t" \
    "v_cvt_pk_f16_f32 v122, v127, v103\n\tv_mfma_f32_16x16x32_f16 v[8:11], v[92:95], v[24:27], v[8:11]\n\tv_cvt_pk_f16_f32 v17, v108, v112\n\tv_cvt_pk_f16_f32 v19, v109, v113\n\t" \
    "v_mfma_f32_16x16x32_f16 v[116:119], v[92:95], v[28:31], v[116:119]\n\tv_cvt_pk_f16_f32 v121, v110, v114\n\tv_cvt_pk_f16_f32 v123, v111, v115\n\tv_mfma_f32_16x16x32_f16 v[96:99], v[92:95], v[32:35], 0\n\t" \
    "v_mfma_f32_16x16x32_f16 v[8:11], v[12:15], v[36:39], v[8:11]\n\tv_mfma_f32_16x16x32_f16 v[116:119], v[12:15], v[40:43], v[116:119]\n\tds_read_b128 v[104:107], %1 offset:544\n\t" \
    "v_mfma_f32_16x16x32_f16 v[96:99], v[12:15], v[44:47], v[96:99]\n\tds_read_b128 v[124:127], %1 offset:608\n\tv_mfma_f32_16x16x32_f16 v[116:119], v[16:19], v[24:27], v[116:119]\n\t" \
    "ds_read_b128 v[100:103], %1 offset:672\n\tv_mfma_f32_16x16x32_f16 v[96:99], v[16:19], v[28:31], v[96:99]\n\tds_read_b128 v[108:111], %1 offset:736\n\tv_mfma_f32_16x16x32_f16 v[112:115], v[16:19], v[32:35], 0\n\t" \
    "ds_read_b128 v[92:95], %1 offset:800\n\tv_mfma_f32_16x16x32_f16 v[116:119], v[120:123], v[36:39], v[116:119]\n\tds_read_b128 v[24:27], %1 offset:864\n\t" \
    "v_mfma_f32_16x16x32_f16 v[96:99], v[120:123], v[40:43], v[96:99]\n\tds_read_b128 v[12:15], %1 offset:928\n\tv_mfma_f32_16x16x32_f16 v[112:115], v[120:123], v[44:47], v[112:115]\n\t" \
    "ds_read_b128 v[28:31], %1 offset:992\n\ts_waitcnt lgkmcnt(4)\n\tv_mfma_f32_16x16x32_f16 v[8:11], v[48:51], v[104:107], v[8:11]\n\tds_read_b128 v[32:35], %1 offset:1056\n\t" \
    "v_mfma_f32_16x16x32_f16 v[8:11], v[52:55], v[124:127], v[8:11]\n\tv_mfma_f32_16x16x32_f16 v[8:11], v[56:59], v[100:103], v[8:11]\n\tds_read_b128 v[36:39], %1 offset:1120\n\t" \
    "v_mfma_f32_16x16x32_f16 v[8:11], v[60:63], v[108:111], v[8:11]\n\ts_waitcnt lgkmcnt(2)\n\tv_mfma_f32_16x16x32_f16 v[8:11], v[64:67], v[92:95], v[8:11]\n\t" \
    "ds_read_b128 v[16:19], %1 offset:1184\n\tv_mfma_f32_16x16x32_f16 v[8:11], v[68:71], v[24:27], v[8:11]\n\tds_read2_b32 v[40:41], %0 offset0:176 offset1:177\n\t" \
    "ds_read2_b32 v[42:43], %0 offset0:178 offset1:179\n\tv_mfma_f32_16x16x32_f16 v[8:11], v[72:75], v[12:15], v[8:11]\n\tds_read_b128 v[44:47], %1 offset:1248\n\t" \
    "v_mfma_f32_16x16x32_f16 v[8:11], v[76:79], v[28:31], v[8:11]\n\tds_read2_b32 v[120:121], %0 offset0:192 offset1:193\n\tds_read2_b32 v[122:123], %0 offset0:194 offset1:195\n\t" \
    "s_waitcnt lgkmcnt(6)\n\tv_mfma_f32_16x16x32_f16 v[8:11], v[80:83], v[32:35], v[8:11]\n\tds_read_b128 v[104:107], %1 offset:1312\n\tv_mfma_f32_16x16x32_f16 v[116:119], v[48:51], v[32:35], v[116:119]\n\t" \
    "ds_read2_b32 v[124:125], %0 offset0:208 offset1:209\n\tds_read2_b32 v[126:127], %0 offset0:210 offset1:211\n\tv_mfma_f32_16x16x32_f16 v[8:11], v[84:87], v[36:39], v[8:11]\n\t" \
    "ds_read_b128 v[100:103], %1 offset:1376\n\tv_mfma_f32_16x16x32_f16 v[116:119], v[52:55], v[36:39], v[116:119]\n\tds_read2_b32 v[108:109], %0 offset0:224 offset1:225\n\t" \
    "ds_read2_b32 v[110:111], %0 offset0:226 offset1:227\n\ts_waitcnt lgkmcnt(8)\n\tv_mfma_f32_16x16x32_f16 v[8:11], v[88:91], v[16:19], v[8:11]\n\tds_read_b128 v[92:95], %1 offset:1440\n\t" \
    "v_mfma_f32_16x16x32_f16 v[116:119], v[56:59], v[16:19], v[116:119]\n\tds_read2_b32 v[24:25], %0 offset0:240 offset1:241\n\tds_read2_b32 v[26:27], %0 offset0:242 offset1:243\n\t" \
    "v_mfma_f32_16x16x32_f16 v[8:11], v[40:43], v[44:47], v[8:11]\n\tds_read_b128 v[12:15], %1 offset:1504\n\tv_mfma_f32_16x16x32_f16 v[116:119], v[60:63], v[44:47], v[116:119]\n\t" \
    "s_waitcnt lgkmcnt(6)\n\tv_mfma_f32_16x16x32_f16 v[8:11], v[120:123], v[104:107], v[8:11]\n\tv_mfma_f32_16x16x32_f16 v[116:119], v[64:67], v[104:107], v[116:119]\n\t" \
    "ds_read_b128 v[28:31], %1 offset:1568\n\tv_mfma_f32_16x16x32_f16 v[8:11], v[124:127], v[100:103], v[8:11]\n\tv_mfma_f32_16x16x32_f16 v[116:119], v[68:71], v[100:103], v[116:119]\n\t" \
    "ds_read_b128 v[32:35], %1 offset:1632\n\ts_waitcnt lgkmcnt(2)\n\tv_mfma_f32_16x16x32_f16 v[8:11], v[108:111], v[92:95], v[8:11]\n\tv_mfma_f32_16x16x32_f16 v[116:119], v[72:75], v[92:95], v[116:119]\n\t" \
    "ds_read_b128 v[36:39], %1 offset:1696\n\tv_mfma_f32_16x16x32_f16 v[8:11], v[24:27], v[12:15], v[8:11]\n\tv_mfma_f32_16x16x32_f16 v[116:119], v[76:79], v[12:15], v[116:119]\n\t" \
    "ds_read_b128 v[16:19], %1 offset:1760\n\tv_mfma_f32_16x16x32_f16 v[96:99], v[20:23], v[12:15], v[96:99]\n\tds_read_b128 v[44:47], %1 offset:1824\n\tds_read_b128 v[104:107], %1 offset:1888\n\t" \
    "s_waitcnt lgkmcnt(4)\n\tv_mfma_f32_16x16x32_f16 v[116:119], v[80:83], v[28:31], v[116:119]\n\ts_nop 0\n\tv_mul_f32 v8, %8, v8\n\tv_mul_f32 v9, %8, v9\n\t" \
    "v_mfma_f32_16x16x32_f16 v[96:99], v[48:51], v[28:31], v[96:99]\n\tv_mul_f32 v10, %8, v10\n\tv_mul_f32 v11, %8, v11\n\tv_mfma_f32_16x16x32_f16 v[116:119], v[84:87], v[32:35], v[116:119]\n\t" \
    "ds_write_b128 %2, v[8:11] offset:0\n\tds_write_b8 %6, %6 offset:0\n\t" \
    CONV_P0 \
    "v_mfma_f32_16x16x32_f16 v[96:99], v[52:55], v[32:35], v[96:99]\n\ts_waitcnt lgkmcnt(4)\n\tv_mfma_f32_16x16x32_f16 v[116:119], v[88:91], v[36:39], v[116:119]\n\t" \
    "ds_read_b128 v[100:103], %1 offset:1952\n\tv_mfma_f32_16x16x32_f16 v[96:99], v[56:59], v[36:39], v[96:99]\n\tv_mfma_f32_16x16x32_f16 v[116:119], v[40:43], v[16:19], v[116:119]\n\t" \
    "ds_read_b128 v[92:95], %1 offset:2016\n\tv_mfma_f32_16x16x32_f16 v[96:99], v[60:63], v[16:19], v[96:99]\n\ts_waitcnt lgkmcnt(4)\n\tv_mfma_f32_16x16x32_f16 v[116:119], v[120:123], v[44:47], v[116:119]\n\t" \
    "v_mfma_f32_16x16x32_f16 v[96:99], v[64:67], v[44:47], v[96:99]\n\tds_read_b128 v[12:15], %1 offset:2080\n\tv_mfma_f32_16x16x32_f16 v[116:119], v[124:127], v[104:107], v[116:119]\n\t" \
    "v_mfma_f32_16x16x32_f16 v[96:99], v[68:71], v[104:107], v[96:99]\n\tds_read_b128 v[8:11], %1 offset:2144\n\ts_waitcnt lgkmcnt(2)\n\tv_mfma_f32_16x16x32_f16 v[116:119], v[108:111], v[100:103], v[116:119]\n\t" \
    "v_mfma_f32_16x16x32_f16 v[96:99], v[72:75], v[100:103], v[96:99]\n\tds_read_b128 v[28:31], %1 offset:2208\n\tv_mfma_f32_16x16x32_f16 v[116:119], v[24:27], v[92:95], v[116:119]\n\t" \
    "v_mfma_f32_16x16x32_f16 v[96:99], v[76:79], v[92:95], v[96:99]\n\tds_read_b128 v[32:35], %1 offset:2272\n\tv_mfma_f32_16x16x32_f16 v[112:115], v[20:23], v[92:95], v[112:115]\n\t" \
    "ds_read_b128 v[36:39], %1 offset:2336\n\tds_read_b128 v[16:19], %1 offset:2400\n\ts_waitcnt lgkmcnt(4)\n\tv_mfma_f32_16x16x32_f16 v[96:99], v[80:83], v[12:15], v[96:99]\n\t" \
    "s_nop 0\n\tv_mul_f32 v116, %8, v116\n\tv_mul_f32 v117, %8, v117\n\tv_mfma_f32_16x16x32_f16 v[112:115], v[48:51], v[12:15], v[112:115]\n\tv_mul_f32 v118, %8, v118\n\t" \
    "v_mul_f32 v119, %8, v119\n\tv_mfma_f32_16x16x32_f16 v[96:99], v[84:87], v[8:11], v[96:99]\n\tds_write_b128 %2, v[116:119] offset:1024\n\tds_write_b8 %6, %6 offset:8\n\t" \
    CONV_P1 \
    "v_mfma_f32_16x16x32_f16 v[112:115], v[52:55], v[8:11], v[112:115]\n\ts_waitcnt lgkmcnt(4)\n\tv_mfma_f32_16x16x32_f16 v[96:99], v[88:91], v[28:31], v[96:99]\n\t" \
    "ds_read_b128 v[44:47], %1 offset:2464\n\tv_mfma_f32_16x16x32_f16 v[112:115], v[56:59], v[28:31], v[112:115]\n\tv_mfma_f32_16x16x32_f16 v[96:99], v[40:43], v[32:35], v[96:99]\n\t" \
    "ds_read_b128 v[104:107], %1 offset:2528\n\tv_mfma_f32_16x16x32_f16 v[112:115], v[60:63], v[32:35], v[112:115]\n\ts_waitcnt lgkmcnt(4)\n\tv_mfma_f32_16x16x32_f16 v[96:99], v[120:123], v[36:39], v[96:99]\n\t" \
    "ds_read_b128 v[100:103], %1 offset:2592\n\tv_mfma_f32_16x16x32_f16 v[112:115], v[64:67], v[36:39], v[112:115]\n\tds_read_b128 v[20:23], %1 offset:2656\n\t" \
    "v_mfma_f32_16x16x32_f16 v[96:99], v[124:127], v[16:19], v[96:99]\n\tds_read_b128 v[92:95], %1 offset:2720\n\tv_mfma_f32_16x16x32_f16 v[112:115], v[68:71], v[16:19], v[112:115]\n\t" \
    "ds_read_b128 v[48:51], %1 offset:464\n\tds_read_b128 v[52:55], %3 offset:1024\n\ts_waitcnt lgkmcnt(5)\n\tv_mfma_f32_16x16x32_f16 v[96:99], v[108:111], v[44:47], v[96:99]\n\t" \
    "ds_read_b128 v[116:119], %1 offset:480\n\tds_read_b128 v[12:15], %3 offset:4096\n\tv_mfma_f32_16x16x32_f16 v[112:115], v[72:75], v[44:47], v[112:115]\n\t" \
    "ds_read_b128 v[8:11], %1 offset:2784\n\tv_mfma_f32_16x16x32_f16 v[96:99], v[24:27], v[104:107], v[96:99]\n\tds_read_b128 v[56:59], %1 offset:1488\n\tds_read_b128 v[28:31], %3 offset:0\n\t" \
    "v_mfma_f32_16x16x32_f16 v[112:115], v[76:79], v[104:107], v[112:115]\n\tds_read_b128 v[60:63], %1 offset:1504\n\tds_read_b128 v[32:35], %3 offset:2048\n\t" \
    "s_waitcnt lgkmcnt(7)\n\tv_mfma_f32_16x16x32_f16 v[112:115], v[80:83], v[100:103], v[112:115]\n\tv_mfma_f32_16x16x32_f16 v[112:115], v[84:87], v[20:23], v[112:115]\n\t" \
    "ds_read_b128 v[64:67], %3 offset:3072\n\tv_mfma_f32_16x16x32_f16 v[112:115], v[88:91], v[92:95], v[112:115]\n\tds_read_b128 v[36:39], %1 offset:2848\n\t" \
    "v_mfma_f32_16x16x32_f16 v[68:71], v[48:51], v[52:55], 0\n\tds_read_b128 v[16:19], %1 offset:976\n\ts_waitcnt lgkmcnt(3)\n\tv_mfma_f32_16x16x32_f16 v[72:75], v[116:119], v[12:15], 0\n\t" \
    "ds_read_b128 v[44:47], %1 offset:992\n\tv_mfma_f32_16x16x32_f16 v[112:115], v[40:43], v[8:11], v[112:115]\n\tv_add_u32 %7, 1880, %0\n\tds_read2_b32 v[76:77], %7 offset0:0 offset1:1\n\t" \
    "ds_read2_b32 v[78:79], %7 offset0:2 offset1:3\n\tv_mfma_f32_16x16x32_f16 v[104:107], v[56:59], v[28:31], 0\n\tds_read_b128 v[80:83], %1 offset:2000\n\t" \
    "ds_read_b128 v[84:87], %1 offset:2016\n\tv_mfma_f32_16x16x32_f16 v[100:103], v[60:63], v[32:35], 0\n\tv_cvt_pk_f16_f32 v20, v68, v72\n\tv_cvt_pk_f16_f32 v21, v69, v73\n\t" \
    "v_mfma_f32_16x16x32_f16 v[88:91], v[60:63], v[28:31], 0\n\tv_cvt_pk_f16_f32 v22, v70, v74\n\tv_cvt_pk_f16_f32 v23, v71, v75\n\tds_read2_b32 v[92:93], %5 offset0:48 offset1:49\n\t" \
    "ds_read2_b32 v[94:95], %5 offset0:50 offset1:51\n\ts_waitcnt lgkmcnt(6)\n\tv_mfma_f32_16x16x32_f16 v[48:51], v[60:63], v[64:67], 0\n\tds_read2_b64 v[40:43], %4 offset0:32 offset1:33\n\t" \
    "ds_read2_b64 v[8:11], %4 offset0:34 offset1:35\n\tv_mfma_f32_16x16x32_f16 v[112:115], v[120:123], v[36:39], v[112:115]\n\tv_cvt_pk_f16_f32 v56, v104, v100\n\t" \
    "v_cvt_pk_f16_f32 v58, v105, v101\n\tv_mfma_f32_16x16x32_f16 v[68:71], v[16:19], v[52:55], 0\n\tv_cvt_pk_f16_f32 v72, v106, v102\n\tv_cvt_pk_f16_f32 v74, v107, v103\n\t" \
    "v_mfma_f32_16x16x32_f16 v[60:63], v[44:47], v[12:15], 0\n\tv_cvt_pk_f16_f32 v57, v88, v48\n\tv_cvt_pk_f16_f32 v59, v89, v49\n\ts_waitcnt lgkmcnt(2)\n\t" \
    "v_mfma_f32_16x16x32_f16 v[96:99], v[76:79], v[116:119], v[96:99]\n\tv_cvt_pk_f16_f32 v73, v90, v50\n\tv_cvt_pk_f16_f32 v75, v91, v51\n\tds_read_b128 v[120:123], %1 offset:2912\n\t" \
    "v_mfma_f32_16x16x32_f16 v[36:39], v[80:83], v[28:31], 0\n\tds_read2_b32 v[52:53], %5 offset0:32 offset1:33\n\tds_read2_b32 v[54:55], %5 offset0:34 offset1:35\n\t" \
    "v_mfma_f32_16x16x32_f16 v[16:19], v[84:87], v[32:35], 0\n\tv_cvt_pk_f16_f32 v104, v68, v60\n\tv_cvt_pk_f16_f32 v105, v69, v61\n\tv_mfma_f32_16x16x32_f16 v[96:99], v[20:23], v[92:95], v[96:99]\n\t" \
    "v_cvt_pk_f16_f32 v106, v70, v62\n\tv_cvt_pk_f16_f32 v107, v71, v63\n\tds_read_b128 v[100:103], %1 offset:2976\n\tv_mfma_f32_16x16x32_f16 v[116:119], v[84:87], v[28:31], 0\n\t" \
    "ds_read2_b64 v[12:15], %4 offset0:16 offset1:17\n\tds_read2_b64 v[88:91], %4 offset0:18 offset1:19\n\tv_mfma_f32_16x16x32_f16 v[48:51], v[84:87], v[64:67], 0\n\t" \
    "v_cvt_pk_f16_f32 v80, v36, v16\n\tv_cvt_pk_f16_f32 v82, v37, v17\n\ts_waitcnt lgkmcnt(5)\n\tv_mfma_f32_16x16x32_f16 v[96:99], v[56:59], v[40:43], v[96:99]\n\t" \
    "v_cvt_pk_f16_f32 v32, v38, v18\n\tv_cvt_pk_f16_f32 v34, v39, v19\n\tv_mfma_f32_16x16x32_f16 v[96:99], v[72:75], v[8:11], v[96:99]\n\tds_read_b128 v[68:71], %1 offset:3040\n\t" \
    "v_mfma_f32_16x16x32_f16 v[112:115], v[124:127], v[120:123], v[112:115]\n\tv_cvt_pk_f16_f32 v81, v116, v48\n\tv_cvt_pk_f16_f32 v83, v117, v49\n\tv_mfma_f32_16x16x32_f16 v[112:115], v[76:79], v[44:47], v[112:115]\n\t" \
    "v_cvt_pk_f16_f32 v33, v118, v50\n\tv_cvt_pk_f16_f32 v35, v119, v51\n\ts_waitcnt lgkmcnt(1)\n\tv_mfma_f32_16x16x32_f16 v[112:115], v[20:23], v[52:55], v[112:115]\n\t" \
    "v_mul_f32 v96, %8, v96\n\tv_mul_f32 v97, %8, v97\n\tv_mfma_f32_16x16x32_f16 v[112:115], v[108:111], v[100:103], v[112:115]\n\tv_mul_f32 v98, %8, v98\n\t" \
    "v_mul_f32 v99, %8, v99\n\tv_mfma_f32_16x16x32_f16 v[112:115], v[56:59], v[12:15], v[112:115]\n\tds_write_b128 %2, v[96:99] offset:2048\n\tds_write_b8 %6, %6 offset:16\n\t" \
    CONV_P2 \
    "v_mfma_f32_16x16x32_f16 v[112:115], v[72:75], v[88:91], v[112:115]\n\tv_mfma_f32_16x16x32_f16 v[112:115], v[104:107], v[92:95], v[112:115]\n\ts_waitcnt lgkmcnt(2)\n\t" \
    "v_mfma_f32_16x16x32_f16 v[112:115], v[24:27], v[68:71], v[112:115]\n\tv_mfma_f32_16x16x32_f16 v[112:115], v[80:83], v[40:43], v[112:115]\n\tv_mfma_f32_16x16x32_f16 v[112:115], v[32:35], v[8:11], v[112:115]\n\t" \
    "s_nop 7\n\tv_mul_f32 v112, %8, v112\n\tv_mul_f32 v113, %8, v113\n\tv_mul_f32 v114, %8, v114\n\tv_mul_f32 v115, %8, v115\n\tds_write_b128 %2, v[112:115] offset:3072\n\t" \
    "ds_write_b8 %6, %6 offset:24\n\t"
#define CONV_SPIN(lab, next, off, slp) lab ":\n\tv_and_b32 %7, -8, %6\n\tds_read_b32 %7, %7 offset:" off "\n\ts_waitcnt lgkmcnt(0)\n\tv_readfirstlane_b32 %9, %7\n\t" \
    "s_cmp_ge_u32 %9, 4\n\ts_cbranch_scc1 " next "f\n\ts_sleep " slp "\n\ts_branch " lab "b\n"
#define CONV_WAIT "s_setprio 1\n" CONV_SPIN("3", "1", "36", "6") CONV_SPIN("1", "2", "32", "1") "2:\n\ts_setprio %11\n\t"
#define CONV_P0 "s_setprio %12\n\t"
#define CONV_P1 "s_setprio %13\n\t"
#define CONV_P2 "s_setprio %14\n\t"
#define CONV_CLOBBERS \
    "v8", "v9", "v10", "v11", "v12", "v13", "v14", "v15", "v16", "v17", "v18", "v19", "v20", "v21", "v22", "v23", "v24", "v25", "v26", "v27", "v28", "v29", "v30", "v31", \
    "v32", "v33", "v34", "v35", "v36", "v37", "v38", "v39", "v40", "v41", "v42", "v43", "v44", "v45", "v46", "v47", "v48", "v49", "v50", "v51", "v52", "v53", "v54", "v55", \
    "v56", "v57", "v58", "v59", "v60", "v61", "v62", "v63", "v64", "v65", "v66", "v67", "v68", "v69", "v70", "v71", "v72", "v73", "v74", "v75", "v76", "v77", "v78", "v79", \
    "v80", "v81", "v82", "v83", "v84", "v85", "v86", "v87", "v88", "v89", "v90", "v91", "v92", "v93", "v94", "v95", "v96", "v97", "v98", "v99", "v100", "v101", "v102", \
    "v103", "v104", "v105", "v106", "v107", "v108", "v109", "v110", "v111", "v112", "v113", "v114", "v115", "v116", "v117", "v118", "v119", "v120", "v121", "v122", "v123", \
    "v124", "v125", "v126", "v127"
DEV void conv(int hf, u32 sm0, int cw, float yscale) {
    int lane;
    asm volatile("v_mbcnt_lo_u32_b32 %0, -1, 0\n\tv_mbcnt_hi_u32_b32 %0, -1, %0" : "=v"(lane));
    __builtin_assume((unsigned)lane < 64u);
    const int al = lane & 15, q = lane >> 4;
    const u32 chb = sm0 + L_CH + cw * CH_SZ, kpar = chb + ((al & 1) ? 0 : REC_B + 2);
    u32 ka = kpar + (R_KA + ZA - al - 16 + 8 * q) * 2 - 960, ub0 = chb + C_U + (16 * al + 8 * q) * 2;
    u32 yb = sm0 + L_STG + cw * STG_BUF + 64 * al + 16 * (q ^ ((al >> 1) & 3));
    u32 bxp = sm0 + L_BOX + lane * 16, k2b = chb + (R_K2 + 192 - 4 * al - 4 + 16 * q - 128) * 2, k3b = chb + (R_K3 + 128 - 2 * al - 2 + 8 * q) * 2 - 192;
    u32 fl = sm0 + L_CNT + 8 + cw, t0, t1;
    u32 ys = __builtin_amdgcn_readfirstlane(__float_as_uint(yscale));
    static_assert(UPAD == 256 && (R_KP + 15 - (R_KA + ZA - 16)) * 2 + 960 == 1880 && L_CNT % 16 == 0, "offsets in the streams");
    asm volatile("s_cmp_eq_u32 %10, 0\n\ts_cbranch_scc1 8f\n\t" CONV_ASM_1 "s_branch 9f\n8:\n\t" CONV_ASM_0 "\n9:"
                 : "+v"(ka), "+v"(ub0), "+v"(yb), "+v"(bxp), "+v"(k2b), "+v"(k3b), "+v"(fl), "=&v"(t0), "+s"(ys), "=&s"(t1) : "s"(hf), "n"(CP_S), "n"(CP_0), "n"(CP_1), "n"(CP_2) : "memory", "scc", CONV_CLOBBERS);
}

constexpr int G_KEY = 8 * SL * 8, G_SZ = G_KEY + 256 * NREP * 16;
constexpr size_t WS_EPOCH = 0, WS_GRAN = 4096, WS_HAND = WS_GRAN + G_SZ, WS_U = WS_HAND + (size_t)(GU_WORDS + DN_WORDS) * 4,
                 WS_K = WS_U + (size_t)SL * SD * 2, WS_END = WS_K + (size_t)SD * 2048;
typedef __attribute__((ext_vector_type(8))) u32 u8v;
typedef __amdgpu_buffer_rsrc_t rsrc_t;
DEV rsrc_t mk_rsrc(u32 lo, u32 hi, size_t off, u32 bytes) {
    return __builtin_amdgcn_make_buffer_rsrc((__attribute__((address_space(1))) void*)((((unsigned long long)hi << 32) | lo) + off), 0, bytes, 0x00020000);
}
#define BLD128(rs, vo, so, aux) __builtin_amdgcn_raw_buffer_load_b128(rs, vo, so, aux)
DEV u32 lds_addr(const void* p) { return (u32)(uintptr_t)(__attribute__((address_space(3))) const void*)p; }

extern "C" __global__ __launch_bounds__(1024, 4) void k_fused(
    const u16* x_, const u16* wr_, char* ws_, u16* out_,
    const u16* logA_, const u16* Aim_, const u16* Cre_, const u16* Cim_,
    const u16* logdt_, const u16* dskip_, const u16* wg_, const u16* wu_, const u16* wd_) {
    __shared__ __attribute__((aligned(16))) char smem[L_TOTAL];
    const int tid = threadIdx.x, lane = tid & 63;
    const int wave = __builtin_amdgcn_readfirstlane(tid >> 6);
    const int xcd = blockIdx.x & 7, slot = blockIdx.x >> 3;
    const int g = xcd * 16 + (slot >> 1), hf = slot & 1;
    STAMP_DECL;
    STAMP(0);
    u32* cnt = reinterpret_cast<u32*>(smem + L_CNT);
    const u32 sm0 = lds_addr(smem);
    if (wave == 0) {
        if (lane < 32) cnt[lane] = 0;
        __builtin_amdgcn_s_waitcnt(0xc07f);
    }
    MEM_FENCE(); SCHED_FENCE();
    u32 kw0, kw1;
    asm volatile("s_load_dword %0, %2, 0x0\n\ts_load_dword %1, %2, 0x40" : "=&s"(kw0), "=&s"(kw1) : "s"(__builtin_amdgcn_kernarg_segment_ptr()) : "memory");
#define ARGS_LOAD() u8v ka0, ka1, ka2; u2 ka3; \
    asm volatile("s_load_dwordx8 %0, %4, 0x0\n\ts_load_dwordx8 %1, %4, 0x20\n\ts_load_dwordx8 %2, %4, 0x40\n\ts_load_dwordx2 %3, %4, 0x60" \
                 : "=&s"(ka0), "=&s"(ka1), "=&s"(ka2), "=&s"(ka3) : "s"(__builtin_amdgcn_kernarg_segment_ptr()) : "memory"); SCHED_FENCE()
#define ARGS_WAIT() do { SCHED_FENCE(); asm volatile("s_waitcnt lgkmcnt(0)" : "+s"(ka0), "+s"(ka1), "+s"(ka2), "+s"(ka3) : "s"(kw0), "s"(kw1) : "memory"); SCHED_FENCE(); } while (0)
#define LANE_IDS() int lane; asm volatile("v_mbcnt_lo_u32_b32 %0, -1, 0\n\tv_mbcnt_hi_u32_b32 %0, -1, %0" : "=v"(lane)); __builtin_assume((unsigned)lane < 64u); \
    const int tid = wave * 64 + lane, al = lane & 15, q = lane >> 4
#define XRS mk_rsrc(ka0[0], ka0[1], 0, SL * SD * 2)
#define GRS mk_rsrc(ka0[4], ka0[5], WS_GRAN, G_SZ)
#define URS mk_rsrc(ka0[4], ka0[5], WS_U, SL * SD * 2)
#define HRS mk_rsrc(ka0[4], ka0[5], WS_HAND, (GU_WORDS + DN_WORDS) * 4)
#define ORS mk_rsrc(ka0[6], ka0[7], 0, SL * SD * 2)
    u32 code_pf = 0;
    if (wave == 8 && slot * 2 < CODE_KB) {
        unsigned long long pc;
        asm volatile("s_getpc_b64 %0" : "=s"(pc));
        code_pf = *(const __attribute__((address_space(1))) u32*)((pc & ~255ull) + (unsigned)(slot * 2048 + lane * 32));
    }
    MEM_FENCE(); SCHED_FENCE();
    __builtin_amdgcn_s_barrier();
    MEM_FENCE(); SCHED_FENCE();
#define M0_ADD(n) "s_add_u32 m0, m0, " #n "\n\ts_nop 0\n\t"
    if (wave == 6) {
        unsigned long long pe;
        u32 ep;
        asm volatile("s_load_dwordx2 %0, %2, 0x10\n\ts_waitcnt lgkmcnt(0)\n\ts_load_dword %1, %0, %3\n\ts_waitcnt lgkmcnt(0)"
                     : "=&s"(pe), "=s"(ep) : "s"(__builtin_amdgcn_kernarg_segment_ptr()), "n"(WS_EPOCH) : "memory");
        const u32 tg = ep + 1u;
        STAMP(1);
        if (lane < 5) cnt[24 + lane] = lane == 4 ? tg : ((tg >> (6 * lane)) & 7u) | (((tg >> (6 * lane + 3)) & 7u) << 16);
        MEM_FENCE();
        lds_add1(cnt + 0);
    }
    MEM_FENCE(); SCHED_FENCE();
#define FIRST_BARRIER(bulk) do { MEM_FENCE(); SCHED_FENCE(); lds_add1(cnt + 17); MEM_FENCE(); SCHED_FENCE(); } while (0)
    const int b = xcd * 32 + slot;
    const unsigned long long all8 = 0x0706050403020100ull + ((sm0 + L_CNT + 8) & 0xffu) * 0x0101010101010101ull;
    const int ksl = 8 * (xcd >> 2) + (slot >> 2);
    typedef __attribute__((address_space(3))) u32* lptr32;
    typedef __attribute__((address_space(3))) u4* lptr128;
    auto stats = [&](const u4 (&v)[4], float& mb, float& den, float& rd) __attribute__((always_inline)) {
        f2 p[8];
#pragma unroll
        for (int i = 0; i < 8; ++i) { p[i].x = __uint_as_float(v[i >> 1][2 * (i & 1)]); p[i].y = __uint_as_float(v[i >> 1][2 * (i & 1) + 1]); }
        const f2 t = (((p[0] + p[1]) + (p[2] + p[3])) + ((p[4] + p[5]) + (p[6] + p[7]))) * (1.0f / SD);
        const float var = fmaxf(__builtin_fmaf(-t.x, t.x, t.y), 0.0f);
        mb = rbf(t.x);
        den = rbf(__builtin_amdgcn_sqrtf(rbf(rbf(var) + 1e-5f)));
        rd = __builtin_amdgcn_rcpf(den);
    };
    auto e_role = [&]() __attribute__((always_inline)) {
        ARGS_LOAD();
        __builtin_amdgcn_s_setprio(3);
        LANE_IDS();
        u32 vw = wave * 512 + (al >> 3) * 64 + q * 16;
        u32 vrow = (b * 8 + (lane >> 3)) * (SD * 2) + wave * 512 + (((lane & 7) ^ ((lane >> 3) & 2)) << 4);
        const u32 l_row = sm0 + L_CH + wave * CH_SZ + C_U + UPAD * 2;
        u32 a_x = l_row + (al & 7) * 128 + (((4 * (al >> 3) + q) ^ (al & 2)) << 4);
        PIN(vrow);
        u4 xa[4], wa[4];
        ARGS_WAIT();
#define XLD(nt) asm volatile("s_mov_b32 m0, %0\n\ts_nop 0\n\tglobal_load_lds_dwordx4 %1, %2" nt "\n\t" M0_ADD(896) "global_load_lds_dwordx4 %1, %2 offset:128" nt "\n\t" \
                     M0_ADD(896) "global_load_lds_dwordx4 %1, %2 offset:256" nt "\n\t" M0_ADD(896) "global_load_lds_dwordx4 %1, %2 offset:384" nt \
                     :: "s"(l_row), "v"(vrow), "s"(((unsigned long long)ka0[1] << 32) | ka0[0]) : "memory", "scc")
        if (X_NT) XLD(" nt"); else XLD("");
        MEM_FENCE(); SCHED_FENCE();
#pragma unroll
        for (int j = 0; j < 4; ++j) { wa[j] = u4{0x3f803f80u, 0x3f803f80u, 0x3f803f80u, 0x3f803f80u}; PIN(wa[j]); }
        PIN(vw); PIN(a_x);
        if (!(al & 4)) {
            const rsrc_t rrs = mk_rsrc(ka0[2], ka0[3], 0, SD * 2);
#pragma unroll
            for (int j = 0; j < 4; ++j) wa[j] = BLD128(rrs, vw + 128 * j, 0, 0);
        }
        FIRST_BARRIER(false);
        u4 sel[2];
#pragma unroll
        for (int h = 0; h < 2; ++h) {
#pragma unroll
            for (int i = 0; i < 4; ++i) sel[h][i] = (q == 2 * h + (al >> 3) && i == ((al & 7) >> 1)) ? (0x3f80u << (16 * (al & 1))) : 0u;
            PIN(sel[h]);
        }
        const int part = 2 * wave + (al >> 3);
        u32 a_p2 = sm0 + L_P12 + (al & 7) * P12_S + part * 8;
        u32 a_p1 = (q == 1) ? a_p2 : (q == (al >> 3) * 2) ? sm0 + L_P3 + (al & 7) * P3_S + part * 4 : sm0 + L_NUL + lane * 4;
        a_p2 = (q == (al >> 2)) ? a_p2 + 4 : sm0 + L_NUL + lane * 4;
        u32 a_pr = sm0 + L_P12 + (4 * (q & 1) + (al & 3)) * P12_S, a_cnt = sm0 + L_CNT;
        u32 vu[4], m7 = 0x00070007u;
#pragma unroll
        for (int j = 0; j < 4; ++j) { vu[j] = ((32 * wave + 8 * j + (lane >> 3)) * 256 + b) * 128 + (lane & 7) * 16; PIN(vu[j]); }
        u32 vp = g * 32768 + tid * 16, vs = (xcd * SL + tid) * 8;
        u32 a_u = sm0 + L_CH + (lane & 7) * CH_SZ + C_U + (UPAD + 64 * wave + 8 * (lane >> 3)) * 2;
        PIN(a_p1); PIN(a_p2); PIN(a_pr); PIN(a_cnt); PIN(m7); PIN(vp); PIN(vs); PIN(a_u);
        const rsrc_t urs = URS, grs = GRS;
        bool c1 = al & 1, c2_ = al & 2;
        lds_wait(cnt + 0, 1);
        const u32 tag = __builtin_amdgcn_readfirstlane(cnt[28]);
        u4 pat = *reinterpret_cast<const u4*>(cnt + 24);
        PIN(pat);
        MEM_FENCE(); SCHED_FENCE();
        {
            const f4 z4 = {0, 0, 0, 0};
            f4 d2 = z4, d1 = z4, xt[4][2];
            asm volatile("s_waitcnt vmcnt(4)\n\tds_read_b128 %0, %4\n\tds_read_b128 %1, %4 offset:1024\n\tds_read_b128 %2, %4 offset:2048\n\t"
                         "ds_read_b128 %3, %4 offset:3072\n\ts_waitcnt lgkmcnt(0)"
                         : "=&v"(xa[0]), "=&v"(xa[1]), "=&v"(xa[2]), "=&v"(xa[3]) : "v"(a_x) : "memory");
#pragma unroll
            for (int j = 0; j < 4; ++j) { d2 = mfma16b(xa[j], xa[j], d2); d1 = mfma16b(wa[j], xa[j], d1); }
            STAMP(1);
#pragma unroll
            for (int j = 0; j < XT_PRE; ++j) xt[j >> 1][j & 1] = mfma16b(xa[j >> 1], sel[j & 1], z4);
            SCHED_FENCE();
            const float e0 = c1 ? d2[1] : d2[0], e1 = c1 ? d2[3] : d2[2];
            *(lptr32)(uintptr_t)a_p2 = __float_as_uint(c2_ ? e1 : e0);
            *(lptr32)(uintptr_t)a_p1 = __float_as_uint(d1[0]);
            MEM_FENCE();
            lds_add1a(a_cnt + 4);
            __builtin_amdgcn_s_setprio(E_PRIO);
            SCHED_FENCE();
#pragma unroll
            for (int j = XT_PRE; j < XT_MID; ++j) xt[j >> 1][j & 1] = mfma16b(xa[j >> 1], sel[j & 1], z4);
#pragma unroll
            for (int j = 0; j < XT_MID; ++j) asm volatile("" :: "v"(xt[j >> 1][j & 1]));
            SCHED_FENCE();
            u4 pv[4];
            for (;;) {
                u32 c;
                asm volatile("ds_read_b32 %0, %5 offset:4\n\tds_read_b128 %1, %6\n\tds_read_b128 %2, %6 offset:16\n\tds_read_b128 %3, %6 offset:32\n\t"
                             "ds_read_b128 %4, %6 offset:48\n\ts_waitcnt lgkmcnt(0)"
                             : "=&v"(c), "=&v"(pv[0]), "=&v"(pv[1]), "=&v"(pv[2]), "=&v"(pv[3]) : "v"(a_cnt), "v"(a_pr) : "memory");
                if (__builtin_amdgcn_readfirstlane(c) >= 4u) break;
            }
            STAMP(2);
#pragma unroll
            for (int j = XT_MID; j < 8; ++j) xt[j >> 1][j & 1] = mfma16b(xa[j >> 1], sel[j & 1], z4);
            float mb, den, rd;
            stats(pv, mb, den, rd);
            const float a = LOG2E * rd, c = -mb * a;
            f2 a2[2], c2[2];
            a2[0].x = dpp_mov<0x00>(a); a2[0].y = dpp_mov<0x55>(a); a2[1].x = dpp_mov<0xaa>(a); a2[1].y = dpp_mov<0xff>(a);
            c2[0].x = dpp_mov<0x00>(c); c2[0].y = dpp_mov<0x55>(c); c2[1].x = dpp_mov<0xaa>(c); c2[1].y = dpp_mov<0xff>(c);
            STAMP(3);
#pragma unroll
            for (int jj = 0; jj < 4; jj += 2) {
                f2 v[8];
                u32 p[8];
#pragma unroll
                for (int i = 0; i < 8; ++i) {
                    const f4 t = xt[jj + (i >> 2)][(i >> 1) & 1];
                    v[i].x = t[2 * (i & 1)]; v[i].y = t[2 * (i & 1) + 1];
                    v[i] = __builtin_elementwise_fma(v[i], a2[i & 1], c2[i & 1]);
                }
                SCHED_FENCE();
#pragma unroll
                for (int i = 0; i < 8; ++i) p[i] = pkh(v[i]);
                SCHED_FENCE();
#pragma unroll
                for (int i = 0; i < 8; ++i) p[i] += 0x00040004u;
                SCHED_FENCE();
                u4 f[2];
#pragma unroll
                for (int k = 0; k < 2; ++k) {
                    const auto s0 = __builtin_amdgcn_permlane16_swap(p[4 * k], p[4 * k + 2], false, false);
                    const auto s1 = __builtin_amdgcn_permlane16_swap(p[4 * k + 1], p[4 * k + 3], false, false);
                    f[k].x = s0[0]; f[k].y = s1[0]; f[k].z = s0[1]; f[k].w = s1[1];
                }
                SCHED_FENCE();
#pragma unroll
                for (int k = 0; k < 2; ++k) {
#pragma unroll
                    for (int i = 0; i < 4; ++i) f[k][i] = bfi(m7, pat[i], f[k][i]);
                    __builtin_amdgcn_raw_buffer_store_b128(f[k], urs, vu[jj + k], 0, US_AUX);
                }
                SCHED_FENCE();
            }
        }
        MEM_FENCE(); SCHED_FENCE();
        lds_add1a(a_cnt + 23 * 4);
        __builtin_amdgcn_s_setprio(3);
        MEM_FENCE(); SCHED_FENCE();
        STAMP(4);
        const int t01 = 2 * hf, t23 = 6 - 2 * hf;
        u32 a_g = sm0 + L_CH + ((lane >> 2) & 3) * CH_SZ + C_U + (UPAD + 64 * wave + 16 * (lane >> 4) + 4 * (lane & 3)) * 2;
        u32 a_g0 = a_g + t01 * 512, a_g1 = a_g + t23 * 512;
        u32 vp2 = hf ? 0x80000000u : vp;
        PIN(a_g0); PIN(a_g1); PIN(vp2);
        __builtin_amdgcn_s_sleep(U_DELAY);
        lds_wait(cnt + 31, U_POS == 2 ? 4 : 1);
        STAMP(5);
        u4 up[8];
        u2 sg[4];
        auto ld_stats = [&]() __attribute__((always_inline)) {
#pragma unroll
            for (int t = 0; t < 4; ++t) sg[t] = __builtin_amdgcn_raw_buffer_load_b64(grs, vs + (t & 1) * 2048, (t < 2 ? t01 : t23) * 2048, 16);
        };
#pragma unroll
        for (int p = 0; p < 8; ++p) up[p] = BLD128(urs, p < 6 ? vp : vp2, p * 4096, U_AUX);
#pragma unroll
        for (int t = 0; t < 4; ++t) sg[t] = __builtin_amdgcn_raw_buffer_load_b64(grs, vs + (t & 1) * 2048, (t < 2 ? t01 : t23) * 2048, U_AUX);
        MEM_FENCE(); SCHED_FENCE();
        lds_add1a(a_cnt + 44);
        MEM_FENCE(); SCHED_FENCE();
        {
            u32 bad = 0, bad6 = 0;
#pragma unroll
            for (int p = 0; p < 8; ++p) {
#pragma unroll
                for (int i = 0; i < 4; ++i) up[p][i] ^= pat[i];
                *(lptr128)(uintptr_t)(a_u + p * 512) = up[p];
                bad |= up[p].x | up[p].y; PIN(bad);
                bad |= up[p].z | up[p].w; PIN(bad);
                if (p == 5) bad6 = bad;
                SCHED_FENCE();
            }
            STAMP(13);
            if (__builtin_expect(__any(((hf ? bad6 : bad) & m7) != 0), 0)) {
                u32 miss = 0;
#pragma unroll
                for (int p = 0; p < 8; ++p) if (__any(((up[p].x | up[p].y | up[p].z | up[p].w) & m7) != 0)) miss |= 1u << p;
                if (hf) miss &= 0x3fu;
#pragma unroll 1
                for (int spin = 0; miss != 0 && spin < SPIN_MAX; ++spin) {
                    MEM_FENCE();
#pragma unroll
                    for (int p = 0; p < 8; ++p) if ((miss >> p) & 1u) up[p] = BLD128(urs, vp, p * 4096, 16);
#pragma unroll
                    for (int p = 0; p < 8; ++p) if ((miss >> p) & 1u) {
#pragma unroll
                        for (int i = 0; i < 4; ++i) up[p][i] ^= pat[i];
                        *(lptr128)(uintptr_t)(a_u + p * 512) = up[p];
                        if (!__any(((up[p].x | up[p].y | up[p].z | up[p].w) & m7) != 0)) miss &= ~(1u << p);
                    }
                }
            }
            STAMP(6);
            MEM_FENCE();
            lds_add1a(a_cnt + 40);
            MEM_FENCE(); SCHED_FENCE();
        }
        __builtin_amdgcn_s_setprio(E_GP);
        u2 ut[4][2];
        asm volatile("ds_read_b64_tr_b16 %0, %8\n\tds_read_b64_tr_b16 %1, %8 offset:%10\n\tds_read_b64_tr_b16 %2, %8 offset:512\n\tds_read_b64_tr_b16 %3, %8 offset:512+%10\n\t"
                     "ds_read_b64_tr_b16 %4, %9\n\tds_read_b64_tr_b16 %5, %9 offset:%10\n\tds_read_b64_tr_b16 %6, %9 offset:512\n\tds_read_b64_tr_b16 %7, %9 offset:512+%10\n\t"
                     "s_waitcnt lgkmcnt(0)"
                     : "=&v"(ut[0][0]), "=&v"(ut[0][1]), "=&v"(ut[1][0]), "=&v"(ut[1][1]), "=&v"(ut[2][0]), "=&v"(ut[2][1]), "=&v"(ut[3][0]), "=&v"(ut[3][1])
                     : "v"(a_g0), "v"(a_g1), "n"(4 * CH_SZ) : "memory");
        if (__builtin_expect(__any(sg[0].y != tag || sg[1].y != tag || sg[2].y != tag || sg[3].y != tag), 0)) {
#pragma unroll 1
            for (int spin = 0; spin < SPIN_MAX; ++spin) {
                MEM_FENCE();
                ld_stats();
                if (!__any(sg[0].y != tag || sg[1].y != tag || sg[2].y != tag || sg[3].y != tag)) break;
                __builtin_amdgcn_s_sleep(G_SLEEP);
            }
        }
        STAMP(7);
        u32 gh[4][4];
        float xf[4][8];
        auto gate = [&](int t) __attribute__((always_inline)) {
            gate4(gh[t], ut[t][0].x, ut[t][0].y, ut[t][1].x, ut[t][1].y);
            const float mt = bflo(sg[t].x), dt = bfhi(sg[t].x) * (1.0f / LOG2E);
#pragma unroll
            for (int c = 0; c < 8; ++c) {
                const u32 uw = ut[t][c >> 2][(c >> 1) & 1];
                float xx = __builtin_fmaf((float)__builtin_bit_cast(h2, uw)[c & 1], dt, mt);
                PIN(xx);
                xf[t][c] = xx;
            }
            MEM_FENCE(); SCHED_FENCE();
        };
        gate(0); gate(1);
        if (!E_ORD) { gate(2); gate(3); }
        lds_wait(cnt + 19, 1);
        u32 vo[4];
        {
            const int i0 = 0xffff - (int)(__builtin_amdgcn_readfirstlane(cnt[12]) & 0xffffu);
            const int i1 = 0xffff - (int)(__builtin_amdgcn_readfirstlane(cnt[13]) & 0xffffu);
#pragma unroll
            for (int j = 0; j < 4; ++j) {
                const int l = 256 * ((j < 2 ? t01 : t23) + (j & 1)) + tid;
                vo[j] = (l == i0 || l == i1) ? 0x80000000u : l * (SD * 2) + g * 16;
                PIN(vo[j]);
            }
        }
        const char* yp = smem + L_STG + 64 * (tid >> 4) + 16 * (((tid >> 2) & 3) ^ ((tid >> 5) & 3)) + 4 * (tid & 3);
        const rsrc_t ors = ORS;
        STAMP(8);
        auto epi = [&](int t) __attribute__((always_inline)) {
            if (t >= E_UP) __builtin_amdgcn_s_setprio(E_EP);
            while (__hip_atomic_load(reinterpret_cast<unsigned long long*>(cnt + 2 + 2 * t), __ATOMIC_RELAXED, __HIP_MEMORY_SCOPE_WORKGROUP) != all8) __builtin_amdgcn_s_sleep(E_SLP);
            __builtin_amdgcn_fence(__ATOMIC_ACQUIRE, "wavefront");
            __builtin_amdgcn_wave_barrier();
            if (t == 0) STAMP(9);
            if (t == 2) STAMP(14);
            if (t == 3) STAMP(11);
            float yv[8];
#pragma unroll
            for (int c = 0; c < 8; ++c) yv[c] = *reinterpret_cast<const float*>(yp + c * STG_BUF + 1024 * t);
            u4 o;
#pragma unroll
            for (int h = 0; h < 4; ++h) {
                const h2 gg = __builtin_bit_cast(h2, gh[t][h]);
                float o0 = __builtin_fmaf(yv[2 * h], (float)gg[0], xf[t][2 * h]);
                float o1 = __builtin_fmaf(yv[2 * h + 1], (float)gg[1], xf[t][2 * h + 1]);
                PIN(o0); PIN(o1);
                o[h] = pkbf(o0, o1);
            }
            if (t < PACE) { __builtin_amdgcn_s_waitcnt(0x0f70); MEM_FENCE(); SCHED_FENCE(); }
            __builtin_amdgcn_raw_buffer_store_b128(o, ors, vo[t], 0, 0);
            MEM_FENCE(); SCHED_FENCE();
            if (t == 1) STAMP(10);
            if (t == 3) STAMP(12);
        };
        epi(0); epi(1);
        if (E_ORD) { __builtin_amdgcn_s_setprio(E_GP); gate(2); gate(3); }
        epi(2); epi(3);
    };
    auto f_role = [&]() __attribute__((always_inline)) {
        constexpr int WE = W_A;
        static_assert(W_A >= 2 && W_A + W_B + W_E <= 16, "weight loads");
        ARGS_LOAD();
        __builtin_amdgcn_s_setprio(3);
        LANE_IDS();
        const int hw = wave - 4;
        const bool has_gu = hf || hw < 3;
        const int mat = (slot >> 1) & 1, cblk = 7 * ((xcd + 3) & 3) + (hf ? hw : 4 + (hw < 3 ? hw : 0));
        const int chk = (xcd & 3) * 32 + slot, dblk = (xcd < 4 ? 4 : 0) + hw;
        auto f_box = [&]() __attribute__((always_inline)) {
            const int off = (hw == 2) ? 4 : 8, bw = (hw == 0) ? 2 : (hw == 1) ? 4 : (hw == 2) ? 1 : 3;
            *reinterpret_cast<h8*>(smem + L_BOX + (hw == 3 ? 4 : hw) * 1024 + lane * 16) = box_frag(al, q, off, bw);
            if (hw == 2) *reinterpret_cast<h8*>(smem + L_BOX + 3072 + lane * 16) = box_frag(al, q, 12, 1);
            const u4 z = {0, 0, 0, 0};
#pragma unroll
            for (int i = 0; i < 1; ++i) *reinterpret_cast<u4*>(smem + L_Z + min((hw * 64 + lane) * 16, Z_SZ - 16)) = z;
            static_assert(Z_SZ <= 4096, "one store per thread");
            MEM_FENCE();
            SCHED_FENCE();
        };
        u32 wvo = ((64 * ksl + q) * SF + 128 * cblk + 8 * al) * 2;
        u32 a_cnt = sm0 + L_CNT, v16 = (hw == 0) ? lane * 16 : 0x80000000u;
        PIN(wvo); PIN(a_cnt); PIN(v16);
        ARGS_WAIT();
        u4 wr[2], wv[16], wdv[8];
        {
            const rsrc_t rrs = mk_rsrc(ka0[2], ka0[3], 0, SD * 2);
            wr[0] = BLD128(rrs, v16, 0, 0); wr[1] = BLD128(rrs, v16, 1024, 0);
        }
        MEM_FENCE(); SCHED_FENCE();
        if (W_DELAY && hw != 2) __builtin_amdgcn_s_sleep(W_DELAY);
        lds_wait(cnt + 17, 12);
        const rsrc_t wrs = mk_rsrc(mat ? ka2[6] : ka2[4], mat ? ka2[7] : ka2[5], 0, has_gu ? SD * SF * 2 : 0);
        auto load_w = [&](int j) __attribute__((always_inline)) {
            wv[j] = BLD128(wrs, wvo, 4 * j * (SF * 2), W_AUX);
            MEM_FENCE(); SCHED_FENCE();

        };
        STAMP(8);
        load_w(0); load_w(1);
        f_box();
        lds_add1a(a_cnt + 22 * 4);
#pragma unroll
        for (int j = 2; j < WE; ++j) { load_w(j); if (j + 1 < WE) __builtin_amdgcn_s_sleep(W_PACE); }
        MEM_FENCE(); SCHED_FENCE();
        STAMP(10);
        const rsrc_t grs = GRS;
        u32 tag;
        {
            float sw = 0.f;
            u32 a_pr = sm0 + L_P12 + (lane & 7) * P12_S, a_p3 = sm0 + L_P3 + (lane & 7) * P3_S;
            u32 vpub = (hw == 0) ? (lane < NREP ? G_KEY + (lane * 256 + (KMAP ? (int)blockIdx.x : b)) * 16 : 0x80000000u) : ((lane & 1) ? 0x80000000u : ((lane >> 3) * SL + b * 8 + (lane & 7)) * 8);
            u32 ck = 0xffff - (b * 8 + (lane & 7));
            PIN(a_pr); PIN(a_p3); PIN(vpub); PIN(ck);
            lds_wait(cnt + 0, 1);
            tag = __builtin_amdgcn_readfirstlane(cnt[28]);
            MEM_FENCE(); SCHED_FENCE();
            if (hw == 0) {
#pragma unroll
                for (int j = 0; j < 8; ++j) sw = dot2(wr[j >> 2][j & 3], 0x3f803f80u, sw);
                sw = wave_sum(sw);
            }
            MEM_FENCE(); SCHED_FENCE();
            if (hw < 2) {
                u4 pv[4];
                f4 s0, s1;
                lds_wait(cnt + 1, P_LIGHT);
                for (;;) {
                    u32 c;
                    asm volatile("ds_read_b32 %0, %7 offset:4\n\tds_read_b128 %1, %8\n\tds_read_b128 %2, %8 offset:16\n\tds_read_b128 %3, %8 offset:32\n\t"
                                 "ds_read_b128 %4, %8 offset:48\n\tds_read_b128 %5, %9\n\tds_read_b128 %6, %9 offset:16\n\ts_waitcnt lgkmcnt(0)"
                                 : "=&v"(c), "=&v"(pv[0]), "=&v"(pv[1]), "=&v"(pv[2]), "=&v"(pv[3]), "=&v"(s0), "=&v"(s1) : "v"(a_cnt), "v"(a_pr), "v"(a_p3) : "memory");
                    const u32 cc = __builtin_amdgcn_readfirstlane(c);
                    if (cc >= 4u) break;
                    if (cc == 0u) __builtin_amdgcn_s_sleep(1);
                }
                float mb, den, rd;
                stats(pv, mb, den, rd);
                u4 f; f.y = tag; f.w = tag;
                if (hw == 0) {
                    const f4 sv = s0 + s1;
                    const u32 rb = __float_as_uint(rbf(__builtin_fmaf(-mb, sw, (sv[0] + sv[1]) + (sv[2] + sv[3])) * rd));
                    u32 m1 = ((rb ^ ((u32)((int)rb >> 31) | 0x80000000u)) & 0xffff0000u) | ck, m2 = 0;
                    top2_of8(m1, m2);
                    f.x = __builtin_amdgcn_readlane(m1, 7); f.z = __builtin_amdgcn_readlane(m2, 7);
                } else {
                    f.x = __builtin_amdgcn_perm(__float_as_uint(den), __float_as_uint(mb), 0x07060302u);
                    f.z = __builtin_amdgcn_mov_dpp(f.x, 0xf5, 0xf, 0xf, true);
                }
                __builtin_amdgcn_raw_buffer_store_b128(f, grs, vpub, 0, 16);
            }
            MEM_FENCE(); SCHED_FENCE();
        }
        if (W_GATE) lds_wait<F_SLP>(cnt + 23, 4);
        STAMP(4);
#pragma unroll
        for (int j = WE; j < WE + W_B; ++j) { load_w(j); if (hw >= 2) __builtin_amdgcn_s_sleep(W_PACE2); }
        u32 vtag = tag, dvo = (q * SD + 128 * dblk + 8 * al) * 2;
        u32 aoff = sm0 + L_XN + (al >> 2) * XN_S + 16 * q, hoff = sm0 + L_H + (al >> 2) * H_S + 16 * q;
        u32 idx = ((al & 3) * 0x4444u) | (((al & 3) ^ 1) * 0x1111u);
        const u32 Q = 32 * cblk + (lane & 31), cj = __umul24(Q, 9363u) >> 16, gi = Q - 7 * cj;
        u32 gu_off = !has_gu ? 0x80000000u : GU_L ? ((((cj * 2 + (q >> 1)) * 16 + ksl) * 2 + mat) * 8 + gi) * 16 : (((((cj * 2 + (q >> 1)) * 2 + (gi >> 2)) * 16 + ksl) * 2 + mat) * 4 + (gi & 3)) * 16;
        u32 aoffs = ((GU_L ? (chk * 2 + (hw & 1)) * 256 + (lane >> 2) * 8 + 4 * (hw >> 1) + (lane & 3) : (chk * 4 + (hw & 1) * 2 + (hw >> 1)) * 128 + lane) - ((hw >= 2 && (lane & 3) == 3) ? 1 : 0)) * 16;
        u32 h_st = sm0 + L_H + (hw & 1) * (2 * H_S) + (16 * (hw >> 1) + 4 * (lane & 3) + q) * 8;
        u32 dn_off = (GU_WORDS * 4) + (DN_T ? (((dblk * 16 + al) * 4 + q) * 128 + (DN_T == 1 ? slot * 4 + (xcd & 3) : chk)) * 16 : ((chk * 8 + dblk) * 64 + lane) * 16);
        PIN(vtag); PIN(dvo); PIN(aoff); PIN(hoff); PIN(idx); PIN(gu_off); PIN(aoffs); PIN(h_st); PIN(dn_off);
        MEM_FENCE(); SCHED_FENCE();
#pragma unroll
        for (int j = WE + W_B; j < 16 - W_E; ++j) { load_w(j); __builtin_amdgcn_s_sleep(W_PACE2); }
        STAMP(5);
        int dso[7];
#pragma unroll
        for (int j = 0; j < 7; ++j) {
            const int Qd = 7 * chk + j;
            dso[j] = (128 * (Qd >> 5) + 8 * (Qd & 15) + 4 * ((Qd >> 4) & 1)) * (SD * 2);
            asm volatile("" : "+s"(dso[j]));
        }
        u32 oxr = 0x80000000u, osr = 0x80000000u;
        if (hw == 3) {
            u32 vk = G_KEY + ((blockIdx.x % NREP) * 256 + lane) * 16;
            PIN(vk);
            lds_wait(cnt + 22, 4);
            lds_wait(cnt + 23, 4);
            __builtin_amdgcn_s_sleep(G_DELAY);
            u4 kg[4];
            poll_pieces<4, G_SLEEP>(kg, [&](int p) { return BLD128(grs, vk + 1024 * p, 0, 16); }, [&](u4 v_) { return v_.w != tag; }, [&]() {});
            if (U_POS == 0) lds_add1a(a_cnt + 31 * 4);
            STAMP(2);
            u32 m1 = max(kg[0].x, kg[1].x), m2 = min(kg[0].x, kg[1].x);
            m2 = med3u(m1, m2, kg[2].x); m1 = max(m1, kg[2].x); m2 = med3u(m1, m2, kg[3].x); m1 = max(m1, kg[3].x);
            m2 = max(max(m2, max(kg[0].z, kg[1].z)), max(kg[2].z, kg[3].z));
            wave_top2(m1, m2);
            lds_add1a(a_cnt + 21 * 4);
            if (U_POS == 1) lds_add1a(a_cnt + 31 * 4);
            const u32 k1 = __builtin_amdgcn_readlane(m1, 63), k2 = __builtin_amdgcn_readlane(m2, 63);
            const u32 keyl = (lane & 32) ? k2 : k1;
            const u32 ts = (0xffffu - keyl) & (SL - 1);
            osr = (xcd * SL + ts) * 8;
            oxr = ts * (SD * 2) + 128 * ksl + 4 * (lane & 31);
            if ((lane & 31) == 0) cnt[12 + (lane >> 5)] = keyl;
        } else {
            lds_wait<F_SLP>(cnt + 21, 1);
        }
        MEM_FENCE(); SCHED_FENCE();
        const u32 xr = __builtin_amdgcn_raw_buffer_load_b32(XRS, oxr, 0, 0);
        u2 sr = __builtin_amdgcn_raw_buffer_load_b64(grs, osr, 0, 16);
        MEM_FENCE(); SCHED_FENCE();
        if (U_POS < 2 && W_E > 0) lds_wait(cnt + 11, 4);
#pragma unroll
        for (int j = 16 - W_E; j < 16; ++j) load_w(j);
        if (U_POS == 2) lds_add1a(a_cnt + 31 * 4);
        MEM_FENCE(); SCHED_FENCE();
        if (hw == 3) {
            u32 a_xn = sm0 + L_XN + (lane >> 5) * (2 * XN_S) + (lane & 31) * 16;
            PIN(a_xn);
            if (__builtin_expect(!__all(sr.y == tag), 0)) {
#pragma unroll 1
                for (int spin = 0; spin < SPIN_MAX && !__all(sr.y == tag); ++spin) { MEM_FENCE(); sr = __builtin_amdgcn_raw_buffer_load_b64(grs, osr, 0, 16); __builtin_amdgcn_s_waitcnt(0x0f70); }
            }
            const u32 ssl = sr.x;
            const float mb2 = bflo(ssl), rc2 = __builtin_amdgcn_rcpf(bfhi(ssl));
            const f2 o0 = rbf2(rbf2(bfup(xr) - mb2) * rc2);
#pragma unroll
            for (int h = 0; h < 2; ++h) {
                *(lptr32)(uintptr_t)(a_xn + h * (XN_S + 4)) = __float_as_uint(o0.x); *(lptr32)(uintptr_t)(a_xn + h * (XN_S + 4) + 8) = __float_as_uint(o0.y);
            }
            if ((lane & 31) == 0) cnt[14 + (lane >> 5)] = ssl;
            MEM_FENCE();
            lds_add1a(a_cnt + 19 * 4);
            MEM_FENCE(); SCHED_FENCE();
            STAMP(3);
        }
        auto load_wd = [&](int j0, int j1) __attribute__((always_inline)) {
            const rsrc_t drs = mk_rsrc(ka3[0], ka3[1], 0, SD * SF * 2);
            MEM_FENCE(); SCHED_FENCE();
#pragma unroll
            for (int j = j0; j < j1; ++j) {
                wdv[j] = BLD128(drs, dvo, dso[j], W_AUX);
                if (D_PACE) { MEM_FENCE(); SCHED_FENCE(); __builtin_amdgcn_s_sleep(D_PACE); }
            }
            wdv[7] = u4{0, 0, 0, 0};
            MEM_FENCE(); SCHED_FENCE();
        };
        if (WD_POS == 0) { lds_wait(cnt + 11, 4); load_wd(0, WD_1); }
        STAMP(6);
        const rsrc_t hrs = HRS;
        {
            f4 a0 = {0, 0, 0, 0};
            u4 Af[4], Ag[4];
            lds_wait(cnt + 19, 1);
            STAMP(7);
#pragma unroll
            for (int t = 0; t < 4; ++t) Ag[t] = *(__attribute__((address_space(3))) const u4*)(uintptr_t)(aoff + 64 * t);
#pragma unroll
            for (int t = 0; t < 4; ++t) Af[t] = *(__attribute__((address_space(3))) const u4*)(uintptr_t)(aoff + 256 + 64 * t);
#pragma unroll
            for (int t = 0; t < 4; ++t) a0 = smfma(Ag[t], wv[2 * t], wv[2 * t + 1], a0, idx);
#pragma unroll
            for (int t = 0; t < 4; ++t) a0 = smfma(Af[t], wv[8 + 2 * t], wv[9 + 2 * t], a0, idx);
            __builtin_amdgcn_raw_buffer_store_b128(tag4(a0, vtag), hrs, gu_off, 0, 16);
        }
        MEM_FENCE(); SCHED_FENCE();
        STAMP(9);
        if (WD_DELAY) __builtin_amdgcn_s_sleep(WD_DELAY);
        if (WD_POS != 0) load_wd(0, WD_1);
        if (blockIdx.x == 0 && tid == 256)
            __hip_atomic_store((__attribute__((address_space(1))) u32*)((((unsigned long long)ka0[5] << 32) | ka0[4]) + WS_EPOCH), tag, __ATOMIC_RELAXED, __HIP_MEMORY_SCOPE_AGENT);
        const u32 keyf = __builtin_amdgcn_readfirstlane(cnt[12 + (hw >> 1)]), stf = __builtin_amdgcn_readfirstlane(cnt[14 + (hw >> 1)]);
        const int tokf = (0xffff - (int)(keyf & 0xffffu)) & (SL - 1), ftile = tokf >> 8;
        const bool mine = hf ? (ftile >= 2 && ftile <= 5) : (ftile < 2 || ftile > 5);
        const int chf = 4 * (hw & 1) + q;
        const u32 eoff = (tokf * SD + g * 8 + chf) * 2;
        const u32 xe = __builtin_amdgcn_raw_buffer_load_b16(XRS, mine ? eoff : 0x80000000u, 0, 0);
        MEM_FENCE(); SCHED_FENCE();
        __builtin_amdgcn_s_sleep(A_DELAY);
        STAMP(11);
        {
            u4 pv[2];
            poll_pieces<2, A_SLEEP>(pv, [&](int i) { return BLD128(hrs, aoffs + (GU_L ? 2048 : 1024) * i, 0, 16); }, [&](u4 v) { return tagof(v) != tag; }, [&]() { load_wd(WD_1, 7); });
            STAMP(12);
            float v[4];
#pragma unroll
            for (int i = 0; i < 4; ++i) v[i] = __uint_as_float(pv[0][i] & 0xffffff00u) + __uint_as_float(pv[1][i] & 0xffffff00u);
            const float r = rbf(dpp_add<0x128, 0xf>(fold4(v[0], v[2], v[1], v[3])));
            const float up = dpp_mov<0x104>(r);
            const u32 hv = __float_as_uint(rbf(rbf(silu_fast(r)) * up));
            if (al < (hw >= 2 ? 3 : 4)) { *(lptr32)(uintptr_t)h_st = hv; *(lptr32)(uintptr_t)(h_st + H_S + 4) = hv; }
            MEM_FENCE();
            lds_add1a(a_cnt + 18 * 4);
            u4 Ah[4];
            lds_wait_read4<0, 64>(a_cnt + 18 * 4, 4, hoff, Ah);
            f4 a0 = {0, 0, 0, 0};
#pragma unroll
            for (int t = 0; t < 4; ++t) a0 = smfma(Ah[t], wdv[2 * t], wdv[2 * t + 1], a0, idx);
            __builtin_amdgcn_raw_buffer_store_b128(tag4(a0, vtag), hrs, dn_off, 0, 16);
        }
        MEM_FENCE(); SCHED_FENCE();
        STAMP(13);
        if (mine) {
            u4 pv[2];
            u32 boff = (GU_WORDS * 4) + (DN_T ? ((g * 4 + hw) * 128 + lane) * 16 : ((lane * 8 + (g >> 4)) * 64 + hw * 16 + (g & 15)) * 16);
            constexpr int BO2 = DN_T ? 1024 : 64 * 8 * 1024;
            const u32 rb = (keyf & 0x80000000u) ? (keyf & 0x7fff0000u) : (~keyf & 0xffff0000u);
            float rs = rbf(__builtin_amdgcn_rcpf(1.0f + __builtin_amdgcn_exp2f(-LOG2E * __uint_as_float(rb))));
            const int oi = hf ? ftile - 2 : (ftile < 2 ? ftile : ftile - 4);
            float xnv = rbf(rbf(bflo(xe) - bflo(stf)) * __builtin_amdgcn_rcpf(bfhi(stf)));
            const int tt = tokf & 255;
            u32 ya = sm0 + L_STG + chf * STG_BUF + 1024 * oi + 64 * (tt >> 4) + 16 * (((tt >> 2) & 3) ^ ((tt >> 5) & 3)) + 4 * (tt & 3);
            u32 yc = a_cnt + 8 + 8 * oi + chf;
            u32 eo = (al == 0) ? eoff : 0x80000000u;
            PIN(boff); PIN(rs); PIN(xnv); PIN(ya); PIN(yc); PIN(eo);
            MEM_FENCE(); SCHED_FENCE();
            __builtin_amdgcn_s_sleep(B_DELAY);
            pv[0] = BLD128(hrs, boff, 0, 16); pv[1] = BLD128(hrs, boff, BO2, 16);
            if (B_GAP == 0) {
#pragma unroll 1
                for (int spin = 0; spin < SPIN_MAX; ++spin) {
                    const bool b0 = tagof(pv[0]) != tag, b1 = tagof(pv[1]) != tag;
                    if (!__any(b0 || b1)) break;
                    MEM_FENCE();
                    __builtin_amdgcn_s_sleep(B_SLEEP);
                    if (b0) pv[0] = BLD128(hrs, boff, 0, 16);
                    if (b1) pv[1] = BLD128(hrs, boff, BO2, 16);
                }
            } else {
#pragma unroll 1
                for (int spin = 0; spin < SPIN_MAX; ++spin) {
                    const bool b0 = tagof(pv[0]) != tag, b1 = tagof(pv[1]) != tag;
                    if (!__any(b0 || b1)) break;
                    const u32 o0 = b0 ? boff : 0x80000000u, o1 = b1 ? boff : 0x80000000u;
                    MEM_FENCE(); SCHED_FENCE();
                    const u4 r0 = BLD128(hrs, o0, 0, 16), r1 = BLD128(hrs, o1, BO2, 16);
                    MEM_FENCE(); SCHED_FENCE();
                    __builtin_amdgcn_s_sleep(B_GAP);
                    const u4 s0 = BLD128(hrs, o0, 0, 16), s1 = BLD128(hrs, o1, BO2, 16);
                    MEM_FENCE(); SCHED_FENCE();
                    if (b0) pv[0] = r0;
                    if (b1) pv[1] = r1;
                    PIN(pv[0]); PIN(pv[1]);
                    if (!__any(tagof(pv[0]) != tag || tagof(pv[1]) != tag)) break;
                    if (b0) pv[0] = s0;
                    if (b1) pv[1] = s1;
                }
            }
            STAMP(14);
            float sv; u32 c;
            asm volatile("ds_read_u8 %0, %2\n\tds_read_b32 %1, %3" : "=&v"(c), "=&v"(sv) : "v"(yc), "v"(ya) : "memory");
            SCHED_FENCE();
            float v[4];
#pragma unroll
            for (int i = 0; i < 4; ++i) v[i] = __uint_as_float(pv[0][i] & 0xffffff00u) + __uint_as_float(pv[1][i] & 0xffffff00u);
            const float tot = row_sum(fold4(v[0], v[2], v[1], v[3]));
            float gt = LOG2E * silu_fast(rbf(xnv + rbf(rbf(tot) * rs)));
            asm volatile("s_waitcnt lgkmcnt(0)" : "+v"(c), "+v"(sv), "+v"(gt) :: "memory");
            while (__any(c == 0u)) {
                __builtin_amdgcn_s_sleep(1);
                asm volatile("ds_read_u8 %0, %2\n\tds_read_b32 %1, %3\n\ts_waitcnt lgkmcnt(0)" : "=&v"(c), "=&v"(sv) : "v"(yc), "v"(ya) : "memory");
            }
            const float ov = __builtin_fmaf(sv, gt, bflo(xe));
            __builtin_amdgcn_raw_buffer_store_b16((u16)pkbf(ov, ov), ORS, eo, 0, 0);
        }
    };
    if (__builtin_expect(wave < 4, E_FIRST)) {
        e_role();
    } else if (wave < 8) {
        f_role();
    } else {
        ARGS_LOAD();
        const bool gen = !KX || wave < 12;
        const int cw = KX ? (wave + 4 * hf) & 7 : wave - 8;
        __builtin_amdgcn_s_setprio(2);
        const int d = g * 8 + cw;
        char* ch = smem + L_CH + cw * CH_SZ;
        u32 vp = d * (SN * 2) + (tid & 63) * 4, vd = d * 2;
        PIN(vp); PIN(vd);
        ARGS_WAIT();
        const u32 pDT = __builtin_amdgcn_raw_buffer_load_b16(mk_rsrc(ka2[0], ka2[1], 0, SD * 2), vd, 0, 0);
        const u32 pLA = __builtin_amdgcn_raw_buffer_load_b32(mk_rsrc(ka1[0], ka1[1], 0, SD * SN * 2), vp, 0, 2);
        const u32 pAI = __builtin_amdgcn_raw_buffer_load_b32(mk_rsrc(ka1[2], ka1[3], 0, SD * SN * 2), vp, 0, 2);
        const u32 pCR = __builtin_amdgcn_raw_buffer_load_b32(mk_rsrc(ka1[4], ka1[5], 0, SD * SN * 2), vp, 0, 2);
        const u32 pCI = __builtin_amdgcn_raw_buffer_load_b32(mk_rsrc(ka1[6], ka1[7], 0, SD * SN * 2), vp, 0, 2);
        const u32 pDS = __builtin_amdgcn_raw_buffer_load_b16(mk_rsrc(ka2[2], ka2[3], 0, SD * 2), vd, 0, 0);
        STAMP(4);
        FIRST_BARRIER(false);
        int lane = tid & 63;
        PIN(lane);
        const int al = lane & 15, q = lane >> 4;
        auto clear = [&](char* c, int n) {
            const u4 z = {0, 0, 0, 0};
#pragma unroll
            for (int i = 0; i < 4; ++i) *reinterpret_cast<u4*>(c + i * 1024 + lane * 16) = z;
            if (lane < n) *reinterpret_cast<u4*>(c + 4096 + lane * 16) = z;
        };
        auto put4 = [&](char* c, int idx, u32 lo, u32 hi) {
            u16* rec = reinterpret_cast<u16*>(c);
            u16* rec1 = reinterpret_cast<u16*>(c + REC_B + 2);
            u2 o; o.x = lo; o.y = hi;
            *reinterpret_cast<u2*>(rec + idx) = o;
            rec1[idx] = (u16)lo;
            *reinterpret_cast<u32*>(rec1 + idx + 1) = (lo >> 16) | (hi << 16);
            rec1[idx + 3] = (u16)(hi >> 16);
        };
        const u32 chb = sm0 + L_CH + cw * CH_SZ, nul = sm0 + L_NUL + lane * 4;
        u32 sc_x = R_KA + ZA - 3 - 128 - 256 * (q >> 1) - 4 * (q & 1) - 8 * al;
        u32 sc_2 = chb + (R_K2 + 190 - 32 * (q >> 1) - 64 * (q & 1) - 2 * al) * 2, sc_h = chb + (R_K3 + 127 - 16 - 32 * q - al) * 2, sc_l = lane ? sc_h + 32 : nul;
        const int pj = (lane < 18) ? (lane >= 9 ? lane - 9 : lane) : (lane >= 21 ? lane - 21 : lane - 18), pcopy = (lane < 18) ? (lane >= 9) : (lane >= 21);
        u32 sc_p = lane < 24 ? chb + ((lane < 18) ? (R_KP + 1055 - 1027 - pj) : (R_KA + ZA - 514 + pj)) * 2 + (pcopy ? REC_B + 2 : 0) : nul;
        int pcls = (lane < 18) ? (pj < 2 ? 0 : 1) : 2;
        const u32 stg = sm0 + L_STG + cw * STG_BUF + q * 16;
        u32 fr_b = stg + al * STG_ROW, fr_z = stg + 16 * STG_ROW;
        u32 fr_x = stg + (al < 8 ? al : 8 + (((al & 3) == 2) ? ((al - 8) >> 1) : (2 * ((al - 7) >> 2)))) * STG_ROW;
        u32 fr_y = stg + ((al & 10) == 0 ? 13 + (al >> 1) + (al & 1) - ((al >> 1) & 1) : (al >> 2)) * STG_ROW;
        asm volatile("s_mov_b32 m0, %0\n\ts_nop 0" :: "s"(sm0 + L_STG + cw * STG_BUF) : "memory");
        {
            hcx one; one.r = h2{(f16)1.0f, (f16)1.0f}; one.i = h2{(f16)0.0f, (f16)0.0f};
            stg_put<0>(one);
        }
        clear(ch, 40);
        PIN(sc_x); PIN(sc_2); PIN(sc_h); PIN(sc_l); PIN(sc_p); PIN(pcls); PIN(fr_b); PIN(fr_z); PIN(fr_x); PIN(fr_y);
        MEM_FENCE(); SCHED_FENCE();
        const float dt = rbf(exp_acc(bflo(pDT)));
        f2 ar, ai, cr, ci;
        {
            f2 la = bfup(pLA);
            f2 ex; ex.x = __builtin_amdgcn_exp2f(la.x * LOG2E); ex.y = __builtin_amdgcn_exp2f(la.y * LOG2E);
            ar = (-rbf2(ex) * dt) * LOG2E;
            ai = (bfup(pAI) * dt) * 0.15915494309189535f;
            cr = bfup(pCR) * dt;
            ci = bfup(pCI) * dt;
        }
        asm volatile("" :: "v"(code_pf));
        STAMP(1);
        const float bound = __builtin_fmaf(2.0f, wave_sum(fabsf(cr.x) + fabsf(cr.y) + fabsf(ci.x) + fabsf(ci.y)), fabsf(bflo(pDS)));
        int ke = 14 - ((int)((__float_as_uint(bound) >> 23) & 0xff) - 127);
        ke = __builtin_amdgcn_readfirstlane(min(max(ke, -100), 100));
        const float sig2 = __uint_as_float((u32)(127 + ke + 1) << 23);
        const float yscale = __uint_as_float((u32)(127 - ke) << 23) * (1.0f / (LOG2E * LOG2E));
        cplx G0; G0.r = cr * sig2; G0.i = -ci * sig2;
        float skip = (lane == 0) ? 0.5f * sig2 * bflo(pDS) : 0.0f;
        PIN(skip);
        typedef __attribute__((address_space(3))) u16* lptr16;
        auto scatter_y = [&](u32 xl, u32 y01, u32 y3) __attribute__((always_inline)) {
            *(lptr32)(uintptr_t)sc_2 = y01;
            *(lptr16)(uintptr_t)sc_l = (u16)y3; *(lptr16)(uintptr_t)sc_h = (u16)(y3 >> 16);
            const u32 v0 = __builtin_amdgcn_readlane(y01, 63), v1 = __builtin_amdgcn_readlane(y3, 0), v2 = __builtin_amdgcn_readlane(xl, 63);
            *(lptr16)(uintptr_t)sc_p = (u16)((pcls == 0) ? v0 : (pcls == 1) ? v1 : v2);
            WAVE_SYNC();
        };
        const rsrc_t krs = mk_rsrc(ka0[4], ka0[5], WS_K, SD * 2048);
        u32 ktag = 0;
        if (KX) { lds_wait(cnt + 0, 1); ktag = __builtin_amdgcn_readfirstlane(cnt[28]); }
        if (!gen) {
            const u32 vk = d * 2048 + lane * 16;
            __builtin_amdgcn_s_sleep(K_PRE);
            lds_wait<4>(cnt + 20, 4);
            __builtin_amdgcn_s_sleep(K_DELAY);
            STAMP(8);
            u4 kv[2];
            poll_pieces<2, G_SLEEP>(kv, [&](int i) { return BLD128(krs, vk + 1024 * i, 0, 16); }, [&](u4 v_) { return v_.w != ktag; }, [&]() {});
            STAMP(2);
            put4(ch, sc_x + 128, kv[0].x, kv[0].y);
            put4(ch, sc_x, kv[0].z, kv[1].x);
            scatter_y(kv[0].z, kv[1].y, kv[1].z);
            STAMP(3);
        } else {
            typedef __attribute__((address_space(3))) const u4* lq;
            const cplx E1 = cexp2(ar, ai, 1.0f), E2 = csq(E1), E4 = csq(E2), E8 = csq(E4);
            const hcx z1 = toh(E1), z2 = toh(E2), z4 = toh(E4), z8 = toh(E8);
            {
                const cplx E16 = csq(E8), E32 = csq(E16), E64 = csq(E32);
                hcx c[8];
                c[1] = z8; c[2] = toh(E16); c[4] = toh(E32);
                const hcx p64 = toh(E64), p128 = toh(csq(E64));
                c[3] = hmul(c[1], c[2]);
                stg_put<1>(c[1]); stg_put<2>(c[2]); stg_put<4>(c[4]); stg_put<8>(p64);
                c[5] = hmul(c[1], c[4]); c[6] = hmul(c[2], c[4]); c[7] = hmul(c[3], c[4]);
                stg_put<16>(p128); stg_put<3>(c[3]);
                const hcx t9 = hmul(c[1], p64), t10 = hmul(c[2], p64);
                stg_put<5>(c[5]); stg_put<6>(c[6]);
                const hcx t11 = hmul(c[3], p64), t12 = hmul(c[4], p64);
                stg_put<7>(c[7]); stg_put<9>(t9);
                const hcx t13 = hmul(c[5], p64), t14 = hmul(c[6], p64);
                stg_put<10>(t10); stg_put<11>(t11);
                const hcx t15 = hmul(c[7], p64);
                stg_put<12>(t12); stg_put<13>(t13); stg_put<14>(t14); stg_put<15>(t15);
            }
            WAVE_SYNC();
            u4 B0[8], B1[8], Af[8];
#pragma unroll
            for (int s = 0; s < 8; ++s) B0[s] = *(lq)(uintptr_t)(fr_b + s * 64);
#pragma unroll
            for (int s = 0; s < 8; ++s) B1[s] = *(lq)(uintptr_t)(fr_z + s * 64);
            WAVE_SYNC();
            STAMP(8);
            if (!KX && (KG_SW & 1) && cw < 4) __builtin_amdgcn_s_setprio(1);
            hcx J[4];
            {
                const cplx F1 = cexp2(ar, ai, 256.0f), F2 = csq(F1);
                const hcx f1 = toh(F1), f2_ = toh(F2), f4_ = toh(csq(F2));
                const hcx g0 = toh(G0), g1 = hmul_conj(g0, z1), g2 = hmul_conj(g0, z2), g4 = hmul_conj(g0, z4), g8 = hmul_conj(g0, z8);
                stg_put<0>(g0); stg_put<1>(g1);
                const hcx g3 = hmul_conj(g1, z2), g5 = hmul_conj(g1, z4);
                stg_put<2>(g2); stg_put<4>(g4);
                const hcx g6 = hmul_conj(g2, z4), g7 = hmul_conj(g3, z4);
                stg_put<3>(g3); stg_put<5>(g5);
                const hcx h0 = hmul_conj(g0, f1), h2_ = hmul_conj(g2, f1);
                stg_put<6>(g6); stg_put<7>(g7);
                const hcx h4 = hmul_conj(g4, f1), h6 = hmul_conj(g6, f1);
                stg_put<8>(h0); stg_put<9>(h2_);
                const hcx j1 = hmul_conj(g8, f1), k2 = hmul_conj(g4, f2_);
                stg_put<10>(h4); stg_put<11>(h6);
                const hcx j2 = hmul_conj(g8, f2_), k3 = hmul_conj(h4, f2_);
                stg_put<12>(j1); stg_put<13>(k2);
                const hcx j3 = hmul_conj(j1, f2_);
                J[0] = hmul_conj(g8, f4_);
                stg_put<14>(j2); stg_put<15>(k3);
                J[1] = hmul_conj(j1, f4_); J[2] = hmul_conj(j2, f4_);
                stg_put<16>(j3);
                J[3] = hmul_conj(j3, f4_);
            }
            WAVE_SYNC();
#pragma unroll
            for (int s = 0; s < 8; ++s) Af[s] = *(lq)(uintptr_t)(fr_x + s * 64);
            WAVE_SYNC();
            stg_put<0>(J[0]); stg_put<1>(J[1]); stg_put<2>(J[2]); stg_put<3>(J[3]);
            STAMP(9);
            if (!KX && KG_SW == 2 && cw < 4) __builtin_amdgcn_s_setprio(1);
            if (!KX && KG_SW == 3 && cw >= 4) __builtin_amdgcn_s_setprio(1);
            f4 X0 = {0, 0, 0, 0}, X1 = {0, 0, 0, 0}, Y0 = {0, 0, 0, 0}, Y1 = {0, 0, 0, 0};
            X0[0] = skip;
            auto mf = [](u4 a, u4 b, f4 c) { return mfma16(__builtin_bit_cast(h8, a), __builtin_bit_cast(h8, b), c); };
#pragma unroll
            for (int s = 0; s < 4; ++s) {
#pragma unroll
                for (int i = 0; i < 4; ++i) {
                    u32 zr = B1[s][i], zi = B1[s + 4][i], t;
                    asm volatile("v_pk_mul_f16 %2, %4, %1\n\tv_pk_mul_f16 %1, %3, %1\n\tv_pk_fma_f16 %1, %4, %0, %1\n\tv_pk_fma_f16 %0, %3, %0, %2 neg_lo:[0,0,1] neg_hi:[0,0,1]"
                                 : "+v"(zr), "+v"(zi), "=&v"(t) : "v"(B0[s][i]), "v"(B0[s + 4][i]));
                    B1[s][i] = zr; B1[s + 4][i] = zi;
                }
                X0 = mf(Af[2 * s], B0[2 * s], X0); X0 = mf(Af[2 * s + 1], B0[2 * s + 1], X0);
                asm volatile("" :: "v"(X0));
                SCHED_FENCE();
            }
            STAMP(10);
            WAVE_SYNC();
#pragma unroll
            for (int s = 0; s < 8; ++s) { X1 = mf(Af[s], B1[s], X1); asm volatile("" :: "v"(X1) : "memory"); Af[s] = *(lq)(uintptr_t)(fr_y + s * 64); SCHED_FENCE(); }
            u4 k0, k1;
            k0.x = pkh(X0[3], X0[2]); k0.y = pkh(X0[1], X0[0]);
            put4(ch, sc_x + 128, k0.x, k0.y);
            STAMP(11);
#pragma unroll
            for (int s = 0; s < 8; ++s) { Y0 = mf(Af[s], B0[s], Y0); Y1 = mf(Af[s], B1[s], Y1); }
            k0.z = pkh(X1[3], X1[2]); k1.x = pkh(X1[1], X1[0]);
            put4(ch, sc_x, k0.z, k1.x);
            STAMP(2);
            k1.y = __builtin_amdgcn_permlane32_swap(pkh(Y0[1], Y0[0]), pkh(Y1[1], Y1[0]), false, false)[0];
            k1.z = pkh(Y0[2], Y1[2]);
            if (KX) {
                u32 ln;
                asm volatile("v_mbcnt_lo_u32_b32 %0, -1, 0\n\tv_mbcnt_hi_u32_b32 %0, -1, %0" : "=v"(ln));
                const u32 vk = d * 2048 + ln * 16;
                k0.w = k1.w = ktag;
                __builtin_amdgcn_raw_buffer_store_b128(k0, krs, vk, 0, 16);
                __builtin_amdgcn_raw_buffer_store_b128(k1, krs, vk + 1024, 0, 16);
                MEM_FENCE();
                lds_add1(cnt + 20);
            }
            scatter_y(k0.z, k1.y, k1.z);
            STAMP(3);
        }
        __builtin_amdgcn_s_setprio(CV_P0);
        lds_wait(cnt + 22, 4);
        STAMP(5);
        conv(hf, sm0, cw, yscale);
        STAMP(7);

    }
}

extern "C" void kernel_launch(void* const* d_in, const int* in_sizes, int n_in,
                              void* d_out, int out_size, void* d_ws, size_t ws_size,
                              hipStream_t stream) {
    char* p = (char*)d_ws + ((WS_OFF < 0 ? ws_size + (long long)WS_OFF * 1048576ll : (size_t)WS_OFF * 1048576ull) & ~(size_t)255);
    static_assert(WS_OFF >= 0 || WS_END <= (size_t)(-WS_OFF) * 1048576, "workspace");
    k_fused<<<256, 1024, 0, stream>>>((const u16*)d_in[0], (const u16*)d_in[8], p, (u16*)d_out,
                                      (const u16*)d_in[1], (const u16*)d_in[2], (const u16*)d_in[3], (const u16*)d_in[4],
                                      (const u16*)d_in[5], (const u16*)d_in[6], (const u16*)d_in[9], (const u16*)d_in[10], (const u16*)d_in[11]);
}
```

```cpp
#include <hip/hip_runtime.h>
#include <stdint.h>
#include <type_traits>

typedef uint16_t u16;
typedef uint32_t u32;
typedef _Float16 f16;
typedef __attribute__((ext_vector_type(8))) _Float16 h8;
typedef __attribute__((ext_vector_type(2))) _Float16 h2;
typedef __attribute__((ext_vector_type(2))) __bf16 b2;
typedef __attribute__((ext_vector_type(8))) __bf16 b8;
typedef __attribute__((ext_vector_type(4))) float f4;
typedef __attribute__((ext_vector_type(2))) float f2;
typedef __attribute__((ext_vector_type(4))) u32 u4;
typedef __attribute__((ext_vector_type(2))) u32 u2;

constexpr int SL = 2048, SD = 1024, SN = 128, SF = 3584;
constexpr int GU_WORDS = 128 * 2 * 4 * 2 * 8 * 4;
constexpr int DN_WORDS = 32 * 8 * 64 * 4;
#define SPIN_LOG 20
constexpr int SPIN_MAX = 1 << SPIN_LOG;

#define DEV __device__ __forceinline__
#define STAMP_DECL
#define STAMP(k)
#define STAMP_FLUSH(kn)
#define STAMP_ARG
#define STAMP_PAR
#define WAVE_SYNC() do { __builtin_amdgcn_fence(__ATOMIC_ACQ_REL, "wavefront"); __builtin_amdgcn_wave_barrier(); } while (0)
#define PIN(v) asm volatile("" : "+v"(v))
#define SCHED_FENCE() __builtin_amdgcn_sched_barrier(0)
#define MEM_FENCE() asm volatile("" ::: "memory")

DEV float bflo(u32 v) { return __uint_as_float(v << 16); }
DEV float bfhi(u32 v) { return __uint_as_float(v & 0xffff0000u); }
DEV f2 bfup(u32 v) { f2 r; r.x = bflo(v); r.y = bfhi(v); return r; }
DEV u32 pkbf(f2 v) { b2 t = __builtin_convertvector(v, b2); return __builtin_bit_cast(u32, t); }
DEV u32 pkbf(float a, float b) { f2 v; v.x = a; v.y = b; return pkbf(v); }
DEV u32 pkh(f2 v) { h2 t = __builtin_convertvector(v, h2); return __builtin_bit_cast(u32, t); }
DEV u32 pkh(float a, float b) { f2 v; v.x = a; v.y = b; return pkh(v); }
DEV u32 pkh_r(float a, float b) { PIN(a); return pkh(a, b); }
DEV float rbf(float a) { return __uint_as_float(pkbf(0.0f, a)); }
DEV f2 rbf2(f2 a) { f2 r; r.x = rbf(a.x); r.y = rbf(a.y); return r; }
#define KNOB(name, value) constexpr int name = value
KNOB(NREP, 16);
KNOB(WS_OFF, -10);
KNOB(CODE_KB, 24);
KNOB(W_DELAY, 0);
KNOB(W_A, 4);
KNOB(W_B, 2);
KNOB(W_E, 6);
KNOB(DN_T, 0);
KNOB(KG_SW, 3);
KNOB(E_GP, 0);
KNOB(WB_AT, -1);
KNOB(WB_W, 8);
KNOB(W_F, 0);
KNOB(U_N, 1);
KNOB(GU_L, 1);
KNOB(WD_DELAY, 0);
KNOB(B_GAP, 0);
KNOB(KX, 0);
KNOB(K_PRE, 40);
KNOB(K_DELAY, 16);
KNOB(WD_1, 7);
KNOB(CP_S, 2);
KNOB(CP_0, 2);
KNOB(CP_1, 1);
KNOB(CP_2, 1);
KNOB(E_ORD, 1);
KNOB(E_EP, 3);
KNOB(P_LIGHT, 3);
KNOB(US_AUX, 16);
KNOB(W_AUX, 2);
KNOB(W_GATE, 0);
KNOB(CV_P0, 2);
KNOB(CV_P1, 1);
KNOB(W_PACE, 6);
KNOB(W_PACE2, 4);
KNOB(D_PACE, 0);
KNOB(U_AUX, 16);
KNOB(A_DELAY, 12);
KNOB(U_DELAY, 22);
KNOB(U_POS, 0);
KNOB(WD_A, 0);
KNOB(WD_B, 0);
KNOB(WD_T, 2);
KNOB(WD_G, 5);
KNOB(G_DELAY, 16);
KNOB(G_SLEEP, 2);
KNOB(A_SLEEP, 4);
KNOB(B_DELAY, 20);
KNOB(B_SLEEP, 2);
KNOB(PACE, 3);
KNOB(XT_PRE, 2);
KNOB(XT_MID, 2);
KNOB(E_UP, 2);
KNOB(E_SLP, 1);
KNOB(F_SLP, 2);
KNOB(CV_SW, -1);
KNOB(KMAP, 1);
KNOB(X_NT, 0);
KNOB(E_FIRST, 0);
KNOB(BOX_POS, 0);
KNOB(X_SLEEP, 2);
KNOB(E_PRIO, 2);
constexpr float LOG2E = 1.4426950408889634f;
DEV float silu_fast(float a) { return a * __builtin_amdgcn_rcpf(1.0f + __builtin_amdgcn_exp2f(-1.4426950408889634f * a)); }

template <int CTRL, int RMASK>
DEV float dpp_add(float v) {
    int t = __builtin_amdgcn_update_dpp(0, __float_as_int(v), CTRL, RMASK, 0xf, false);
    return v + __int_as_float(t);
}
DEV float wave_sum(float v) {
    v = dpp_add<0x111, 0xf>(v);
    v = dpp_add<0x112, 0xf>(v);
    v = dpp_add<0x114, 0xf>(v);
    v = dpp_add<0x118, 0xf>(v);
    v = dpp_add<0x142, 0xa>(v);
    v = dpp_add<0x143, 0xc>(v);
    return __int_as_float(__builtin_amdgcn_readlane(__float_as_int(v), 63));
}
DEV float fold4(float a, float b, float c, float d) {
    const auto s0 = __builtin_amdgcn_permlane32_swap(__float_as_uint(a), __float_as_uint(b), false, false);
    const auto s1 = __builtin_amdgcn_permlane32_swap(__float_as_uint(c), __float_as_uint(d), false, false);
    const u32 w0 = __float_as_uint(__uint_as_float(s0[0]) + __uint_as_float(s0[1]));
    const u32 w1 = __float_as_uint(__uint_as_float(s1[0]) + __uint_as_float(s1[1]));
    const auto s2 = __builtin_amdgcn_permlane16_swap(w0, w1, false, false);
    return __uint_as_float(s2[0]) + __uint_as_float(s2[1]);
}
DEV float row_sum(float v) {
    v = dpp_add<0x128, 0xf>(v);
    v = dpp_add<0x124, 0xf>(v);
    v = dpp_add<0x122, 0xf>(v);
    return dpp_add<0x121, 0xf>(v);
}
#define TOP2(ctl) "v_max_u32_dpp %2, %1, %1 " ctl "\n\tv_min_u32_dpp %3, %0, %0 " ctl "\n\tv_max_u32_dpp %0, %0, %0 " ctl "\n\tv_max_u32 %1, %2, %3\n\ts_nop 1\n\t"
DEV void wave_top2(u32& m1, u32& m2) {
    u32 t, s;
    asm volatile("s_nop 1\n\t"
                 TOP2("row_shr:1 row_mask:0xf bank_mask:0xf bound_ctrl:1") TOP2("row_shr:2 row_mask:0xf bank_mask:0xf bound_ctrl:1")
                 TOP2("row_shr:4 row_mask:0xf bank_mask:0xf bound_ctrl:1") TOP2("row_shr:8 row_mask:0xf bank_mask:0xf bound_ctrl:1")
                 TOP2("row_bcast:15 row_mask:0xa bank_mask:0xf") TOP2("row_bcast:31 row_mask:0xc bank_mask:0xf")
                 : "+v"(m1), "+v"(m2), "=&v"(t), "=&v"(s));
}
DEV void top2_of8(u32& m1, u32& m2) {
    u32 t, s;
    asm volatile("s_nop 1\n\t"
                 TOP2("row_shr:1 row_mask:0xf bank_mask:0xf bound_ctrl:1") TOP2("row_shr:2 row_mask:0xf bank_mask:0xf bound_ctrl:1")
                 TOP2("row_shr:4 row_mask:0xf bank_mask:0xf bound_ctrl:1")
                 : "+v"(m1), "+v"(m2), "=&v"(t), "=&v"(s));
}
DEV u32 med3u(u32 a, u32 b, u32 c) { return max(min(a, b), min(max(a, b), c)); }
DEV float dot2(u32 a, u32 b, float c) { return __builtin_amdgcn_fdot2_f32_bf16(__builtin_bit_cast(b2, a), __builtin_bit_cast(b2, b), c, false); }

template <int CTRL> DEV float dpp_mov(float v) { return __int_as_float(__builtin_amdgcn_mov_dpp(__float_as_int(v), CTRL, 0xf, 0xf, true)); }
DEV u32 bfi(u32 m, u32 a, u32 b) { u32 r; asm("v_bfi_b32 %0, %1, %2, %3" : "=v"(r) : "v"(m), "v"(a), "v"(b)); return r; }
DEV void gate4(u32 (&g)[4], u32 u0, u32 u1, u32 u2, u32 u3) {
#define LO "dst_sel:WORD_0 dst_unused:UNUSED_PRESERVE src0_sel:WORD_0\n\t"
#define HI "dst_sel:WORD_1 dst_unused:UNUSED_PRESERVE src0_sel:WORD_1\n\t"
    asm volatile("v_exp_f16_sdwa %0, -%4 " LO "v_exp_f16_sdwa %1, -%5 " LO "v_exp_f16_sdwa %2, -%6 " LO "v_exp_f16_sdwa %3, -%7 " LO
                 "v_exp_f16_sdwa %0, -%4 " HI "v_exp_f16_sdwa %1, -%5 " HI "v_exp_f16_sdwa %2, -%6 " HI "v_exp_f16_sdwa %3, -%7 " HI
                 "v_pk_add_f16 %0, %0, 1.0 op_sel_hi:[1,0]\n\tv_pk_add_f16 %1, %1, 1.0 op_sel_hi:[1,0]\n\tv_pk_add_f16 %2, %2, 1.0 op_sel_hi:[1,0]\n\tv_pk_add_f16 %3, %3, 1.0 op_sel_hi:[1,0]\n\t"
                 "v_rcp_f16_sdwa %0, %0 " LO "v_rcp_f16_sdwa %1, %1 " LO "v_rcp_f16_sdwa %2, %2 " LO "v_rcp_f16_sdwa %3, %3 " LO
                 "v_rcp_f16_sdwa %0, %0 " HI "v_rcp_f16_sdwa %1, %1 " HI "v_rcp_f16_sdwa %2, %2 " HI "v_rcp_f16_sdwa %3, %3 " HI
                 "v_pk_mul_f16 %0, %0, %4\n\tv_pk_mul_f16 %1, %1, %5\n\tv_pk_mul_f16 %2, %2, %6\n\tv_pk_mul_f16 %3, %3, %7"
                 : "=&v"(g[0]), "=&v"(g[1]), "=&v"(g[2]), "=&v"(g[3]) : "v"(u0), "v"(u1), "v"(u2), "v"(u3));
#undef LO
#undef HI
}
struct cplx { f2 r, i; };
DEV cplx cmul(cplx a, cplx b) { cplx z; z.r = a.r * b.r - a.i * b.i; z.i = a.r * b.i + a.i * b.r; return z; }
DEV cplx cmul_conj(cplx g, cplx e) { cplx z; z.r = g.r * e.r + g.i * e.i; z.i = g.i * e.r - g.r * e.i; return z; }
DEV cplx cexp2(f2 ar, f2 ai, float p) {
    cplx z;
    const f2 am = ar * p, rv = ai * p;
    f2 mag, c, s;
#pragma unroll
    for (int k = 0; k < 2; ++k) {
        mag[k] = __builtin_amdgcn_exp2f(am[k]);
        const float rev = __builtin_amdgcn_fractf(rv[k]);
        c[k] = __builtin_amdgcn_cosf(rev);
        s[k] = __builtin_amdgcn_sinf(rev);
    }
    z.r = mag * c; z.i = mag * s;
    return z;
}
DEV float exp_acc(float x) {
    x = __builtin_amdgcn_fmed3f(x, -150.0f, 150.0f);
    const float t = x * 1.4426950408889634f;
    const float r = __builtin_fmaf(x, 1.925963033500011e-08f, __builtin_fmaf(x, 1.4426950408889634f, -t));
    return __builtin_amdgcn_exp2f(t) * __builtin_fmaf(r, 0.6931471805599453f, 1.0f);
}
DEV h8 lds_h8(const char* p) { return *reinterpret_cast<const h8*>(p); }
DEV f4 mfma16(h8 a, h8 b, f4 c) { return __builtin_amdgcn_mfma_f32_16x16x32_f16(a, b, c, 0, 0, 0); }
DEV f4 mfma16b(u4 a, u4 b, f4 c) {
    return __builtin_amdgcn_mfma_f32_16x16x32_bf16(__builtin_bit_cast(b8, a), __builtin_bit_cast(b8, b), c, 0, 0, 0);
}
typedef __attribute__((ext_vector_type(16))) __bf16 b16v;
DEV f4 smfma(u4 a, u4 b0, u4 b1, f4 c, u32 idx) {
    return __builtin_amdgcn_smfmac_f32_16x16x64_bf16(__builtin_bit_cast(b8, a), __builtin_bit_cast(b16v, __builtin_shufflevector(b0, b1, 0, 1, 2, 3, 4, 5, 6, 7)), c, (int)idx, 0, 0);
}
DEV void lds_add1(u32* p) {
    const u32 a = (u32)(uintptr_t)(__attribute__((address_space(3))) u32*)p, one = 1u;
    asm volatile("s_mov_b64 exec, 1\n\tds_add_u32 %0, %1\n\ts_mov_b64 exec, -1" :: "v"(a), "v"(one) : "memory");
}
template <int SLP = 1>
DEV void lds_wait(u32* cnt, u32 n) {
    while (__hip_atomic_load(cnt, __ATOMIC_RELAXED, __HIP_MEMORY_SCOPE_WORKGROUP) < n) __builtin_amdgcn_s_sleep(SLP);
    __builtin_amdgcn_fence(__ATOMIC_ACQUIRE, "wavefront");
    __builtin_amdgcn_wave_barrier();
}

constexpr int KREC = 1024;
constexpr int R_KA = 0, ZA = 543, R_K2 = 560, R_K3 = 812, R_KP = 972;
constexpr int STG_ROW = 544;
constexpr int STG_BUF = 37 * 256;
constexpr int REC_B = KREC * 2 + 64;
constexpr int REC_SZ = 2 * KREC * 2 + 128;
constexpr int UPAD = 256;
constexpr int C_U = REC_SZ;
constexpr int CH_SZ = C_U + (UPAD + SL) * 2 + 16;
static_assert(CH_SZ % 32 == 16, "bank offset of the channel regions");
constexpr int L_STG = 0;
constexpr int L_CH = L_STG + 8 * STG_BUF;
constexpr int L_BOX = L_CH + 8 * CH_SZ;
constexpr int XN_S = 512 + 64, IMG = 4 * XN_S;
constexpr int L_Z = L_BOX + 5 * 1024;
constexpr int L_XN = L_Z, Z_SZ = 4 * IMG;
constexpr int L_CNT = L_Z + Z_SZ;
constexpr int L_P12 = L_CNT + 128, P12_S = 80;
constexpr int L_P3 = L_P12 + 8 * P12_S, P3_S = 48;
constexpr int L_NUL = L_P3 + 8 * P3_S;
constexpr int L_TOTAL = L_NUL + 256;
static_assert(L_TOTAL <= 160 * 1024 && Z_SZ % 16 == 0, "LDS");

DEV h8 box_frag(int al, int q, int off, int hw) {
    const u32 m = (u32)((((2ull << (2 * hw)) - 1ull) << (al - 8 * q + off - hw + 32)) >> 32) & 0xffu;
    const u32 t = m | (m << 15);
    u4 r;
#pragma unroll
    for (int i = 0; i < 4; ++i) r[i] = ((t >> (2 * i)) & 0x10001u) * 0x3c00u;
    return __builtin_bit_cast(h8, r);
}
DEV u4 tag4(f4 v, u32 vtag) {
    u4 b = __builtin_bit_cast(u4, v);
#pragma unroll
    for (int i = 0; i < 4; ++i) b[i] = __builtin_amdgcn_perm(b[i], vtag, 0x07060500u + i);
    return b;
}
DEV f4 fold_halves(f4 a0, f4 a1) {
    f4 o;
#pragma unroll
    for (int i = 0; i < 4; ++i) {
        const f4 a = (i < 2) ? a0 : a1;
        float t = a[i & 1] + __int_as_float(__builtin_amdgcn_update_dpp(0, __float_as_int(a[2 + (i & 1)]), 0x128, 0xf, 0xf, true));
        PIN(t);
        o[i] = t;
    }
    return o;
}
template <int O0, int ST>
DEV void lds_wait_read4(u32 a_cnt, u32 n, u32 a, u4 (&v)[4]) {
    for (;;) {
        u32 c;
        asm volatile("ds_read_b32 %0, %5\n\tds_read_b128 %1, %6 offset:%7\n\tds_read_b128 %2, %6 offset:%8\n\tds_read_b128 %3, %6 offset:%9\n\t"
                     "ds_read_b128 %4, %6 offset:%10\n\ts_waitcnt lgkmcnt(0)"
                     : "=&v"(c), "=&v"(v[0]), "=&v"(v[1]), "=&v"(v[2]), "=&v"(v[3])
                     : "v"(a_cnt), "v"(a), "n"(O0), "n"(O0 + ST), "n"(O0 + 2 * ST), "n"(O0 + 3 * ST) : "memory");
        if (__builtin_amdgcn_readfirstlane(c) >= n) break;
        __builtin_amdgcn_s_sleep(1);
    }
}
DEV u32 tagof(u4 v) { return __builtin_amdgcn_perm(v.y, v.x, 0x0c0c0400u) | __builtin_amdgcn_perm(v.w, v.z, 0x04000c0cu); }
DEV u4 untag4(u4 v) { return v & 0xffffff00u; }
template <int N, int SLEEP, class LD, class BAD, class HOOK>
DEV void poll_pieces(u4 (&v)[N], LD ld, BAD bad, HOOK hook) {
#pragma unroll
    for (int p = 0; p < N; ++p) v[p] = ld(p);
    MEM_FENCE(); SCHED_FENCE();
    hook();
    MEM_FENCE(); SCHED_FENCE();
    {
        bool any = false;
#pragma unroll
        for (int p = 0; p < N; ++p) any |= bad(v[p]);
        if (__builtin_expect(!__any(any), 1)) return;
    }
    u32 miss = 0;
#pragma unroll
    for (int p = 0; p < N; ++p) if (!__all(!bad(v[p]))) miss |= 1u << p;
#pragma unroll 1
    for (int spin = 0; miss != 0 && spin < SPIN_MAX; ++spin) {
        MEM_FENCE();
        __builtin_amdgcn_s_sleep(SLEEP);
#pragma unroll
        for (int p = 0; p < N; ++p) if ((miss >> p) & 1u) v[p] = ld(p);
#pragma unroll
        for (int p = 0; p < N; ++p) if (((miss >> p) & 1u) && __all(!bad(v[p]))) miss &= ~(1u << p);
        __builtin_amdgcn_s_waitcnt(0x0f70);
    }
}
struct hcx { h2 r, i; };
DEV hcx toh(cplx v) { hcx z; z.r = __builtin_convertvector(v.r, h2); z.i = __builtin_convertvector(v.i, h2); return z; }
DEV hcx hmul(hcx a, hcx b) { hcx z; z.r = __builtin_elementwise_fma(a.r, b.r, -(a.i * b.i)); z.i = __builtin_elementwise_fma(a.r, b.i, a.i * b.r); return z; }
DEV hcx hmul_conj(hcx g, hcx e) { hcx z; z.r = __builtin_elementwise_fma(g.r, e.r, g.i * e.i); z.i = __builtin_elementwise_fma(g.i, e.r, -(g.r * e.i)); return z; }
DEV cplx csq(cplx a) { cplx z; z.r = a.r * a.r - a.i * a.i; z.i = (a.r + a.r) * a.i; return z; }
template <int ROW>
DEV void stg_put(hcx v) {
    asm volatile("ds_write_addtid_b32 %0 offset:%2\n\tds_write_addtid_b32 %1 offset:%3"
                 :: "v"(v.r), "v"(v.i), "n"(ROW * STG_ROW), "n"(ROW * STG_ROW + 256) : "memory");
}
#define ST2(a, b, o) "ds_write_addtid_b32 %" #a " offset:%8+" #o "*544\n\tds_write_addtid_b32 %" #b " offset:%8+" #o "*544+256\n\t"
template <int ROW>
DEV void stg_put4(const hcx (&v)[4]) {
    static_assert(STG_ROW == 544, "row stride");
    asm volatile(ST2(0, 1, 0) ST2(2, 3, 1) ST2(4, 5, 2) ST2(6, 7, 3)
                 :: "v"(v[0].r), "v"(v[0].i), "v"(v[1].r), "v"(v[1].i), "v"(v[2].r), "v"(v[2].i), "v"(v[3].r), "v"(v[3].i), "n"(ROW * STG_ROW) : "memory");
}
template <int R0, int N, class F, class G> DEV void stg_rows(F f, G between) {
    hcx v[2][4];
#pragma unroll
    for (int i = 0; i < 4; ++i) v[0][i] = f(i);
    if constexpr (N > 1) {
#pragma unroll
        for (int i = 0; i < 4; ++i) { v[1][i] = f(4 + i); PIN(v[1][i].r); PIN(v[1][i].i); }
    }
    stg_put4<R0>(v[0]); between(0);
    if constexpr (N > 2) {
#pragma unroll
        for (int i = 0; i < 4; ++i) { v[0][i] = f(8 + i); PIN(v[0][i].r); PIN(v[0][i].i); }
    }
    if constexpr (N > 1) { stg_put4<R0 + 4>(v[1]); between(1); }
    if constexpr (N > 3) {
#pragma unroll
        for (int i = 0; i < 4; ++i) { v[1][i] = f(12 + i); PIN(v[1][i].r); PIN(v[1][i].i); }
    }
    if constexpr (N > 2) { stg_put4<R0 + 8>(v[0]); between(2); }
    if constexpr (N > 3) { stg_put4<R0 + 12>(v[1]); between(3); }
}

typedef __attribute__((address_space(3))) const char* lptr;
DEV void lds_add1a(u32 a) {
    const u32 one = 1u;
    asm volatile("s_mov_b64 exec, 1\n\tds_add_u32 %0, %1\n\ts_mov_b64 exec, -1" :: "v"(a), "v"(one) : "memory");
}
#define CONV_ASM_0 \
    "v_add_u32 %7, -64, %0\n\tds_read2_b32 v[8:9], %0 offset0:128 offset1:129\n\tds_read2_b32 v[10:11], %0 offset0:130 offset1:131\n\tds_read2_b32 v[12:13], %0 offset0:0 offset1:1\n\t" \
    "ds_read2_b32 v[14:15], %0 offset0:2 offset1:3\n\tds_read2_b32 v[16:17], %0 offset0:144 offset1:145\n\tds_read2_b32 v[18:19], %0 offset0:146 offset1:147\n\t" \
    "ds_read2_b32 v[20:21], %0 offset0:16 offset1:17\n\tds_read2_b32 v[22:23], %0 offset0:18 offset1:19\n\tds_read2_b32 v[24:25], %0 offset0:160 offset1:161\n\t" \
    "ds_read2_b32 v[26:27], %0 offset0:162 offset1:163\n\tds_read2_b32 v[28:29], %0 offset0:32 offset1:33\n\tds_read2_b32 v[30:31], %0 offset0:34 offset1:35\n\t" \
    "ds_read2_b32 v[32:33], %0 offset0:176 offset1:177\n\tds_read2_b32 v[34:35], %0 offset0:178 offset1:179\n\tds_read2_b32 v[36:37], %0 offset0:48 offset1:49\n\t" \
    "ds_read2_b32 v[38:39], %0 offset0:50 offset1:51\n\tds_read2_b32 v[40:41], %0 offset0:192 offset1:193\n\tds_read2_b32 v[42:43], %0 offset0:194 offset1:195\n\t" \
    "ds_read2_b32 v[44:45], %0 offset0:64 offset1:65\n\tds_read2_b32 v[46:47], %0 offset0:66 offset1:67\n\tds_read2_b32 v[48:49], %0 offset0:208 offset1:209\n\t" \
    "ds_read2_b32 v[50:51], %0 offset0:210 offset1:211\n\tds_read2_b32 v[52:53], %0 offset0:80 offset1:81\n\tds_read2_b32 v[54:55], %0 offset0:82 offset1:83\n\t" \
    "ds_read2_b32 v[56:57], %0 offset0:224 offset1:225\n\tds_read2_b32 v[58:59], %0 offset0:226 offset1:227\n\tds_read2_b32 v[60:61], %0 offset0:96 offset1:97\n\t" \
    "ds_read2_b32 v[62:63], %0 offset0:98 offset1:99\n\tds_read2_b32 v[64:65], %0 offset0:240 offset1:241\n\tds_read2_b32 v[66:67], %0 offset0:242 offset1:243\n\t" \
    "ds_read2_b32 v[68:69], %0 offset0:112 offset1:113\n\tds_read2_b32 v[70:71], %0 offset0:114 offset1:115\n\tds_read2_b32 v[72:73], %7 offset0:0 offset1:1\n\t" \
    "ds_read2_b32 v[74:75], %7 offset0:2 offset1:3\n\t" \
    CONV_WAIT \
    "ds_read_b128 v[76:79], %1 offset:32\n\tds_read_b128 v[80:83], %1 offset:96\n\tds_read_b128 v[84:87], %1 offset:160\n\tds_read_b128 v[88:91], %1 offset:224\n\t" \
    "s_waitcnt lgkmcnt(3)\n\tv_mfma_f32_16x16x32_f16 v[92:95], v[8:11], v[76:79], 0\n\tds_read_b128 v[96:99], %1 offset:288\n\tv_mfma_f32_16x16x32_f16 v[100:103], v[12:15], v[76:79], 0\n\t" \
    "s_waitcnt lgkmcnt(3)\n\tv_mfma_f32_16x16x32_f16 v[92:95], v[16:19], v[80:83], v[92:95]\n\tds_read_b128 v[104:107], %1 offset:352\n\tv_mfma_f32_16x16x32_f16 v[100:103], v[20:23], v[80:83], v[100:103]\n\t" \
    "s_waitcnt lgkmcnt(2)\n\tv_mfma_f32_16x16x32_f16 v[92:95], v[24:27], v[84:87], v[92:95]\n\tds_read_b128 v[108:111], %1 offset:416\n\tv_mfma_f32_16x16x32_f16 v[100:103], v[28:31], v[84:87], v[100:103]\n\t" \
    "v_mfma_f32_16x16x32_f16 v[92:95], v[32:35], v[88:91], v[92:95]\n\tds_read_b128 v[112:115], %1 offset:480\n\tv_mfma_f32_16x16x32_f16 v[100:103], v[36:39], v[88:91], v[100:103]\n\t" \
    "s_waitcnt lgkmcnt(2)\n\tv_mfma_f32_16x16x32_f16 v[92:95], v[40:43], v[96:99], v[92:95]\n\tds_read_b128 v[116:119], %1 offset:544\n\tv_mfma_f32_16x16x32_f16 v[100:103], v[44:47], v[96:99], v[100:103]\n\t" \
    "ds_read_b128 v[120:123], %1 offset:608\n\tv_mfma_f32_16x16x32_f16 v[92:95], v[48:51], v[104:107], v[92:95]\n\tds_read_b128 v[124:127], %1 offset:2528\n\t" \
    "v_mfma_f32_16x16x32_f16 v[100:103], v[52:55], v[104:107], v[100:103]\n\tds_read_b128 v[76:79], %1 offset:672\n\ts_waitcnt lgkmcnt(4)\n\tv_mfma_f32_16x16x32_f16 v[92:95], v[56:59], v[108:111], v[92:95]\n\t" \
    "ds_read_b128 v[80:83], %1 offset:2592\n\tv_mfma_f32_16x16x32_f16 v[100:103], v[60:63], v[108:111], v[100:103]\n\tds_read_b128 v[84:87], %1 offset:736\n\t" \
    "v_mfma_f32_16x16x32_f16 v[92:95], v[64:67], v[112:115], v[92:95]\n\tds_read_b128 v[88:91], %1 offset:2656\n\tv_mfma_f32_16x16x32_f16 v[100:103], v[68:71], v[112:115], v[100:103]\n\t" \
    "ds_read_b128 v[96:99], %1 offset:800\n\tds_read_b128 v[104:107], %1 offset:2784\n\ts_waitcnt lgkmcnt(5)\n\tv_mfma_f32_16x16x32_f16 v[100:103], v[8:11], v[116:119], v[100:103]\n\t" \
    "ds_read_b128 v[108:111], %1 offset:2720\n\tds_read_b128 v[112:115], %1 offset:864\n\tv_mfma_f32_16x16x32_f16 v[100:103], v[16:19], v[120:123], v[100:103]\n\t" \
    "v_mul_f32 v92, %8, v92\n\tv_mul_f32 v93, %8, v93\n\tv_mfma_f32_16x16x32_f16 v[116:119], v[72:75], v[124:127], 0\n\tv_mul_f32 v94, %8, v94\n\tv_mul_f32 v95, %8, v95\n\t" \
    "v_mfma_f32_16x16x32_f16 v[100:103], v[24:27], v[76:79], v[100:103]\n\tds_write_b128 %2, v[92:95] offset:0\n\tds_write_b8 %6, %6 offset:0\n\t" \
    CONV_P0 \
    "ds_read_b128 v[120:123], %1 offset:928\n\ts_waitcnt lgkmcnt(6)\n\tv_mfma_f32_16x16x32_f16 v[116:119], v[12:15], v[80:83], v[116:119]\n\tds_read_b128 v[124:127], %1 offset:2848\n\t" \
    "v_mfma_f32_16x16x32_f16 v[100:103], v[32:35], v[84:87], v[100:103]\n\tds_read_b128 v[76:79], %1 offset:992\n\tv_mfma_f32_16x16x32_f16 v[116:119], v[20:23], v[88:91], v[116:119]\n\t" \
    "ds_read_b128 v[80:83], %1 offset:2912\n\tv_mfma_f32_16x16x32_f16 v[100:103], v[40:43], v[96:99], v[100:103]\n\tds_read_b128 v[92:95], %1 offset:2976\n\t" \
    "s_waitcnt lgkmcnt(4)\n\tv_mfma_f32_16x16x32_f16 v[116:119], v[28:31], v[108:111], v[116:119]\n\tds_read_b128 v[84:87], %1 offset:3040\n\tv_mfma_f32_16x16x32_f16 v[100:103], v[48:51], v[112:115], v[100:103]\n\t" \
    "v_mfma_f32_16x16x32_f16 v[116:119], v[36:39], v[104:107], v[116:119]\n\tds_read_b128 v[88:91], %1 offset:3104\n\tv_mfma_f32_16x16x32_f16 v[100:103], v[56:59], v[120:123], v[100:103]\n\t" \
    "s_waitcnt lgkmcnt(2)\n\tv_mfma_f32_16x16x32_f16 v[116:119], v[44:47], v[124:127], v[116:119]\n\tds_read_b128 v[96:99], %1 offset:3168\n\tv_mfma_f32_16x16x32_f16 v[100:103], v[64:67], v[76:79], v[100:103]\n\t" \
    "v_mfma_f32_16x16x32_f16 v[116:119], v[52:55], v[80:83], v[116:119]\n\tds_read_b128 v[108:111], %1 offset:3232\n\tv_mfma_f32_16x16x32_f16 v[116:119], v[60:63], v[92:95], v[116:119]\n\t" \
    "ds_read_b128 v[104:107], %1 offset:3296\n\tds_read_b128 v[112:115], %1 offset:3360\n\ts_waitcnt lgkmcnt(4)\n\tv_mfma_f32_16x16x32_f16 v[116:119], v[68:71], v[84:87], v[116:119]\n\t" \
    "s_nop 0\n\tv_mul_f32 v100, %8, v100\n\tv_mul_f32 v101, %8, v101\n\tv_mfma_f32_16x16x32_f16 v[120:123], v[72:75], v[84:87], 0\n\tv_mul_f32 v102, %8, v102\n\t" \
    "v_mul_f32 v103, %8, v103\n\tv_mfma_f32_16x16x32_f16 v[116:119], v[8:11], v[88:91], v[116:119]\n\tds_write_b128 %2, v[100:103] offset:1024\n\tds_write_b8 %6, %6 offset:8\n\t" \
    CONV_P1 \
    "v_mfma_f32_16x16x32_f16 v[120:123], v[12:15], v[88:91], v[120:123]\n\ts_waitcnt lgkmcnt(4)\n\tv_mfma_f32_16x16x32_f16 v[116:119], v[16:19], v[96:99], v[116:119]\n\t" \
    "ds_read_b128 v[124:127], %1 offset:3424\n\tv_mfma_f32_16x16x32_f16 v[120:123], v[20:23], v[96:99], v[120:123]\n\tv_mfma_f32_16x16x32_f16 v[116:119], v[24:27], v[108:111], v[116:119]\n\t" \
    "ds_read_b128 v[76:79], %1 offset:3488\n\tv_mfma_f32_16x16x32_f16 v[120:123], v[28:31], v[108:111], v[120:123]\n\ts_waitcnt lgkmcnt(4)\n\tv_mfma_f32_16x16x32_f16 v[116:119], v[32:35], v[104:107], v[116:119]\n\t" \
    "ds_read_b128 v[80:83], %1 offset:3552\n\tv_mfma_f32_16x16x32_f16 v[120:123], v[36:39], v[104:107], v[120:123]\n\tv_mfma_f32_16x16x32_f16 v[116:119], v[40:43], v[112:115], v[116:119]\n\t" \
    "ds_read_b128 v[92:95], %1 offset:3616\n\tv_mfma_f32_16x16x32_f16 v[120:123], v[44:47], v[112:115], v[120:123]\n\tds_read_b128 v[12:15], %1 offset:3680\n\t" \
    "s_waitcnt lgkmcnt(3)\n\tv_mfma_f32_16x16x32_f16 v[116:119], v[48:51], v[124:127], v[116:119]\n\tds_read_b128 v[72:75], %1 offset:3744\n\tv_mfma_f32_16x16x32_f16 v[120:123], v[52:55], v[124:127], v[120:123]\n\t" \
    "ds_read_b128 v[100:103], %1 offset:3808\n\tv_mfma_f32_16x16x32_f16 v[116:119], v[56:59], v[76:79], v[116:119]\n\tds_read_b128 v[84:87], %1 offset:464\n\t" \
    "ds_read_b128 v[88:91], %3 offset:1024\n\tv_mfma_f32_16x16x32_f16 v[120:123], v[60:63], v[76:79], v[120:123]\n\tds_read_b128 v[20:23], %1 offset:480\n\t" \
    "ds_read_b128 v[96:99], %3 offset:4096\n\ts_waitcnt lgkmcnt(6)\n\tv_mfma_f32_16x16x32_f16 v[116:119], v[64:67], v[80:83], v[116:119]\n\tds_read_b128 v[28:31], %1 offset:3872\n\t" \
    "v_mfma_f32_16x16x32_f16 v[120:123], v[68:71], v[80:83], v[120:123]\n\tds_read_b128 v[108:111], %1 offset:976\n\tv_mfma_f32_16x16x32_f16 v[120:123], v[8:11], v[92:95], v[120:123]\n\t" \
    "ds_read_b128 v[36:39], %1 offset:992\n\tv_mfma_f32_16x16x32_f16 v[120:123], v[16:19], v[12:15], v[120:123]\n\tds_read_b128 v[104:107], %1 offset:3936\n\t" \
    "s_waitcnt lgkmcnt(4)\n\tv_mfma_f32_16x16x32_f16 v[120:123], v[24:27], v[72:75], v[120:123]\n\tds_read_b128 v[44:47], %1 offset:1488\n\tv_mfma_f32_16x16x32_f16 v[120:123], v[32:35], v[100:103], v[120:123]\n\t" \
    "ds_read_b128 v[112:115], %1 offset:1504\n\tv_mfma_f32_16x16x32_f16 v[52:55], v[84:87], v[88:91], 0\n\tds_read_b128 v[124:127], %1 offset:4000\n\tv_mfma_f32_16x16x32_f16 v[60:63], v[20:23], v[96:99], 0\n\t" \
    "ds_read_b128 v[76:79], %3 offset:0\n\ts_waitcnt lgkmcnt(4)\n\tv_mfma_f32_16x16x32_f16 v[120:123], v[40:43], v[28:31], v[120:123]\n\tds_read_b128 v[68:71], %3 offset:2048\n\t" \
    "v_mfma_f32_16x16x32_f16 v[80:83], v[108:111], v[88:91], 0\n\tds_read_b128 v[8:11], %3 offset:3072\n\tds_read_b128 v[92:95], %1 offset:4064\n\tv_mfma_f32_16x16x32_f16 v[16:19], v[36:39], v[96:99], 0\n\t" \
    "v_cvt_pk_f16_f32 v12, v52, v60\n\tv_cvt_pk_f16_f32 v13, v53, v61\n\tv_mfma_f32_16x16x32_f16 v[120:123], v[48:51], v[104:107], v[120:123]\n\tv_cvt_pk_f16_f32 v14, v54, v62\n\t" \
    "v_cvt_pk_f16_f32 v15, v55, v63\n\ts_waitcnt lgkmcnt(3)\n\tv_mfma_f32_16x16x32_f16 v[24:27], v[44:47], v[88:91], 0\n\tds_read2_b32 v[72:73], %5 offset0:16 offset1:17\n\t" \
    "ds_read2_b32 v[74:75], %5 offset0:18 offset1:19\n\tv_mfma_f32_16x16x32_f16 v[32:35], v[112:115], v[96:99], 0\n\tv_cvt_pk_f16_f32 v100, v80, v16\n\tv_cvt_pk_f16_f32 v101, v81, v17\n\t" \
    "ds_read2_b32 v[84:85], %5 offset0:0 offset1:1\n\tds_read2_b32 v[86:87], %5 offset0:2 offset1:3\n\tv_mfma_f32_16x16x32_f16 v[120:123], v[56:59], v[124:127], v[120:123]\n\t" \
    "v_cvt_pk_f16_f32 v102, v82, v18\n\tv_cvt_pk_f16_f32 v103, v83, v19\n\tv_add_u32 %7, 1880, %0\n\tds_read2_b32 v[20:21], %7 offset0:0 offset1:1\n\tds_read2_b32 v[22:23], %7 offset0:2 offset1:3\n\t" \
    "v_mfma_f32_16x16x32_f16 v[40:43], v[44:47], v[76:79], 0\n\tds_read_b128 v[28:31], %1 offset:2000\n\tds_read_b128 v[108:111], %1 offset:2016\n\ts_waitcnt lgkmcnt(8)\n\t" \
    "v_mfma_f32_16x16x32_f16 v[36:39], v[112:115], v[68:71], 0\n\tv_cvt_pk_f16_f32 v48, v24, v32\n\tv_cvt_pk_f16_f32 v49, v25, v33\n\tv_mfma_f32_16x16x32_f16 v[104:107], v[112:115], v[76:79], 0\n\t" \
    "v_cvt_pk_f16_f32 v50, v26, v34\n\tv_cvt_pk_f16_f32 v51, v27, v35\n\tv_mfma_f32_16x16x32_f16 v[52:55], v[112:115], v[8:11], 0\n\tds_read2_b32 v[60:61], %5 offset0:32 offset1:33\n\t" \
    "ds_read2_b32 v[62:63], %5 offset0:34 offset1:35\n\tv_mfma_f32_16x16x32_f16 v[120:123], v[64:67], v[92:95], v[120:123]\n\tv_cvt_pk_f16_f32 v56, v40, v36\n\t" \
    "v_cvt_pk_f16_f32 v58, v41, v37\n\ts_waitcnt lgkmcnt(3)\n\tv_mfma_f32_16x16x32_f16 v[116:119], v[12:15], v[72:75], v[116:119]\n\tv_cvt_pk_f16_f32 v124, v42, v38\n\t" \
    "v_cvt_pk_f16_f32 v126, v43, v39\n\tv_mfma_f32_16x16x32_f16 v[120:123], v[12:15], v[84:87], v[120:123]\n\tv_cvt_pk_f16_f32 v57, v104, v52\n\tv_cvt_pk_f16_f32 v59, v105, v53\n\t" \
    "ds_read2_b32 v[80:81], %5 offset0:48 offset1:49\n\tds_read2_b32 v[82:83], %5 offset0:50 offset1:51\n\tv_mfma_f32_16x16x32_f16 v[116:119], v[20:23], v[112:115], v[116:119]\n\t" \
    "v_cvt_pk_f16_f32 v125, v106, v54\n\tv_cvt_pk_f16_f32 v127, v107, v55\n\tv_mfma_f32_16x16x32_f16 v[16:19], v[28:31], v[76:79], 0\n\tds_read_b128 v[44:47], %1 offset:2512\n\t" \
    "s_waitcnt lgkmcnt(3)\n\tv_mfma_f32_16x16x32_f16 v[24:27], v[108:111], v[68:71], 0\n\tds_read_b128 v[32:35], %1 offset:2528\n\tv_mfma_f32_16x16x32_f16 v[64:67], v[108:111], v[76:79], 0\n\t" \
    "v_mfma_f32_16x16x32_f16 v[92:95], v[108:111], v[8:11], 0\n\tds_read2_b64 v[84:87], %4 offset0:0 offset1:1\n\tds_read2_b64 v[40:43], %4 offset0:2 offset1:3\n\t" \
    "v_mfma_f32_16x16x32_f16 v[116:119], v[100:103], v[60:63], v[116:119]\n\ts_nop 1\n\tv_cvt_pk_f16_f32 v36, v16, v24\n\tv_cvt_pk_f16_f32 v38, v17, v25\n\t" \
    "v_mfma_f32_16x16x32_f16 v[120:123], v[100:103], v[72:75], v[120:123]\n\tv_cvt_pk_f16_f32 v112, v18, v26\n\tv_cvt_pk_f16_f32 v114, v19, v27\n\ts_waitcnt lgkmcnt(2)\n\t" \
    "v_mfma_f32_16x16x32_f16 v[116:119], v[48:51], v[80:83], v[116:119]\n\tv_cvt_pk_f16_f32 v37, v64, v92\n\tv_cvt_pk_f16_f32 v39, v65, v93\n\tv_mfma_f32_16x16x32_f16 v[120:123], v[48:51], v[60:63], v[120:123]\n\t" \
    "v_cvt_pk_f16_f32 v113, v66, v94\n\tv_cvt_pk_f16_f32 v115, v67, v95\n\tv_mfma_f32_16x16x32_f16 v[12:15], v[44:47], v[76:79], 0\n\tv_mfma_f32_16x16x32_f16 v[104:107], v[32:35], v[68:71], 0\n\t" \
    "ds_read2_b64 v[52:55], %4 offset0:16 offset1:17\n\tv_mfma_f32_16x16x32_f16 v[72:75], v[32:35], v[76:79], 0\n\tds_read2_b64 v[100:103], %4 offset0:18 offset1:19\n\t" \
    "v_mfma_f32_16x16x32_f16 v[60:63], v[32:35], v[8:11], 0\n\tds_read_b128 v[16:19], %1 offset:3024\n\tds_read_b128 v[24:27], %1 offset:3040\n\ts_waitcnt lgkmcnt(4)\n\t" \
    "v_mfma_f32_16x16x32_f16 v[116:119], v[56:59], v[84:87], v[116:119]\n\tv_cvt_pk_f16_f32 v64, v12, v104\n\tv_cvt_pk_f16_f32 v66, v13, v105\n\tv_mfma_f32_16x16x32_f16 v[116:119], v[124:127], v[40:43], v[116:119]\n\t" \
    "v_cvt_pk_f16_f32 v92, v14, v106\n\tv_cvt_pk_f16_f32 v94, v15, v107\n\tv_mfma_f32_16x16x32_f16 v[48:51], v[28:31], v[88:91], 0\n\tv_cvt_pk_f16_f32 v65, v72, v60\n\t" \
    "v_cvt_pk_f16_f32 v67, v73, v61\n\tv_mfma_f32_16x16x32_f16 v[44:47], v[108:111], v[96:99], 0\n\tv_cvt_pk_f16_f32 v93, v74, v62\n\tv_cvt_pk_f16_f32 v95, v75, v63\n\t" \
    "ds_read2_b64 v[32:35], %4 offset0:32 offset1:33\n\tv_mfma_f32_16x16x32_f16 v[120:123], v[20:23], v[108:111], v[120:123]\n\tds_read2_b64 v[56:59], %4 offset0:34 offset1:35\n\t" \
    "s_waitcnt lgkmcnt(2)\n\tv_mfma_f32_16x16x32_f16 v[116:119], v[36:39], v[52:55], v[116:119]\n\tv_mfma_f32_16x16x32_f16 v[116:119], v[112:115], v[100:103], v[116:119]\n\t" \
    "v_cvt_pk_f16_f32 v12, v48, v44\n\tv_cvt_pk_f16_f32 v13, v49, v45\n\tv_mfma_f32_16x16x32_f16 v[104:107], v[16:19], v[76:79], 0\n\tv_cvt_pk_f16_f32 v14, v50, v46\n\t" \
    "v_cvt_pk_f16_f32 v15, v51, v47\n\tv_mfma_f32_16x16x32_f16 v[88:91], v[24:27], v[68:71], 0\n\tv_mfma_f32_16x16x32_f16 v[28:31], v[24:27], v[76:79], 0\n\t" \
    "v_mfma_f32_16x16x32_f16 v[124:127], v[24:27], v[8:11], 0\n\ts_waitcnt lgkmcnt(0)\n\tv_mfma_f32_16x16x32_f16 v[116:119], v[64:67], v[32:35], v[116:119]\n\t" \
    "s_nop 3\n\tv_cvt_pk_f16_f32 v96, v104, v88\n\tv_cvt_pk_f16_f32 v98, v105, v89\n\tv_mfma_f32_16x16x32_f16 v[116:119], v[92:95], v[56:59], v[116:119]\n\t" \
    "v_cvt_pk_f16_f32 v20, v106, v90\n\tv_cvt_pk_f16_f32 v22, v107, v91\n\tv_mfma_f32_16x16x32_f16 v[120:123], v[36:39], v[84:87], v[120:123]\n\tv_cvt_pk_f16_f32 v97, v28, v124\n\t" \
    "v_cvt_pk_f16_f32 v99, v29, v125\n\tv_mfma_f32_16x16x32_f16 v[120:123], v[112:115], v[40:43], v[120:123]\n\tv_cvt_pk_f16_f32 v21, v30, v126\n\tv_cvt_pk_f16_f32 v23, v31, v127\n\t" \
    "v_mfma_f32_16x16x32_f16 v[120:123], v[12:15], v[80:83], v[120:123]\n\tv_mul_f32 v116, %8, v116\n\tv_mul_f32 v117, %8, v117\n\tv_mfma_f32_16x16x32_f16 v[120:123], v[64:67], v[52:55], v[120:123]\n\t" \
    "v_mul_f32 v118, %8, v118\n\tv_mul_f32 v119, %8, v119\n\tv_mfma_f32_16x16x32_f16 v[120:123], v[92:95], v[100:103], v[120:123]\n\tds_write_b128 %2, v[116:119] offset:2048\n\t" \
    "ds_write_b8 %6, %6 offset:16\n\t" \
    CONV_P2 \
    "v_mfma_f32_16x16x32_f16 v[120:123], v[96:99], v[32:35], v[120:123]\n\tv_mfma_f32_16x16x32_f16 v[120:123], v[20:23], v[56:59], v[120:123]\n\ts_nop 7\n\t" \
    "v_mul_f32 v120, %8, v120\n\tv_mul_f32 v121, %8, v121\n\tv_mul_f32 v122, %8, v122\n\tv_mul_f32 v123, %8, v123\n\tds_write_b128 %2, v[120:123] offset:3072\n\t" \
    "ds_write_b8 %6, %6 offset:24\n\t"
#define CONV_ASM_1 \
    "v_add_u32 %7, -64, %0\n\tds_read_b128 v[8:11], %3 offset:0\n\tds_read_b128 v[12:15], %3 offset:2048\n\tds_read_b128 v[16:19], %3 offset:3072\n\tds_read2_b64 v[24:27], %4 offset0:32 offset1:33\n\t" \
    "ds_read2_b64 v[36:39], %4 offset0:34 offset1:35\n\tds_read2_b64 v[28:31], %4 offset0:16 offset1:17\n\tds_read2_b64 v[40:43], %4 offset0:18 offset1:19\n\t" \
    "ds_read2_b64 v[32:35], %4 offset0:0 offset1:1\n\tds_read2_b64 v[44:47], %4 offset0:2 offset1:3\n\tds_read2_b32 v[20:21], %7 offset0:0 offset1:1\n\tds_read2_b32 v[22:23], %7 offset0:2 offset1:3\n\t" \
    "ds_read2_b32 v[48:49], %0 offset0:0 offset1:1\n\tds_read2_b32 v[50:51], %0 offset0:2 offset1:3\n\tds_read2_b32 v[52:53], %0 offset0:16 offset1:17\n\tds_read2_b32 v[54:55], %0 offset0:18 offset1:19\n\t" \
    "ds_read2_b32 v[56:57], %0 offset0:32 offset1:33\n\tds_read2_b32 v[58:59], %0 offset0:34 offset1:35\n\tds_read2_b32 v[60:61], %0 offset0:48 offset1:49\n\t" \
    "ds_read2_b32 v[62:63], %0 offset0:50 offset1:51\n\tds_read2_b32 v[64:65], %0 offset0:64 offset1:65\n\tds_read2_b32 v[66:67], %0 offset0:66 offset1:67\n\t" \
    "ds_read2_b32 v[68:69], %0 offset0:80 offset1:81\n\tds_read2_b32 v[70:71], %0 offset0:82 offset1:83\n\tds_read2_b32 v[72:73], %0 offset0:96 offset1:97\n\t" \
    "ds_read2_b32 v[74:75], %0 offset0:98 offset1:99\n\tds_read2_b32 v[76:77], %0 offset0:112 offset1:113\n\tds_read2_b32 v[78:79], %0 offset0:114 offset1:115\n\t" \
    "ds_read2_b32 v[80:81], %0 offset0:128 offset1:129\n\tds_read2_b32 v[82:83], %0 offset0:130 offset1:131\n\tds_read2_b32 v[84:85], %0 offset0:144 offset1:145\n\t" \
    "ds_read2_b32 v[86:87], %0 offset0:146 offset1:147\n\tds_read2_b32 v[88:89], %0 offset0:160 offset1:161\n\tds_read2_b32 v[90:91], %0 offset0:162 offset1:163\n\t" \
    CONV_WAIT \
    "ds_read_b128 v[92:95], %1 offset:464\n\tds_read_b128 v[96:99], %1 offset:480\n\tds_read_b128 v[100:103], %1 offset:976\n\tds_read_b128 v[104:107], %1 offset:992\n\t" \
    "s_waitcnt lgkmcnt(3)\n\tv_mfma_f32_16x16x32_f16 v[108:111], v[92:95], v[8:11], 0\n\ts_waitcnt lgkmcnt(2)\n\tv_mfma_f32_16x16x32_f16 v[112:115], v[96:99], v[12:15], 0\n\t" \
    "v_mfma_f32_16x16x32_f16 v[116:119], v[96:99], v[8:11], 0\n\tv_mfma_f32_16x16x32_f16 v[120:123], v[96:99], v[16:19], 0\n\ts_waitcnt lgkmcnt(0)\n\tv_mfma_f32_16x16x32_f16 v[124:127], v[100:103], v[8:11], 0\n\t" \
    "s_nop 3\n\tv_cvt_pk_f16_f32 v92, v108, v112\n\tv_cvt_pk_f16_f32 v94, v109, v113\n\tv_mfma_f32_16x16x32_f16 v[100:103], v[104:107], v[12:15], 0\n\tv_cvt_pk_f16_f32 v12, v110, v114\n\t" \
    "v_cvt_pk_f16_f32 v14, v111, v115\n\tv_mfma_f32_16x16x32_f16 v[108:111], v[104:107], v[8:11], 0\n\tv_cvt_pk_f16_f32 v93, v116, v120\n\tv_cvt_pk_f16_f32 v95, v117, v121\n\t" \
    "v_mfma_f32_16x16x32_f16 v[112:115], v[104:107], v[16:19], 0\n\tv_cvt_pk_f16_f32 v13, v118, v122\n\tv_cvt_pk_f16_f32 v15, v119, v123\n\tv_mfma_f32_16x16x32_f16 v[8:11], v[20:23], v[96:99], 0\n\t" \
    "v_cvt_pk_f16_f32 v16, v124, v100\n\tv_cvt_pk_f16_f32 v18, v125, v101\n\tv_mfma_f32_16x16x32_f16 v[116:119], v[20:23], v[104:107], 0\n\tv_cvt_pk_f16_f32 v120, v126, v102\n\t" \
    "v_cvt_pk_f16_f32 v122, v127, v103\n\tv_mfma_f32_16x16x32_f16 v[8:11], v[92:95], v[24:27], v[8:11]\n\tv_cvt_pk_f16_f32 v17, v108, v112\n\tv_cvt_pk_f16_f32 v19, v109, v113\n\t" \
    "v_mfma_f32_16x16x32_f16 v[116:119], v[92:95], v[28:31], v[116:119]\n\tv_cvt_pk_f16_f32 v121, v110, v114\n\tv_cvt_pk_f16_f32 v123, v111, v115\n\tv_mfma_f32_16x16x32_f16 v[96:99], v[92:95], v[32:35], 0\n\t" \
    "v_mfma_f32_16x16x32_f16 v[8:11], v[12:15], v[36:39], v[8:11]\n\tv_mfma_f32_16x16x32_f16 v[116:119], v[12:15], v[40:43], v[116:119]\n\tds_read_b128 v[104:107], %1 offset:544\n\t" \
    "v_mfma_f32_16x16x32_f16 v[96:99], v[12:15], v[44:47], v[96:99]\n\tds_read_b128 v[124:127], %1 offset:608\n\tv_mfma_f32_16x16x32_f16 v[116:119], v[16:19], v[24:27], v[116:119]\n\t" \
    "ds_read_b128 v[100:103], %1 offset:672\n\tv_mfma_f32_16x16x32_f16 v[96:99], v[16:19], v[28:31], v[96:99]\n\tds_read_b128 v[108:111], %1 offset:736\n\tv_mfma_f32_16x16x32_f16 v[112:115], v[16:19], v[32:35], 0\n\t" \
    "ds_read_b128 v[92:95], %1 offset:800\n\tv_mfma_f32_16x16x32_f16 v[116:119], v[120:123], v[36:39], v[116:119]\n\tds_read_b128 v[24:27], %1 offset:864\n\t" \
    "v_mfma_f32_16x16x32_f16 v[96:99], v[120:123], v[40:43], v[96:99]\n\tds_read_b128 v[12:15], %1 offset:928\n\tv_mfma_f32_16x16x32_f16 v[112:115], v[120:123], v[44:47], v[112:115]\n\t" \
    "ds_read_b128 v[28:31], %1 offset:992\n\ts_waitcnt lgkmcnt(4)\n\tv_mfma_f32_16x16x32_f16 v[8:11], v[48:51], v[104:107], v[8:11]\n\tds_read_b128 v[32:35], %1 offset:1056\n\t" \
    "v_mfma_f32_16x16x32_f16 v[8:11], v[52:55], v[124:127], v[8:11]\n\tv_mfma_f32_16x16x32_f16 v[8:11], v[56:59], v[100:103], v[8:11]\n\tds_read_b128 v[36:39], %1 offset:1120\n\t" \
    "v_mfma_f32_16x16x32_f16 v[8:11], v[60:63], v[108:111], v[8:11]\n\ts_waitcnt lgkmcnt(2)\n\tv_mfma_f32_16x16x32_f16 v[8:11], v[64:67], v[92:95], v[8:11]\n\t" \
    "ds_read_b128 v[16:19], %1 offset:1184\n\tv_mfma_f32_16x16x32_f16 v[8:11], v[68:71], v[24:27], v[8:11]\n\tds_read2_b32 v[40:41], %0 offset0:176 offset1:177\n\t" \
    "ds_read2_b32 v[42:43], %0 offset0:178 offset1:179\n\tv_mfma_f32_16x16x32_f16 v[8:11], v[72:75], v[12:15], v[8:11]\n\tds_read_b128 v[44:47], %1 offset:1248\n\t" \
    "v_mfma_f32_16x16x32_f16 v[8:11], v[76:79], v[28:31], v[8:11]\n\tds_read2_b32 v[120:121], %0 offset0:192 offset1:193\n\tds_read2_b32 v[122:123], %0 offset0:194 offset1:195\n\t" \
    "s_waitcnt lgkmcnt(6)\n\tv_mfma_f32_16x16x32_f16 v[8:11], v[80:83], v[32:35], v[8:11]\n\tds_read_b128 v[104:107], %1 offset:1312\n\tv_mfma_f32_16x16x32_f16 v[116:119], v[48:51], v[32:35], v[116:119]\n\t" \
    "ds_read2_b32 v[124:125], %0 offset0:208 offset1:209\n\tds_read2_b32 v[126:127], %0 offset0:210 offset1:211\n\tv_mfma_f32_16x16x32_f16 v[8:11], v[84:87], v[36:39], v[8:11]\n\t" \
    "ds_read_b128 v[100:103], %1 offset:1376\n\tv_mfma_f32_16x16x32_f16 v[116:119], v[52:55], v[36:39], v[116:119]\n\tds_read2_b32 v[108:109], %0 offset0:224 offset1:225\n\t" \
    "ds_read2_b32 v[110:111], %0 offset0:226 offset1:227\n\ts_waitcnt lgkmcnt(8)\n\tv_mfma_f32_16x16x32_f16 v[8:11], v[88:91], v[16:19], v[8:11]\n\tds_read_b128 v[92:95], %1 offset:1440\n\t" \
    "v_mfma_f32_16x16x32_f16 v[116:119], v[56:59], v[16:19], v[116:119]\n\tds_read2_b32 v[24:25], %0 offset0:240 offset1:241\n\tds_read2_b32 v[26:27], %0 offset0:242 offset1:243\n\t" \
    "v_mfma_f32_16x16x32_f16 v[8:11], v[40:43], v[44:47], v[8:11]\n\tds_read_b128 v[12:15], %1 offset:1504\n\tv_mfma_f32_16x16x32_f16 v[116:119], v[60:63], v[44:47], v[116:119]\n\t" \
    "s_waitcnt lgkmcnt(6)\n\tv_mfma_f32_16x16x32_f16 v[8:11], v[120:123], v[104:107], v[8:11]\n\tv_mfma_f32_16x16x32_f16 v[116:119], v[64:67], v[104:107], v[116:119]\n\t" \
    "ds_read_b128 v[28:31], %1 offset:1568\n\tv_mfma_f32_16x16x32_f16 v[8:11], v[124:127], v[100:103], v[8:11]\n\tv_mfma_f32_16x16x32_f16 v[116:119], v[68:71], v[100:103], v[116:119]\n\t" \
    "ds_read_b128 v[32:35], %1 offset:1632\n\ts_waitcnt lgkmcnt(2)\n\tv_mfma_f32_16x16x32_f16 v[8:11], v[108:111], v[92:95], v[8:11]\n\tv_mfma_f32_16x16x32_f16 v[116:119], v[72:75], v[92:95], v[116:119]\n\t" \
    "ds_read_b128 v[36:39], %1 offset:1696\n\tv_mfma_f32_16x16x32_f16 v[8:11], v[24:27], v[12:15], v[8:11]\n\tv_mfma_f32_16x16x32_f16 v[116:119], v[76:79], v[12:15], v[116:119]\n\t" \
    "ds_read_b128 v[16:19], %1 offset:1760\n\tv_mfma_f32_16x16x32_f16 v[96:99], v[20:23], v[12:15], v[96:99]\n\tds_read_b128 v[44:47], %1 offset:1824\n\tds_read_b128 v[104:107], %1 offset:1888\n\t" \
    "s_waitcnt lgkmcnt(4)\n\tv_mfma_f32_16x16x32_f16 v[116:119], v[80:83], v[28:31], v[116:119]\n\ts_nop 0\n\tv_mul_f32 v8, %8, v8\n\tv_mul_f32 v9, %8, v9\n\t" \
    "v_mfma_f32_16x16x32_f16 v[96:99], v[48:51], v[28:31], v[96:99]\n\tv_mul_f32 v10, %8, v10\n\tv_mul_f32 v11, %8, v11\n\tv_mfma_f32_16x16x32_f16 v[116:119], v[84:87], v[32:35], v[116:119]\n\t" \
    "ds_write_b128 %2, v[8:11] offset:0\n\tds_write_b8 %6, %6 offset:0\n\t" \
    CONV_P0 \
    "v_mfma_f32_16x16x32_f16 v[96:99], v[52:55], v[32:35], v[96:99]\n\ts_waitcnt lgkmcnt(4)\n\tv_mfma_f32_16x16x32_f16 v[116:119], v[88:91], v[36:39], v[116:119]\n\t" \
    "ds_read_b128 v[100:103], %1 offset:1952\n\tv_mfma_f32_16x16x32_f16 v[96:99], v[56:59], v[36:39], v[96:99]\n\tv_mfma_f32_16x16x32_f16 v[116:119], v[40:43], v[16:19], v[116:119]\n\t" \
    "ds_read_b128 v[92:95], %1 offset:2016\n\tv_mfma_f32_16x16x32_f16 v[96:99], v[60:63], v[16:19], v[96:99]\n\ts_waitcnt lgkmcnt(4)\n\tv_mfma_f32_16x16x32_f16 v[116:119], v[120:123], v[44:47], v[116:119]\n\t" \
    "v_mfma_f32_16x16x32_f16 v[96:99], v[64:67], v[44:47], v[96:99]\n\tds_read_b128 v[12:15], %1 offset:2080\n\tv_mfma_f32_16x16x32_f16 v[116:119], v[124:127], v[104:107], v[116:119]\n\t" \
    "v_mfma_f32_16x16x32_f16 v[96:99], v[68:71], v[104:107], v[96:99]\n\tds_read_b128 v[8:11], %1 offset:2144\n\ts_waitcnt lgkmcnt(2)\n\tv_mfma_f32_16x16x32_f16 v[116:119], v[108:111], v[100:103], v[116:119]\n\t" \
    "v_mfma_f32_16x16x32_f16 v[96:99], v[72:75], v[100:103], v[96:99]\n\tds_read_b128 v[28:31], %1 offset:2208\n\tv_mfma_f32_16x16x32_f16 v[116:119], v[24:27], v[92:95], v[116:119]\n\t" \
    "v_mfma_f32_16x16x32_f16 v[96:99], v[76:79], v[92:95], v[96:99]\n\tds_read_b128 v[32:35], %1 offset:2272\n\tv_mfma_f32_16x16x32_f16 v[112:115], v[20:23], v[92:95], v[112:115]\n\t" \
    "ds_read_b128 v[36:39], %1 offset:2336\n\tds_read_b128 v[16:19], %1 offset:2400\n\ts_waitcnt lgkmcnt(4)\n\tv_mfma_f32_16x16x32_f16 v[96:99], v[80:83], v[12:15], v[96:99]\n\t" \
    "s_nop 0\n\tv_mul_f32 v116, %8, v116\n\tv_mul_f32 v117, %8, v117\n\tv_mfma_f32_16x16x32_f16 v[112:115], v[48:51], v[12:15], v[112:115]\n\tv_mul_f32 v118, %8, v118\n\t" \
    "v_mul_f32 v119, %8, v119\n\tv_mfma_f32_16x16x32_f16 v[96:99], v[84:87], v[8:11], v[96:99]\n\tds_write_b128 %2, v[116:119] offset:1024\n\tds_write_b8 %6, %6 offset:8\n\t" \
    CONV_P1 \
    "v_mfma_f32_16x16x32_f16 v[112:115], v[52:55], v[8:11], v[112:115]\n\ts_waitcnt lgkmcnt(4)\n\tv_mfma_f32_16x16x32_f16 v[96:99], v[88:91], v[28:31], v[96:99]\n\t" \
    "ds_read_b128 v[44:47], %1 offset:2464\n\tv_mfma_f32_16x16x32_f16 v[112:115], v[56:59], v[28:31], v[112:115]\n\tv_mfma_f32_16x16x32_f16 v[96:99], v[40:43], v[32:35], v[96:99]\n\t" \
    "ds_read_b128 v[104:107], %1 offset:2528\n\tv_mfma_f32_16x16x32_f16 v[112:115], v[60:63], v[32:35], v[112:115]\n\ts_waitcnt lgkmcnt(4)\n\tv_mfma_f32_16x16x32_f16 v[96:99], v[120:123], v[36:39], v[96:99]\n\t" \
    "ds_read_b128 v[100:103], %1 offset:2592\n\tv_mfma_f32_16x16x32_f16 v[112:115], v[64:67], v[36:39], v[112:115]\n\tds_read_b128 v[20:23], %1 offset:2656\n\t" \
    "v_mfma_f32_16x16x32_f16 v[96:99], v[124:127], v[16:19], v[96:99]\n\tds_read_b128 v[92:95], %1 offset:2720\n\tv_mfma_f32_16x16x32_f16 v[112:115], v[68:71], v[16:19], v[112:115]\n\t" \
    "ds_read_b128 v[48:51], %1 offset:464\n\tds_read_b128 v[52:55], %3 offset:1024\n\ts_waitcnt lgkmcnt(5)\n\tv_mfma_f32_16x16x32_f16 v[96:99], v[108:111], v[44:47], v[96:99]\n\t" \
    "ds_read_b128 v[116:119], %1 offset:480\n\tds_read_b128 v[12:15], %3 offset:4096\n\tv_mfma_f32_16x16x32_f16 v[112:115], v[72:75], v[44:47], v[112:115]\n\t" \
    "ds_read_b128 v[8:11], %1 offset:2784\n\tv_mfma_f32_16x16x32_f16 v[96:99], v[24:27], v[104:107], v[96:99]\n\tds_read_b128 v[56:59], %1 offset:1488\n\tds_read_b128 v[28:31], %3 offset:0\n\t" \
    "v_mfma_f32_16x16x32_f16 v[112:115], v[76:79], v[104:107], v[112:115]\n\tds_read_b128 v[60:63], %1 offset:1504\n\tds_read_b128 v[32:35], %3 offset:2048\n\t" \
    "s_waitcnt lgkmcnt(7)\n\tv_mfma_f32_16x16x32_f16 v[112:115], v[80:83], v[100:103], v[112:115]\n\tv_mfma_f32_16x16x32_f16 v[112:115], v[84:87], v[20:23], v[112:115]\n\t" \
    "ds_read_b128 v[64:67], %3 offset:3072\n\tv_mfma_f32_16x16x32_f16 v[112:115], v[88:91], v[92:95], v[112:115]\n\tds_read_b128 v[36:39], %1 offset:2848\n\t" \
    "v_mfma_f32_16x16x32_f16 v[68:71], v[48:51], v[52:55], 0\n\tds_read_b128 v[16:19], %1 offset:976\n\ts_waitcnt lgkmcnt(3)\n\tv_mfma_f32_16x16x32_f16 v[72:75], v[116:119], v[12:15], 0\n\t" \
    "ds_read_b128 v[44:47], %1 offset:992\n\tv_mfma_f32_16x16x32_f16 v[112:115], v[40:43], v[8:11], v[112:115]\n\tv_add_u32 %7, 1880, %0\n\tds_read2_b32 v[76:77], %7 offset0:0 offset1:1\n\t" \
    "ds_read2_b32 v[78:79], %7 offset0:2 offset1:3\n\tv_mfma_f32_16x16x32_f16 v[104:107], v[56:59], v[28:31], 0\n\tds_read_b128 v[80:83], %1 offset:2000\n\t" \
    "ds_read_b128 v[84:87], %1 offset:2016\n\tv_mfma_f32_16x16x32_f16 v[100:103], v[60:63], v[32:35], 0\n\tv_cvt_pk_f16_f32 v20, v68, v72\n\tv_cvt_pk_f16_f32 v21, v69, v73\n\t" \
    "v_mfma_f32_16x16x32_f16 v[88:91], v[60:63], v[28:31], 0\n\tv_cvt_pk_f16_f32 v22, v70, v74\n\tv_cvt_pk_f16_f32 v23, v71, v75\n\tds_read2_b32 v[92:93], %5 offset0:48 offset1:49\n\t" \
    "ds_read2_b32 v[94:95], %5 offset0:50 offset1:51\n\ts_waitcnt lgkmcnt(6)\n\tv_mfma_f32_16x16x32_f16 v[48:51], v[60:63], v[64:67], 0\n\tds_read2_b64 v[40:43], %4 offset0:32 offset1:33\n\t" \
    "ds_read2_b64 v[8:11], %4 offset0:34 offset1:35\n\tv_mfma_f32_16x16x32_f16 v[112:115], v[120:123], v[36:39], v[112:115]\n\tv_cvt_pk_f16_f32 v56, v104, v100\n\t" \
    "v_cvt_pk_f16_f32 v58, v105, v101\n\tv_mfma_f32_16x16x32_f16 v[68:71], v[16:19], v[52:55], 0\n\tv_cvt_pk_f16_f32 v72, v106, v102\n\tv_cvt_pk_f16_f32 v74, v107, v103\n\t" \
    "v_mfma_f32_16x16x32_f16 v[60:63], v[44:47], v[12:15], 0\n\tv_cvt_pk_f16_f32 v57, v88, v48\n\tv_cvt_pk_f16_f32 v59, v89, v49\n\ts_waitcnt lgkmcnt(2)\n\t" \
    "v_mfma_f32_16x16x32_f16 v[96:99], v[76:79], v[116:119], v[96:99]\n\tv_cvt_pk_f16_f32 v73, v90, v50\n\tv_cvt_pk_f16_f32 v75, v91, v51\n\tds_read_b128 v[120:123], %1 offset:2912\n\t" \
    "v_mfma_f32_16x16x32_f16 v[36:39], v[80:83], v[28:31], 0\n\tds_read2_b32 v[52:53], %5 offset0:32 offset1:33\n\tds_read2_b32 v[54:55], %5 offset0:34 offset1:35\n\t" \
    "v_mfma_f32_16x16x32_f16 v[16:19], v[84:87], v[32:35], 0\n\tv_cvt_pk_f16_f32 v104, v68, v60\n\tv_cvt_pk_f16_f32 v105, v69, v61\n\tv_mfma_f32_16x16x32_f16 v[96:99], v[20:23], v[92:95], v[96:99]\n\t" \
    "v_cvt_pk_f16_f32 v106, v70, v62\n\tv_cvt_pk_f16_f32 v107, v71, v63\n\tds_read_b128 v[100:103], %1 offset:2976\n\tv_mfma_f32_16x16x32_f16 v[116:119], v[84:87], v[28:31], 0\n\t" \
    "ds_read2_b64 v[12:15], %4 offset0:16 offset1:17\n\tds_read2_b64 v[88:91], %4 offset0:18 offset1:19\n\tv_mfma_f32_16x16x32_f16 v[48:51], v[84:87], v[64:67], 0\n\t" \
    "v_cvt_pk_f16_f32 v80, v36, v16\n\tv_cvt_pk_f16_f32 v82, v37, v17\n\ts_waitcnt lgkmcnt(5)\n\tv_mfma_f32_16x16x32_f16 v[96:99], v[56:59], v[40:43], v[96:99]\n\t" \
    "v_cvt_pk_f16_f32 v32, v38, v18\n\tv_cvt_pk_f16_f32 v34, v39, v19\n\tv_mfma_f32_16x16x32_f16 v[96:99], v[72:75], v[8:11], v[96:99]\n\tds_read_b128 v[68:71], %1 offset:3040\n\t" \
    "v_mfma_f32_16x16x32_f16 v[112:115], v[124:127], v[120:123], v[112:115]\n\tv_cvt_pk_f16_f32 v81, v116, v48\n\tv_cvt_pk_f16_f32 v83, v117, v49\n\tv_mfma_f32_16x16x32_f16 v[112:115], v[76:79], v[44:47], v[112:115]\n\t" \
    "v_cvt_pk_f16_f32 v33, v118, v50\n\tv_cvt_pk_f16_f32 v35, v119, v51\n\ts_waitcnt lgkmcnt(1)\n\tv_mfma_f32_16x16x32_f16 v[112:115], v[20:23], v[52:55], v[112:115]\n\t" \
    "v_mul_f32 v96, %8, v96\n\tv_mul_f32 v97, %8, v97\n\tv_mfma_f32_16x16x32_f16 v[112:115], v[108:111], v[100:103], v[112:115]\n\tv_mul_f32 v98, %8, v98\n\t" \
    "v_mul_f32 v99, %8, v99\n\tv_mfma_f32_16x16x32_f16 v[112:115], v[56:59], v[12:15], v[112:115]\n\tds_write_b128 %2, v[96:99] offset:2048\n\tds_write_b8 %6, %6 offset:16\n\t" \
    CONV_P2 \
    "v_mfma_f32_16x16x32_f16 v[112:115], v[72:75], v[88:91], v[112:115]\n\tv_mfma_f32_16x16x32_f16 v[112:115], v[104:107], v[92:95], v[112:115]\n\ts_waitcnt lgkmcnt(2)\n\t" \
    "v_mfma_f32_16x16x32_f16 v[112:115], v[24:27], v[68:71], v[112:115]\n\tv_mfma_f32_16x16x32_f16 v[112:115], v[80:83], v[40:43], v[112:115]\n\tv_mfma_f32_16x16x32_f16 v[112:115], v[32:35], v[8:11], v[112:115]\n\t" \
    "s_nop 7\n\tv_mul_f32 v112, %8, v112\n\tv_mul_f32 v113, %8, v113\n\tv_mul_f32 v114, %8, v114\n\tv_mul_f32 v115, %8, v115\n\tds_write_b128 %2, v[112:115] offset:3072\n\t" \
    "ds_write_b8 %6, %6 offset:24\n\t"
#define CONV_SPIN(lab, next, off, slp) lab ":\n\tv_and_b32 %7, -8, %6\n\tds_read_b32 %7, %7 offset:" off "\n\ts_waitcnt lgkmcnt(0)\n\tv_readfirstlane_b32 %9, %7\n\t" \
    "s_cmp_ge_u32 %9, 4\n\ts_cbranch_scc1 " next "f\n\ts_sleep " slp "\n\ts_branch " lab "b\n"
#define CONV_WAIT "s_setprio 1\n" CONV_SPIN("3", "1", "36", "6") CONV_SPIN("1", "2", "32", "1") "2:\n\ts_setprio %11\n\t"
#define CONV_P0 "s_setprio %12\n\t"
#define CONV_P1 "s_setprio %13\n\t"
#define CONV_P2 "s_setprio %14\n\t"
#define CONV_CLOBBERS \
    "v8", "v9", "v10", "v11", "v12", "v13", "v14", "v15", "v16", "v17", "v18", "v19", "v20", "v21", "v22", "v23", "v24", "v25", "v26", "v27", "v28", "v29", "v30", "v31", \
    "v32", "v33", "v34", "v35", "v36", "v37", "v38", "v39", "v40", "v41", "v42", "v43", "v44", "v45", "v46", "v47", "v48", "v49", "v50", "v51", "v52", "v53", "v54", "v55", \
    "v56", "v57", "v58", "v59", "v60", "v61", "v62", "v63", "v64", "v65", "v66", "v67", "v68", "v69", "v70", "v71", "v72", "v73", "v74", "v75", "v76", "v77", "v78", "v79", \
    "v80", "v81", "v82", "v83", "v84", "v85", "v86", "v87", "v88", "v89", "v90", "v91", "v92", "v93", "v94", "v95", "v96", "v97", "v98", "v99", "v100", "v101", "v102", \
    "v103", "v104", "v105", "v106", "v107", "v108", "v109", "v110", "v111", "v112", "v113", "v114", "v115", "v116", "v117", "v118", "v119", "v120", "v121", "v122", "v123", \
    "v124", "v125", "v126", "v127"
DEV void conv(int hf, u32 sm0, int cw, float yscale) {
    int lane;
    asm volatile("v_mbcnt_lo_u32_b32 %0, -1, 0\n\tv_mbcnt_hi_u32_b32 %0, -1, %0" : "=v"(lane));
    __builtin_assume((unsigned)lane < 64u);
    const int al = lane & 15, q = lane >> 4;
    const u32 chb = sm0 + L_CH + cw * CH_SZ, kpar = chb + ((al & 1) ? 0 : REC_B + 2);
    u32 ka = kpar + (R_KA + ZA - al - 16 + 8 * q) * 2 - 960, ub0 = chb + C_U + (16 * al + 8 * q) * 2;
    u32 yb = sm0 + L_STG + cw * STG_BUF + 64 * al + 16 * (q ^ ((al >> 1) & 3));
    u32 bxp = sm0 + L_BOX + lane * 16, k2b = chb + (R_K2 + 192 - 4 * al - 4 + 16 * q - 128) * 2, k3b = chb + (R_K3 + 128 - 2 * al - 2 + 8 * q) * 2 - 192;
    u32 fl = sm0 + L_CNT + 8 + cw, t0, t1;
    u32 ys = __builtin_amdgcn_readfirstlane(__float_as_uint(yscale));
    static_assert(UPAD == 256 && (R_KP + 15 - (R_KA + ZA - 16)) * 2 + 960 == 1880 && L_CNT % 16 == 0, "offsets in the streams");
    asm volatile("s_cmp_eq_u32 %10, 0\n\ts_cbranch_scc1 8f\n\t" CONV_ASM_1 "s_branch 9f\n8:\n\t" CONV_ASM_0 "\n9:"
                 : "+v"(ka), "+v"(ub0), "+v"(yb), "+v"(bxp), "+v"(k2b), "+v"(k3b), "+v"(fl), "=&v"(t0), "+s"(ys), "=&s"(t1) : "s"(hf), "n"(CP_S), "n"(CP_0), "n"(CP_1), "n"(CP_2) : "memory", "scc", CONV_CLOBBERS);
}

constexpr int G_KEY = 8 * SL * 8, G_SZ = G_KEY + 256 * NREP * 16;
constexpr size_t WS_EPOCH = 0, WS_GRAN = 4096, WS_HAND = WS_GRAN + G_SZ, WS_U = WS_HAND + (size_t)(GU_WORDS + DN_WORDS) * 4,
                 WS_K = WS_U + (size_t)SL * SD * 2, WS_END = WS_K + (size_t)SD * 2048;
typedef __attribute__((ext_vector_type(8))) u32 u8v;
typedef __amdgpu_buffer_rsrc_t rsrc_t;
DEV rsrc_t mk_rsrc(u32 lo, u32 hi, size_t off, u32 bytes) {
    return __builtin_amdgcn_make_buffer_rsrc((__attribute__((address_space(1))) void*)((((unsigned long long)hi << 32) | lo) + off), 0, bytes, 0x00020000);
}
#define BLD128(rs, vo, so, aux) __builtin_amdgcn_raw_buffer_load_b128(rs, vo, so, aux)
DEV u32 lds_addr(const void* p) { return (u32)(uintptr_t)(__attribute__((address_space(3))) const void*)p; }

extern "C" __global__ __launch_bounds__(1024, 4) void k_fused(
    const u16* x_, const u16* wr_, char* ws_, u16* out_,
    const u16* logA_, const u16* Aim_, const u16* Cre_, const u16* Cim_,
    const u16* logdt_, const u16* dskip_, const u16* wg_, const u16* wu_, const u16* wd_) {
    __shared__ __attribute__((aligned(16))) char smem[L_TOTAL];
    const int tid = threadIdx.x, lane = tid & 63;
    const int wave = __builtin_amdgcn_readfirstlane(tid >> 6);
    const int xcd = blockIdx.x & 7, slot = blockIdx.x >> 3;
    const int g = xcd * 16 + (slot >> 1), hf = slot & 1;
    STAMP_DECL;
    STAMP(0);
    u32* cnt = reinterpret_cast<u32*>(smem + L_CNT);
    const u32 sm0 = lds_addr(smem);
    if (wave == 0) {
        if (lane < 32) cnt[lane] = 0;
        __builtin_amdgcn_s_waitcnt(0xc07f);
    }
    MEM_FENCE(); SCHED_FENCE();
    u32 kw0, kw1;
    asm volatile("s_load_dword %0, %2, 0x0\n\ts_load_dword %1, %2, 0x40" : "=&s"(kw0), "=&s"(kw1) : "s"(__builtin_amdgcn_kernarg_segment_ptr()) : "memory");
#define ARGS_LOAD() u8v ka0, ka1, ka2; u2 ka3; \
    asm volatile("s_load_dwordx8 %0, %4, 0x0\n\ts_load_dwordx8 %1, %4, 0x20\n\ts_load_dwordx8 %2, %4, 0x40\n\ts_load_dwordx2 %3, %4, 0x60" \
                 : "=&s"(ka0), "=&s"(ka1), "=&s"(ka2), "=&s"(ka3) : "s"(__builtin_amdgcn_kernarg_segment_ptr()) : "memory"); SCHED_FENCE()
#define ARGS_WAIT() do { SCHED_FENCE(); asm volatile("s_waitcnt lgkmcnt(0)" : "+s"(ka0), "+s"(ka1), "+s"(ka2), "+s"(ka3) : "s"(kw0), "s"(kw1) : "memory"); SCHED_FENCE(); } while (0)
#define LANE_IDS() int lane; asm volatile("v_mbcnt_lo_u32_b32 %0, -1, 0\n\tv_mbcnt_hi_u32_b32 %0, -1, %0" : "=v"(lane)); __builtin_assume((unsigned)lane < 64u); \
    const int tid = wave * 64 + lane, al = lane & 15, q = lane >> 4
#define XRS mk_rsrc(ka0[0], ka0[1], 0, SL * SD * 2)
#define GRS mk_rsrc(ka0[4], ka0[5], WS_GRAN, G_SZ)
#define URS mk_rsrc(ka0[4], ka0[5], WS_U, SL * SD * 2)
#define HRS mk_rsrc(ka0[4], ka0[5], WS_HAND, (GU_WORDS + DN_WORDS) * 4)
#define ORS mk_rsrc(ka0[6], ka0[7], 0, SL * SD * 2)
    u32 code_pf = 0;
    if (wave == 8 && slot * 2 < CODE_KB) {
        unsigned long long pc;
        asm volatile("s_getpc_b64 %0" : "=s"(pc));
        code_pf = *(const __attribute__((address_space(1))) u32*)((pc & ~255ull) + (unsigned)(slot * 2048 + lane * 32));
    }
    MEM_FENCE(); SCHED_FENCE();
    __builtin_amdgcn_s_barrier();
    MEM_FENCE(); SCHED_FENCE();
#define M0_ADD(n) "s_add_u32 m0, m0, " #n "\n\ts_nop 0\n\t"
    if (wave == 6) {
        unsigned long long pe;
        u32 ep;
        asm volatile("s_load_dwordx2 %0, %2, 0x10\n\ts_waitcnt lgkmcnt(0)\n\ts_load_dword %1, %0, %3\n\ts_waitcnt lgkmcnt(0)"
                     : "=&s"(pe), "=s"(ep) : "s"(__builtin_amdgcn_kernarg_segment_ptr()), "n"(WS_EPOCH) : "memory");
        const u32 tg = ep + 1u;
        STAMP(1);
        if (lane < 5) cnt[24 + lane] = lane == 4 ? tg : ((tg >> (6 * lane)) & 7u) | (((tg >> (6 * lane + 3)) & 7u) << 16);
        MEM_FENCE();
        lds_add1(cnt + 0);
    }
    MEM_FENCE(); SCHED_FENCE();
#define FIRST_BARRIER(bulk) do { MEM_FENCE(); SCHED_FENCE(); lds_add1(cnt + 17); MEM_FENCE(); SCHED_FENCE(); } while (0)
    const int b = xcd * 32 + slot;
    const unsigned long long all8 = 0x0706050403020100ull + ((sm0 + L_CNT + 8) & 0xffu) * 0x0101010101010101ull;
    typedef __attribute__((address_space(3))) u32* lptr32;
    typedef __attribute__((address_space(3))) u4* lptr128;
    auto stats = [&](const u4 (&v)[4], float& mb, float& den, float& rd) __attribute__((always_inline)) {
        f2 p[8];
#pragma unroll
        for (int i = 0; i < 8; ++i) { p[i].x = __uint_as_float(v[i >> 1][2 * (i & 1)]); p[i].y = __uint_as_float(v[i >> 1][2 * (i & 1) + 1]); }
        const f2 t = (((p[0] + p[1]) + (p[2] + p[3])) + ((p[4] + p[5]) + (p[6] + p[7]))) * (1.0f / SD);
        const float var = fmaxf(__builtin_fmaf(-t.x, t.x, t.y), 0.0f);
        mb = rbf(t.x);
        den = rbf(__builtin_amdgcn_sqrtf(rbf(rbf(var) + 1e-5f)));
        rd = __builtin_amdgcn_rcpf(den);
    };
    auto e_role = [&]() __attribute__((always_inline)) {
        ARGS_LOAD();
        __builtin_amdgcn_s_setprio(3);
        LANE_IDS();
        u32 vw = wave * 512 + (al >> 3) * 64 + q * 16;
        u32 vrow = (b * 8 + (lane >> 3)) * (SD * 2) + wave * 512 + (((lane & 7) ^ ((lane >> 3) & 2)) << 4);
        const u32 l_row = sm0 + L_CH + wave * CH_SZ + C_U + UPAD * 2;
        u32 a_x = l_row + (al & 7) * 128 + (((4 * (al >> 3) + q) ^ (al & 2)) << 4);
        PIN(vrow);
        u4 xa[4], wa[4];
        ARGS_WAIT();
#define XLD(nt) asm volatile("s_mov_b32 m0, %0\n\ts_nop 0\n\tglobal_load_lds_dwordx4 %1, %2" nt "\n\t" M0_ADD(896) "global_load_lds_dwordx4 %1, %2 offset:128" nt "\n\t" \
                     M0_ADD(896) "global_load_lds_dwordx4 %1, %2 offset:256" nt "\n\t" M0_ADD(896) "global_load_lds_dwordx4 %1, %2 offset:384" nt \
                     :: "s"(l_row), "v"(vrow), "s"(((unsigned long long)ka0[1] << 32) | ka0[0]) : "memory", "scc")
        if (X_NT) XLD(" nt"); else XLD("");
        MEM_FENCE(); SCHED_FENCE();
#pragma unroll
        for (int j = 0; j < 4; ++j) { wa[j] = u4{0x3f803f80u, 0x3f803f80u, 0x3f803f80u, 0x3f803f80u}; PIN(wa[j]); }
        PIN(vw); PIN(a_x);
        if (!(al & 4)) {
            const rsrc_t rrs = mk_rsrc(ka0[2], ka0[3], 0, SD * 2);
#pragma unroll
            for (int j = 0; j < 4; ++j) wa[j] = BLD128(rrs, vw + 128 * j, 0, 0);
        }
        FIRST_BARRIER(false);
        u4 sel[2];
#pragma unroll
        for (int h = 0; h < 2; ++h) {
#pragma unroll
            for (int i = 0; i < 4; ++i) sel[h][i] = (q == 2 * h + (al >> 3) && i == ((al & 7) >> 1)) ? (0x3f80u << (16 * (al & 1))) : 0u;
            PIN(sel[h]);
        }
        const int part = 2 * wave + (al >> 3);
        u32 a_p2 = sm0 + L_P12 + (al & 7) * P12_S + part * 8;
        u32 a_p1 = (q == 1) ? a_p2 : (q == (al >> 3) * 2) ? sm0 + L_P3 + (al & 7) * P3_S + part * 4 : sm0 + L_NUL + lane * 4;
        a_p2 = (q == (al >> 2)) ? a_p2 + 4 : sm0 + L_NUL + lane * 4;
        u32 a_pr = sm0 + L_P12 + (4 * (q & 1) + (al & 3)) * P12_S, a_cnt = sm0 + L_CNT;
        u32 vu[4], m7 = 0x00070007u;
#pragma unroll
        for (int j = 0; j < 4; ++j) { vu[j] = ((32 * wave + 8 * j + (lane >> 3)) * 256 + b) * 128 + (lane & 7) * 16; PIN(vu[j]); }
        u32 vp = g * 32768 + tid * 16, vs = (xcd * SL + tid) * 8;
        u32 a_u = sm0 + L_CH + (lane & 7) * CH_SZ + C_U + (UPAD + 64 * wave + 8 * (lane >> 3)) * 2;
        PIN(a_p1); PIN(a_p2); PIN(a_pr); PIN(a_cnt); PIN(m7); PIN(vp); PIN(vs); PIN(a_u);
        const rsrc_t urs = URS, grs = GRS;
        bool c1 = al & 1, c2_ = al & 2;
        lds_wait(cnt + 0, 1);
        const u32 tag = __builtin_amdgcn_readfirstlane(cnt[28]);
        u4 pat = *reinterpret_cast<const u4*>(cnt + 24);
        PIN(pat);
        MEM_FENCE(); SCHED_FENCE();
        {
            const f4 z4 = {0, 0, 0, 0};
            f4 d2 = z4, d1 = z4, xt[4][2];
            asm volatile("s_waitcnt vmcnt(4)\n\tds_read_b128 %0, %4\n\tds_read_b128 %1, %4 offset:1024\n\tds_read_b128 %2, %4 offset:2048\n\t"
                         "ds_read_b128 %3, %4 offset:3072\n\ts_waitcnt lgkmcnt(0)"
                         : "=&v"(xa[0]), "=&v"(xa[1]), "=&v"(xa[2]), "=&v"(xa[3]) : "v"(a_x) : "memory");
#pragma unroll
            for (int j = 0; j < 4; ++j) { d2 = mfma16b(xa[j], xa[j], d2); d1 = mfma16b(wa[j], xa[j], d1); }
            STAMP(1);
#pragma unroll
            for (int j = 0; j < XT_PRE; ++j) xt[j >> 1][j & 1] = mfma16b(xa[j >> 1], sel[j & 1], z4);
            SCHED_FENCE();
            const float e0 = c1 ? d2[1] : d2[0], e1 = c1 ? d2[3] : d2[2];
            *(lptr32)(uintptr_t)a_p2 = __float_as_uint(c2_ ? e1 : e0);
            *(lptr32)(uintptr_t)a_p1 = __float_as_uint(d1[0]);
            MEM_FENCE();
            lds_add1a(a_cnt + 4);
            __builtin_amdgcn_s_setprio(E_PRIO);
            SCHED_FENCE();
#pragma unroll
            for (int j = XT_PRE; j < XT_MID; ++j) xt[j >> 1][j & 1] = mfma16b(xa[j >> 1], sel[j & 1], z4);
#pragma unroll
            for (int j = 0; j < XT_MID; ++j) asm volatile("" :: "v"(xt[j >> 1][j & 1]));
            SCHED_FENCE();
            u4 pv[4];
            for (;;) {
                u32 c;
                asm volatile("ds_read_b32 %0, %5 offset:4\n\tds_read_b128 %1, %6\n\tds_read_b128 %2, %6 offset:16\n\tds_read_b128 %3, %6 offset:32\n\t"
                             "ds_read_b128 %4, %6 offset:48\n\ts_waitcnt lgkmcnt(0)"
                             : "=&v"(c), "=&v"(pv[0]), "=&v"(pv[1]), "=&v"(pv[2]), "=&v"(pv[3]) : "v"(a_cnt), "v"(a_pr) : "memory");
                if (__builtin_amdgcn_readfirstlane(c) >= 4u) break;
            }
            STAMP(2);
#pragma unroll
            for (int j = XT_MID; j < 8; ++j) xt[j >> 1][j & 1] = mfma16b(xa[j >> 1], sel[j & 1], z4);
            float mb, den, rd;
            stats(pv, mb, den, rd);
            const float a = LOG2E * rd, c = -mb * a;
            f2 a2[2], c2[2];
            a2[0].x = dpp_mov<0x00>(a); a2[0].y = dpp_mov<0x55>(a); a2[1].x = dpp_mov<0xaa>(a); a2[1].y = dpp_mov<0xff>(a);
            c2[0].x = dpp_mov<0x00>(c); c2[0].y = dpp_mov<0x55>(c); c2[1].x = dpp_mov<0xaa>(c); c2[1].y = dpp_mov<0xff>(c);
            STAMP(3);
#pragma unroll
            for (int jj = 0; jj < 4; jj += 2) {
                f2 v[8];
                u32 p[8];
#pragma unroll
                for (int i = 0; i < 8; ++i) {
                    const f4 t = xt[jj + (i >> 2)][(i >> 1) & 1];
                    v[i].x = t[2 * (i & 1)]; v[i].y = t[2 * (i & 1) + 1];
                    v[i] = __builtin_elementwise_fma(v[i], a2[i & 1], c2[i & 1]);
                }
                SCHED_FENCE();
#pragma unroll
                for (int i = 0; i < 8; ++i) p[i] = pkh(v[i]);
                SCHED_FENCE();
#pragma unroll
                for (int i = 0; i < 8; ++i) p[i] += 0x00040004u;
                SCHED_FENCE();
                u4 f[2];
#pragma unroll
                for (int k = 0; k < 2; ++k) {
                    const auto s0 = __builtin_amdgcn_permlane16_swap(p[4 * k], p[4 * k + 2], false, false);
                    const auto s1 = __builtin_amdgcn_permlane16_swap(p[4 * k + 1], p[4 * k + 3], false, false);
                    f[k].x = s0[0]; f[k].y = s1[0]; f[k].z = s0[1]; f[k].w = s1[1];
                }
                SCHED_FENCE();
#pragma unroll
                for (int k = 0; k < 2; ++k) {
#pragma unroll
                    for (int i = 0; i < 4; ++i) f[k][i] = bfi(m7, pat[i], f[k][i]);
                    __builtin_amdgcn_raw_buffer_store_b128(f[k], urs, vu[jj + k], 0, US_AUX);
                }
                SCHED_FENCE();
            }
        }
        MEM_FENCE(); SCHED_FENCE();
        lds_add1a(a_cnt + 23 * 4);
        __builtin_amdgcn_s_setprio(3);
        MEM_FENCE(); SCHED_FENCE();
        STAMP(4);
        const int t01 = 2 * hf, t23 = 6 - 2 * hf;
        u32 a_g = sm0 + L_CH + ((lane >> 2) & 3) * CH_SZ + C_U + (UPAD + 64 * wave + 16 * (lane >> 4) + 4 * (lane & 3)) * 2;
        u32 a_g0 = a_g + t01 * 512, a_g1 = a_g + t23 * 512;
        u32 vp2 = hf ? 0x80000000u : vp;
        PIN(a_g0); PIN(a_g1); PIN(vp2);
        __builtin_amdgcn_s_sleep(U_DELAY);
        lds_wait(cnt + 31, U_POS ? U_N : 1);
        STAMP(5);
        u4 up[8];
        u2 sg[4];
        auto ld_stats = [&]() __attribute__((always_inline)) {
#pragma unroll
            for (int t = 0; t < 4; ++t) sg[t] = __builtin_amdgcn_raw_buffer_load_b64(grs, vs + (t & 1) * 2048, (t < 2 ? t01 : t23) * 2048, 16);
        };
#pragma unroll
        for (int p = 0; p < 8; ++p) up[p] = BLD128(urs, p < 6 ? vp : vp2, p * 4096, U_AUX);
#pragma unroll
        for (int t = 0; t < 4; ++t) sg[t] = __builtin_amdgcn_raw_buffer_load_b64(grs, vs + (t & 1) * 2048, (t < 2 ? t01 : t23) * 2048, U_AUX);
        MEM_FENCE(); SCHED_FENCE();
        lds_add1a(a_cnt + 44);
        MEM_FENCE(); SCHED_FENCE();
        {
            u32 bad = 0, bad6 = 0;
#pragma unroll
            for (int p = 0; p < 8; ++p) {
#pragma unroll
                for (int i = 0; i < 4; ++i) up[p][i] ^= pat[i];
                *(lptr128)(uintptr_t)(a_u + p * 512) = up[p];
                bad |= up[p].x | up[p].y; PIN(bad);
                bad |= up[p].z | up[p].w; PIN(bad);
                if (p == 5) bad6 = bad;
                SCHED_FENCE();
            }
            STAMP(13);
            if (__builtin_expect(__any(((hf ? bad6 : bad) & m7) != 0), 0)) {
                u32 miss = 0;
#pragma unroll
                for (int p = 0; p < 8; ++p) if (__any(((up[p].x | up[p].y | up[p].z | up[p].w) & m7) != 0)) miss |= 1u << p;
                if (hf) miss &= 0x3fu;
#pragma unroll 1
                for (int spin = 0; miss != 0 && spin < SPIN_MAX; ++spin) {
                    MEM_FENCE();
#pragma unroll
                    for (int p = 0; p < 8; ++p) if ((miss >> p) & 1u) up[p] = BLD128(urs, vp, p * 4096, 16);
#pragma unroll
                    for (int p = 0; p < 8; ++p) if ((miss >> p) & 1u) {
#pragma unroll
                        for (int i = 0; i < 4; ++i) up[p][i] ^= pat[i];
                        *(lptr128)(uintptr_t)(a_u + p * 512) = up[p];
                        if (!__any(((up[p].x | up[p].y | up[p].z | up[p].w) & m7) != 0)) miss &= ~(1u << p);
                    }
                }
            }
            STAMP(6);
            MEM_FENCE();
            lds_add1a(a_cnt + 40);
            MEM_FENCE(); SCHED_FENCE();
        }
        __builtin_amdgcn_s_setprio(E_GP);
        u2 ut[4][2];
        asm volatile("ds_read_b64_tr_b16 %0, %8\n\tds_read_b64_tr_b16 %1, %8 offset:%10\n\tds_read_b64_tr_b16 %2, %8 offset:512\n\tds_read_b64_tr_b16 %3, %8 offset:512+%10\n\t"
                     "ds_read_b64_tr_b16 %4, %9\n\tds_read_b64_tr_b16 %5, %9 offset:%10\n\tds_read_b64_tr_b16 %6, %9 offset:512\n\tds_read_b64_tr_b16 %7, %9 offset:512+%10\n\t"
                     "s_waitcnt lgkmcnt(0)"
                     : "=&v"(ut[0][0]), "=&v"(ut[0][1]), "=&v"(ut[1][0]), "=&v"(ut[1][1]), "=&v"(ut[2][0]), "=&v"(ut[2][1]), "=&v"(ut[3][0]), "=&v"(ut[3][1])
                     : "v"(a_g0), "v"(a_g1), "n"(4 * CH_SZ) : "memory");
        if (__builtin_expect(__any(sg[0].y != tag || sg[1].y != tag || sg[2].y != tag || sg[3].y != tag), 0)) {
#pragma unroll 1
            for (int spin = 0; spin < SPIN_MAX; ++spin) {
                MEM_FENCE();
                ld_stats();
                if (!__any(sg[0].y != tag || sg[1].y != tag || sg[2].y != tag || sg[3].y != tag)) break;
                __builtin_amdgcn_s_sleep(G_SLEEP);
            }
        }
        STAMP(7);
        u32 gh[4][4];
        float xf[4][8];
        auto gate = [&](int t) __attribute__((always_inline)) {
            gate4(gh[t], ut[t][0].x, ut[t][0].y, ut[t][1].x, ut[t][1].y);
            const float mt = bflo(sg[t].x), dt = bfhi(sg[t].x) * (1.0f / LOG2E);
#pragma unroll
            for (int c = 0; c < 8; ++c) {
                const u32 uw = ut[t][c >> 2][(c >> 1) & 1];
                float xx = __builtin_fmaf((float)__builtin_bit_cast(h2, uw)[c & 1], dt, mt);
                PIN(xx);
                xf[t][c] = xx;
            }
            MEM_FENCE(); SCHED_FENCE();
        };
        gate(0); gate(1);
        if (!E_ORD) { gate(2); gate(3); }
        lds_wait(cnt + 13, 1);
        u32 vo[4];
        {
            const int i0 = 0xffff - (int)(__builtin_amdgcn_readfirstlane(cnt[12]) & 0xffffu);
            const int i1 = 0xffff - (int)(__builtin_amdgcn_readfirstlane(cnt[13]) & 0xffffu);
#pragma unroll
            for (int j = 0; j < 4; ++j) {
                const int l = 256 * ((j < 2 ? t01 : t23) + (j & 1)) + tid;
                vo[j] = (l == i0 || l == i1) ? 0x80000000u : l * (SD * 2) + g * 16;
                PIN(vo[j]);
            }
        }
        const char* yp = smem + L_STG + 64 * (tid >> 4) + 16 * (((tid >> 2) & 3) ^ ((tid >> 5) & 3)) + 4 * (tid & 3);
        const rsrc_t ors = ORS;
        STAMP(8);
        auto epi = [&](int t) __attribute__((always_inline)) {
            if (t >= E_UP) __builtin_amdgcn_s_setprio(E_EP);
            while (__hip_atomic_load(reinterpret_cast<unsigned long long*>(cnt + 2 + 2 * t), __ATOMIC_RELAXED, __HIP_MEMORY_SCOPE_WORKGROUP) != all8) __builtin_amdgcn_s_sleep(E_SLP);
            __builtin_amdgcn_fence(__ATOMIC_ACQUIRE, "wavefront");
            __builtin_amdgcn_wave_barrier();
            if (t == 0) STAMP(9);
            if (t == 2) STAMP(14);
            if (t == 3) STAMP(11);
            float yv[8];
#pragma unroll
            for (int c = 0; c < 8; ++c) yv[c] = *reinterpret_cast<const float*>(yp + c * STG_BUF + 1024 * t);
            u4 o;
#pragma unroll
            for (int h = 0; h < 4; ++h) {
                const h2 gg = __builtin_bit_cast(h2, gh[t][h]);
                float o0 = __builtin_fmaf(yv[2 * h], (float)gg[0], xf[t][2 * h]);
                float o1 = __builtin_fmaf(yv[2 * h + 1], (float)gg[1], xf[t][2 * h + 1]);
                PIN(o0); PIN(o1);
                o[h] = pkbf(o0, o1);
            }
            if (t < PACE) { __builtin_amdgcn_s_waitcnt(0x0f70); MEM_FENCE(); SCHED_FENCE(); }
            __builtin_amdgcn_raw_buffer_store_b128(o, ors, vo[t], 0, 0);
            MEM_FENCE(); SCHED_FENCE();
            if (t == 1) STAMP(10);
            if (t == 3) STAMP(12);
        };
        epi(0); epi(1);
        if (E_ORD) { __builtin_amdgcn_s_setprio(E_GP); gate(2); gate(3); }
        epi(2); epi(3);
    };
    auto f_role = [&]() __attribute__((always_inline)) {
        constexpr int WE = W_A;
        static_assert(W_A >= 2 && W_A + W_B + W_E <= 16, "weight loads");
        ARGS_LOAD();
        __builtin_amdgcn_s_setprio(3);
        LANE_IDS();
        const int hw = wave - 4;
        const bool has_gu = slot < 28;
        const int s7 = (slot * 37) >> 8, mat = s7 & 1, quart = 2 * (xcd >> 2) + ((s7 >> 1) & 1), cblk = 7 * ((xcd + 3) & 3) + slot - 7 * s7, ksl = 4 * quart + hw;
        const int sup = (xcd & 3) * 8 + (slot >> 2), chk = 4 * sup + hw, dblk = (xcd < 4 ? 4 : 0) + (slot & 3);
        auto f_box = [&]() __attribute__((always_inline)) {
            const int off = (hw == 2) ? 4 : 8, bw = (hw == 0) ? 2 : (hw == 1) ? 4 : (hw == 2) ? 1 : 3;
            *reinterpret_cast<h8*>(smem + L_BOX + (hw == 3 ? 4 : hw) * 1024 + lane * 16) = box_frag(al, q, off, bw);
            if (hw == 2) *reinterpret_cast<h8*>(smem + L_BOX + 3072 + lane * 16) = box_frag(al, q, 12, 1);
            const u4 z = {0, 0, 0, 0};
#pragma unroll
            for (int i = 0; i < 3; ++i) *reinterpret_cast<u4*>(smem + L_Z + hw * IMG + min(i * 1024 + lane * 16, IMG - 16)) = z;
            MEM_FENCE();
            SCHED_FENCE();
        };
        u32 wvo = ((64 * ksl + q) * SF + 128 * cblk + 8 * al) * 2;
        u32 a_cnt = sm0 + L_CNT, v16 = (hw == 0) ? lane * 16 : 0x80000000u;
        PIN(wvo); PIN(a_cnt); PIN(v16);
        ARGS_WAIT();
        u4 wr[2], wv[16], wdv[8];
        {
            const rsrc_t rrs = mk_rsrc(ka0[2], ka0[3], 0, SD * 2);
            wr[0] = BLD128(rrs, v16, 0, 0); wr[1] = BLD128(rrs, v16, 1024, 0);
        }
        MEM_FENCE(); SCHED_FENCE();
        if (W_DELAY && hw != 2) __builtin_amdgcn_s_sleep(W_DELAY);
        lds_wait(cnt + 17, 12);
        const rsrc_t wrs = mk_rsrc(mat ? ka2[6] : ka2[4], mat ? ka2[7] : ka2[5], 0, has_gu ? SD * SF * 2 : 0);
        auto load_w = [&](int j) __attribute__((always_inline)) {
            wv[j] = BLD128(wrs, wvo, 4 * j * (SF * 2), W_AUX);
            MEM_FENCE(); SCHED_FENCE();

        };
        STAMP(8);
        load_w(0); load_w(1);
        f_box();
        lds_add1a(a_cnt + 22 * 4);
#pragma unroll
        for (int j = 2; j < WE; ++j) { load_w(j); if (j + 1 < WE) __builtin_amdgcn_s_sleep(W_PACE); }
        MEM_FENCE(); SCHED_FENCE();
        STAMP(10);
        const rsrc_t grs = GRS;
        u32 tag;
        {
            float sw = 0.f;
            u32 a_pr = sm0 + L_P12 + (lane & 7) * P12_S, a_p3 = sm0 + L_P3 + (lane & 7) * P3_S;
            u32 vpub = (hw == 0) ? (lane < NREP ? G_KEY + (lane * 256 + (KMAP ? (int)blockIdx.x : b)) * 16 : 0x80000000u) : ((lane & 1) ? 0x80000000u : ((lane >> 3) * SL + b * 8 + (lane & 7)) * 8);
            u32 ck = 0xffff - (b * 8 + (lane & 7));
            PIN(a_pr); PIN(a_p3); PIN(vpub); PIN(ck);
            lds_wait(cnt + 0, 1);
            tag = __builtin_amdgcn_readfirstlane(cnt[28]);
            MEM_FENCE(); SCHED_FENCE();
            if (hw == 0) {
#pragma unroll
                for (int j = 0; j < 8; ++j) sw = dot2(wr[j >> 2][j & 3], 0x3f803f80u, sw);
                sw = wave_sum(sw);
            }
            MEM_FENCE(); SCHED_FENCE();
            if (hw < 2) {
                u4 pv[4];
                f4 s0, s1;
                lds_wait(cnt + 1, P_LIGHT);
                for (;;) {
                    u32 c;
                    asm volatile("ds_read_b32 %0, %7 offset:4\n\tds_read_b128 %1, %8\n\tds_read_b128 %2, %8 offset:16\n\tds_read_b128 %3, %8 offset:32\n\t"
                                 "ds_read_b128 %4, %8 offset:48\n\tds_read_b128 %5, %9\n\tds_read_b128 %6, %9 offset:16\n\ts_waitcnt lgkmcnt(0)"
                                 : "=&v"(c), "=&v"(pv[0]), "=&v"(pv[1]), "=&v"(pv[2]), "=&v"(pv[3]), "=&v"(s0), "=&v"(s1) : "v"(a_cnt), "v"(a_pr), "v"(a_p3) : "memory");
                    const u32 cc = __builtin_amdgcn_readfirstlane(c);
                    if (cc >= 4u) break;
                    if (cc == 0u) __builtin_amdgcn_s_sleep(1);
                }
                float mb, den, rd;
                stats(pv, mb, den, rd);
                u4 f; f.y = tag; f.w = tag;
                if (hw == 0) {
                    const f4 sv = s0 + s1;
                    const u32 rb = __float_as_uint(rbf(__builtin_fmaf(-mb, sw, (sv[0] + sv[1]) + (sv[2] + sv[3])) * rd));
                    u32 m1 = ((rb ^ ((u32)((int)rb >> 31) | 0x80000000u)) & 0xffff0000u) | ck, m2 = 0;
                    top2_of8(m1, m2);
                    f.x = __builtin_amdgcn_readlane(m1, 7); f.z = __builtin_amdgcn_readlane(m2, 7);
                } else {
                    f.x = __builtin_amdgcn_perm(__float_as_uint(den), __float_as_uint(mb), 0x07060302u);
                    f.z = __builtin_amdgcn_mov_dpp(f.x, 0xf5, 0xf, 0xf, true);
                }
                __builtin_amdgcn_raw_buffer_store_b128(f, grs, vpub, 0, 16);
            }
            MEM_FENCE(); SCHED_FENCE();
        }
        if (W_GATE) lds_wait<F_SLP>(cnt + 23, 4);
        STAMP(4);
#pragma unroll
        for (int j = WE; j < WE + W_B; ++j) { load_w(j); if (hw >= 2) __builtin_amdgcn_s_sleep(W_PACE2); }
        u32 vtag = tag, dvo = (q * SD + 128 * dblk + 8 * al) * 2;
        const u32 img = sm0 + L_XN + hw * IMG;
        u32 aoff = img + (al >> 2) * XN_S + 16 * q, a_red = sm0 + L_XN + q * XN_S + al * 16;
        u32 idx = ((al & 3) * 0x4444u) | (((al & 3) ^ 1) * 0x1111u);
        const u32 Q = 32 * cblk + (lane & 31), cj = __umul24(Q, 9363u) >> 16;
        u32 gu_off = has_gu ? (__umul24(cj, 121u) + Q + ((q >> 1) * 8 + quart * 2 + mat) * 8) * 16 : 0x80000000u;
        u32 aoffs = (chk * 128 + lane - ((lane & 7) == 7 ? 1 : 0)) * 16;
        u32 h_st = img + ((lane >> 3) & 1) * (2 * XN_S) + (4 * (lane & 7) + q) * 8;
        u32 dn_off = (GU_WORDS * 4) + ((sup * 8 + dblk) * 64 + lane) * 16;
        PIN(vtag); PIN(dvo); PIN(aoff); PIN(a_red); PIN(idx); PIN(gu_off); PIN(aoffs); PIN(h_st); PIN(dn_off);
        MEM_FENCE(); SCHED_FENCE();
#pragma unroll
        for (int j = WE + W_B; j < 16 - W_E - W_F; ++j) { load_w(j); __builtin_amdgcn_s_sleep(W_PACE2); }
        STAMP(5);
        int dso[7];
#pragma unroll
        for (int j = 0; j < 7; ++j) {
            const int Qd = 7 * chk + j;
            dso[j] = (128 * (Qd >> 5) + 8 * (Qd & 15) + 4 * ((Qd >> 4) & 1)) * (SD * 2);
            asm volatile("" : "+s"(dso[j]));
        }
        u32 k1, k2;
        u4 kg[4];
        if (hw == 3) {
            u32 vk = G_KEY + ((blockIdx.x % NREP) * 256 + lane) * 16;
            PIN(vk);
            lds_wait(cnt + 23, 4);
            __builtin_amdgcn_s_sleep(G_DELAY);
            poll_pieces<4, G_SLEEP>(kg, [&](int p) { return BLD128(grs, vk + 1024 * p, 0, 16); }, [&](u4 v_) { return v_.w != tag; }, [&]() {});
            if (W_F) lds_add1a(a_cnt + 21 * 4);
            if (U_POS == 0) lds_add1a(a_cnt + 31 * 4);
            STAMP(2);
        } else {
            __builtin_amdgcn_s_sleep(G_DELAY);
            if (W_F) lds_wait(cnt + 21, 1);
        }
        MEM_FENCE(); SCHED_FENCE();
#pragma unroll
        for (int j = 16 - W_E - W_F; j < 16 - W_E; ++j) load_w(j);
        if (U_POS == 2) lds_add1a(a_cnt + 31 * 4);
        if (hw == 3) {
            u32 m1 = max(kg[0].x, kg[1].x), m2 = min(kg[0].x, kg[1].x);
            m2 = med3u(m1, m2, kg[2].x); m1 = max(m1, kg[2].x); m2 = med3u(m1, m2, kg[3].x); m1 = max(m1, kg[3].x);
            m2 = max(max(m2, max(kg[0].z, kg[1].z)), max(kg[2].z, kg[3].z));
            wave_top2(m1, m2);
            u2 m; m.x = m1; m.y = m2;
            asm volatile("s_mov_b32 exec_lo, 0\n\ts_brev_b32 exec_hi, 1\n\tds_write_b64 %0, %1 offset:48\n\ts_mov_b64 exec, -1" :: "v"(a_cnt), "v"(m) : "memory");
            k1 = __builtin_amdgcn_readlane(m1, 63); k2 = __builtin_amdgcn_readlane(m2, 63);
        } else {
            for (;;) {
                u2 m;
                asm volatile("ds_read_b64 %0, %1 offset:48\n\ts_waitcnt lgkmcnt(0)" : "=v"(m) : "v"(a_cnt) : "memory");
                k1 = __builtin_amdgcn_readfirstlane(m.x); k2 = __builtin_amdgcn_readfirstlane(m.y);
                if (k2) break;
                __builtin_amdgcn_s_sleep(1);
            }
        }
        MEM_FENCE(); SCHED_FENCE();
        const u32 ts = (0xffffu - ((lane & 32) ? k2 : k1)) & (SL - 1), osr = (xcd * SL + ts) * 8;
        const u32 xr = __builtin_amdgcn_raw_buffer_load_b32(XRS, ts * (SD * 2) + 128 * ksl + 4 * (lane & 31), 0, 0);
        u2 sr = __builtin_amdgcn_raw_buffer_load_b64(grs, osr, 0, 16);
        MEM_FENCE(); SCHED_FENCE();
        if (U_POS == 1) lds_add1a(a_cnt + 31 * 4);
        if (W_E > 0) lds_wait(cnt + 11, 4);
#pragma unroll
        for (int j = 16 - W_E; j < 16; ++j) load_w(j);
        MEM_FENCE(); SCHED_FENCE();
        {
            u32 a_xn = img + (lane >> 5) * (2 * XN_S) + (lane & 31) * 16;
            PIN(a_xn);
            if (__builtin_expect(!__all(sr.y == tag), 0)) {
#pragma unroll 1
                for (int spin = 0; spin < SPIN_MAX && !__all(sr.y == tag); ++spin) { MEM_FENCE(); sr = __builtin_amdgcn_raw_buffer_load_b64(grs, osr, 0, 16); __builtin_amdgcn_s_waitcnt(0x0f70); }
            }
            const float mb2 = bflo(sr.x), rc2 = __builtin_amdgcn_rcpf(bfhi(sr.x));
            float e0 = bflo(xr) - mb2, e1 = bfhi(xr) - mb2;
            PIN(e0); PIN(e1);
            e0 = rbf(e0) * rc2; e1 = rbf(e1) * rc2;
            PIN(e0); PIN(e1);
            f2 o0; o0.x = rbf(e0); o0.y = rbf(e1);
#pragma unroll
            for (int h = 0; h < 2; ++h) {
                *(lptr32)(uintptr_t)(a_xn + h * (XN_S + 4)) = __float_as_uint(o0.x); *(lptr32)(uintptr_t)(a_xn + h * (XN_S + 4) + 8) = __float_as_uint(o0.y);
            }
            WAVE_SYNC();
            MEM_FENCE(); SCHED_FENCE();
            STAMP(3);
        }
        const u32 stf = __builtin_amdgcn_readlane(sr.x, 32 * (hw >> 1));
        auto reduce_send = [&](f4 a0, auto off_c, int ci, u32 voff) __attribute__((always_inline)) {
            constexpr int OFF = decltype(off_c)::value;
            *(__attribute__((address_space(3))) f4*)(uintptr_t)(a_red + hw * IMG + OFF) = a0;
            u32 old; f4 r[4];
            const u32 one = 1u;
            asm volatile("s_mov_b64 exec, 1\n\tds_add_rtn_u32 %0, %5, %6\n\ts_mov_b64 exec, -1\n\tds_read_b128 %1, %7 offset:%8\n\tds_read_b128 %2, %7 offset:%8+%9\n\t"
                         "ds_read_b128 %3, %7 offset:%8+2*%9\n\tds_read_b128 %4, %7 offset:%8+3*%9\n\ts_waitcnt lgkmcnt(0)"
                         : "=&v"(old), "=&v"(r[0]), "=&v"(r[1]), "=&v"(r[2]), "=&v"(r[3]) : "v"(a_cnt + 4 * ci), "v"(one), "v"(a_red), "n"(OFF), "n"(IMG) : "memory");
            if (__builtin_amdgcn_readfirstlane(old) == 3u) {
                f4 t;
#pragma unroll
                for (int i = 0; i < 4; ++i) { float t0 = r[0][i] + r[1][i], t1 = r[2][i] + r[3][i]; PIN(t0); PIN(t1); t0 += t1; PIN(t0); t[i] = t0; }
                __builtin_amdgcn_raw_buffer_store_b128(tag4(t, vtag), HRS, voff, 0, 16);
                MEM_FENCE(); SCHED_FENCE();
                lds_add1a(a_cnt + 4 * ci);
            }
        };
        auto load_wd = [&](int j0, int j1) __attribute__((always_inline)) {
            const rsrc_t drs = mk_rsrc(ka3[0], ka3[1], 0, SD * SF * 2);
            MEM_FENCE(); SCHED_FENCE();
#pragma unroll
            for (int j = j0; j < j1; ++j) {
                wdv[j] = BLD128(drs, dvo, dso[j], W_AUX);
                if (D_PACE) { MEM_FENCE(); SCHED_FENCE(); __builtin_amdgcn_s_sleep(D_PACE); }
            }
            wdv[7] = u4{0, 0, 0, 0};
            MEM_FENCE(); SCHED_FENCE();
        };
        load_wd(0, WD_A);
        STAMP(6);
        const rsrc_t hrs = HRS;
        {
            f4 a0 = {0, 0, 0, 0};
            u4 Af[4], Ag[4];
#pragma unroll
            for (int t = 0; t < 4; ++t) Ag[t] = *(__attribute__((address_space(3))) const u4*)(uintptr_t)(aoff + 64 * t);
#pragma unroll
            for (int t = 0; t < 4; ++t) Af[t] = *(__attribute__((address_space(3))) const u4*)(uintptr_t)(aoff + 256 + 64 * t);
#pragma unroll
            for (int t = 0; t < 4; ++t) a0 = smfma(Ag[t], wv[2 * t], wv[2 * t + 1], a0, idx);
#pragma unroll
            for (int t = 0; t < 4; ++t) {
                a0 = smfma(Af[t], wv[8 + 2 * t], wv[9 + 2 * t], a0, idx);
                if (t == WD_T) { asm volatile("" :: "v"(a0)); load_wd(WD_A, WD_A + WD_B); }
            }
            STAMP(7);
            reduce_send(a0, std::integral_constant<int, 288>{}, 19, gu_off);
        }
        MEM_FENCE(); SCHED_FENCE();
        STAMP(9);
        lds_wait(cnt + 19, WD_G);
        load_wd(WD_A + WD_B, WD_1);
        if (blockIdx.x == 0 && tid == 256)
            __hip_atomic_store((__attribute__((address_space(1))) u32*)((((unsigned long long)ka0[5] << 32) | ka0[4]) + WS_EPOCH), tag, __ATOMIC_RELAXED, __HIP_MEMORY_SCOPE_AGENT);
        const u32 keyf = hw < 2 ? k1 : k2;
        const int tokf = (0xffff - (int)(keyf & 0xffffu)) & (SL - 1), ftile = tokf >> 8;
        const bool mine = hf ? (ftile >= 2 && ftile <= 5) : (ftile < 2 || ftile > 5);
        const int chf = 4 * (hw & 1) + q;
        const u32 eoff = (tokf * SD + g * 8 + chf) * 2;
        const u32 xe = __builtin_amdgcn_raw_buffer_load_b16(XRS, mine ? eoff : 0x80000000u, 0, 0);
        MEM_FENCE(); SCHED_FENCE();
        __builtin_amdgcn_s_sleep(A_DELAY);
        STAMP(11);
        {
            u4 pv[2];
            poll_pieces<2, A_SLEEP>(pv, [&](int i) { return BLD128(hrs, aoffs + 1024 * i, 0, 16); }, [&](u4 v) { return tagof(v) != tag; }, [&]() { load_wd(WD_1, 7); });
            STAMP(12);
            int r[2];
#pragma unroll
            for (int i = 0; i < 2; ++i) {
                const u4 p = untag4(pv[i]);
                r[i] = __float_as_int(rbf(fold4(__uint_as_float(p[0]), __uint_as_float(p[2]), __uint_as_float(p[1]), __uint_as_float(p[3]))));
            }
            const float gv = __int_as_float(__builtin_amdgcn_update_dpp(r[0], r[1], 0x128, 0xf, 0xc, false));
            const float up = __int_as_float(__builtin_amdgcn_update_dpp(r[1], r[0], 0x128, 0xf, 0x3, false));
            const u32 hv = __float_as_uint(rbf(rbf(silu_fast(gv)) * up));
            *(lptr32)(uintptr_t)h_st = hv; *(lptr32)(uintptr_t)(h_st + XN_S + 4) = hv;
            WAVE_SYNC();
            u4 Ah[4];
#pragma unroll
            for (int t = 0; t < 4; ++t) Ah[t] = *(__attribute__((address_space(3))) const u4*)(uintptr_t)(aoff + 64 * t);
            f4 a0 = {0, 0, 0, 0};
#pragma unroll
            for (int t = 0; t < 4; ++t) a0 = smfma(Ah[t], wdv[2 * t], wdv[2 * t + 1], a0, idx);
            WAVE_SYNC();
            reduce_send(a0, std::integral_constant<int, 0>{}, 18, dn_off);
        }
        MEM_FENCE(); SCHED_FENCE();
        STAMP(13);
        if (mine) {
            u32 boff = lane < 32 ? (GU_WORDS * 4) + ((lane * 8 + (g >> 4)) * 64 + hw * 16 + (g & 15)) * 16 : 0x80000000u;
            const u32 rb = (keyf & 0x80000000u) ? (keyf & 0x7fff0000u) : (~keyf & 0xffff0000u);
            float rs = rbf(__builtin_amdgcn_rcpf(1.0f + __builtin_amdgcn_exp2f(-LOG2E * __uint_as_float(rb))));
            const int oi = hf ? ftile - 2 : (ftile < 2 ? ftile : ftile - 4);
            float xd = bflo(xe);
            PIN(xd);
            xd -= bflo(stf);
            PIN(xd);
            float xnv = rbf(rbf(xd) * __builtin_amdgcn_rcpf(bfhi(stf)));
            const int tt = tokf & 255;
            u32 ya = sm0 + L_STG + chf * STG_BUF + 1024 * oi + 64 * (tt >> 4) + 16 * (((tt >> 2) & 3) ^ ((tt >> 5) & 3)) + 4 * (tt & 3);
            u32 yc = a_cnt + 8 + 8 * oi + chf;
            u32 eo = (al == 0) ? eoff : 0x80000000u;
            PIN(boff); PIN(rs); PIN(xnv); PIN(ya); PIN(yc); PIN(eo);
            MEM_FENCE(); SCHED_FENCE();
            __builtin_amdgcn_s_sleep(B_DELAY);
            u4 pv = BLD128(hrs, boff, 0, 16);
#pragma unroll 1
            for (int spin = 0; spin < SPIN_MAX; ++spin) {
                const bool b0 = tagof(pv) != tag && lane < 32;
                if (!__any(b0)) break;
                MEM_FENCE();
                __builtin_amdgcn_s_sleep(B_SLEEP);
                if (b0) pv = BLD128(hrs, boff, 0, 16);
            }
            STAMP(14);
            float sv; u32 c;
            asm volatile("ds_read_u8 %0, %2\n\tds_read_b32 %1, %3" : "=&v"(c), "=&v"(sv) : "v"(yc), "v"(ya) : "memory");
            SCHED_FENCE();
            pv = untag4(pv);
            const float tot = row_sum(fold4(__uint_as_float(pv[0]), __uint_as_float(pv[2]), __uint_as_float(pv[1]), __uint_as_float(pv[3])));
            float gt = LOG2E * silu_fast(rbf(xnv + rbf(rbf(tot) * rs)));
            asm volatile("s_waitcnt lgkmcnt(0)" : "+v"(c), "+v"(sv), "+v"(gt) :: "memory");
            while (__any(c == 0u)) {
                __builtin_amdgcn_s_sleep(1);
                asm volatile("ds_read_u8 %0, %2\n\tds_read_b32 %1, %3\n\ts_waitcnt lgkmcnt(0)" : "=&v"(c), "=&v"(sv) : "v"(yc), "v"(ya) : "memory");
            }
            const float ov = __builtin_fmaf(sv, gt, bflo(xe));
            __builtin_amdgcn_raw_buffer_store_b16((u16)pkbf(ov, ov), ORS, eo, 0, 0);
        }
    };
    if (__builtin_expect(wave < 4, E_FIRST)) {
        e_role();
    } else if (wave < 8) {
        f_role();
    } else {
        ARGS_LOAD();
        const bool gen = !KX || wave < 12;
        const int cw = KX ? (wave + 4 * hf) & 7 : wave - 8;
        __builtin_amdgcn_s_setprio(2);
        const int d = g * 8 + cw;
        char* ch = smem + L_CH + cw * CH_SZ;
        u32 vp = d * (SN * 2) + (tid & 63) * 4, vd = d * 2;
        PIN(vp); PIN(vd);
        ARGS_WAIT();
        const u32 pDT = __builtin_amdgcn_raw_buffer_load_b16(mk_rsrc(ka2[0], ka2[1], 0, SD * 2), vd, 0, 0);
        const u32 pLA = __builtin_amdgcn_raw_buffer_load_b32(mk_rsrc(ka1[0], ka1[1], 0, SD * SN * 2), vp, 0, 2);
        const u32 pAI = __builtin_amdgcn_raw_buffer_load_b32(mk_rsrc(ka1[2], ka1[3], 0, SD * SN * 2), vp, 0, 2);
        const u32 pCR = __builtin_amdgcn_raw_buffer_load_b32(mk_rsrc(ka1[4], ka1[5], 0, SD * SN * 2), vp, 0, 2);
        const u32 pCI = __builtin_amdgcn_raw_buffer_load_b32(mk_rsrc(ka1[6], ka1[7], 0, SD * SN * 2), vp, 0, 2);
        const u32 pDS = __builtin_amdgcn_raw_buffer_load_b16(mk_rsrc(ka2[2], ka2[3], 0, SD * 2), vd, 0, 0);
        STAMP(4);
        FIRST_BARRIER(false);
        int lane = tid & 63;
        PIN(lane);
        const int al = lane & 15, q = lane >> 4;
        auto clear = [&](char* c, int n) {
            const u4 z = {0, 0, 0, 0};
#pragma unroll
            for (int i = 0; i < 4; ++i) *reinterpret_cast<u4*>(c + i * 1024 + lane * 16) = z;
            if (lane < n) *reinterpret_cast<u4*>(c + 4096 + lane * 16) = z;
        };
        auto put4 = [&](char* c, int idx, u32 lo, u32 hi) {
            u16* rec = reinterpret_cast<u16*>(c);
            u16* rec1 = reinterpret_cast<u16*>(c + REC_B + 2);
            u2 o; o.x = lo; o.y = hi;
            *reinterpret_cast<u2*>(rec + idx) = o;
            rec1[idx] = (u16)lo;
            *reinterpret_cast<u32*>(rec1 + idx + 1) = (lo >> 16) | (hi << 16);
            rec1[idx + 3] = (u16)(hi >> 16);
        };
        const u32 chb = sm0 + L_CH + cw * CH_SZ, nul = sm0 + L_NUL + lane * 4;
        u32 sc_x = R_KA + ZA - 3 - 128 - 256 * (q >> 1) - 4 * (q & 1) - 8 * al;
        u32 sc_2 = chb + (R_K2 + 190 - 32 * (q >> 1) - 64 * (q & 1) - 2 * al) * 2, sc_h = chb + (R_K3 + 127 - 16 - 32 * q - al) * 2, sc_l = lane ? sc_h + 32 : nul;
        const int pj = (lane < 18) ? (lane >= 9 ? lane - 9 : lane) : (lane >= 21 ? lane - 21 : lane - 18), pcopy = (lane < 18) ? (lane >= 9) : (lane >= 21);
        u32 sc_p = lane < 24 ? chb + ((lane < 18) ? (R_KP + 1055 - 1027 - pj) : (R_KA + ZA - 514 + pj)) * 2 + (pcopy ? REC_B + 2 : 0) : nul;
        int pcls = (lane < 18) ? (pj < 2 ? 0 : 1) : 2;
        const u32 stg = sm0 + L_STG + cw * STG_BUF + q * 16;
        u32 fr_b = stg + al * STG_ROW, fr_z = stg + 16 * STG_ROW;
        u32 fr_x = stg + (al < 8 ? al : 8 + (((al & 3) == 2) ? ((al - 8) >> 1) : (2 * ((al - 7) >> 2)))) * STG_ROW;
        u32 fr_y = stg + ((al & 10) == 0 ? 13 + (al >> 1) + (al & 1) - ((al >> 1) & 1) : (al >> 2)) * STG_ROW;
        asm volatile("s_mov_b32 m0, %0\n\ts_nop 0" :: "s"(sm0 + L_STG + cw * STG_BUF) : "memory");
        {
            hcx one; one.r = h2{(f16)1.0f, (f16)1.0f}; one.i = h2{(f16)0.0f, (f16)0.0f};
            stg_put<0>(one);
        }
        clear(ch, 40);
        PIN(sc_x); PIN(sc_2); PIN(sc_h); PIN(sc_l); PIN(sc_p); PIN(pcls); PIN(fr_b); PIN(fr_z); PIN(fr_x); PIN(fr_y);
        MEM_FENCE(); SCHED_FENCE();
        const float dt = rbf(exp_acc(bflo(pDT)));
        f2 ar, ai, cr, ci;
        {
            f2 la = bfup(pLA);
            f2 ex; ex.x = __builtin_amdgcn_exp2f(la.x * LOG2E); ex.y = __builtin_amdgcn_exp2f(la.y * LOG2E);
            ar = (-rbf2(ex) * dt) * LOG2E;
            ai = (bfup(pAI) * dt) * 0.15915494309189535f;
            cr = bfup(pCR) * dt;
            ci = bfup(pCI) * dt;
        }
        asm volatile("" :: "v"(code_pf));
        STAMP(1);
        const float bound = __builtin_fmaf(2.0f, wave_sum(fabsf(cr.x) + fabsf(cr.y) + fabsf(ci.x) + fabsf(ci.y)), fabsf(bflo(pDS)));
        int ke = 14 - ((int)((__float_as_uint(bound) >> 23) & 0xff) - 127);
        ke = __builtin_amdgcn_readfirstlane(min(max(ke, -100), 100));
        const float sig2 = __uint_as_float((u32)(127 + ke + 1) << 23);
        const float yscale = __uint_as_float((u32)(127 - ke) << 23) * (1.0f / (LOG2E * LOG2E));
        cplx G0; G0.r = cr * sig2; G0.i = -ci * sig2;
        float skip = (lane == 0) ? 0.5f * sig2 * bflo(pDS) : 0.0f;
        PIN(skip);
        typedef __attribute__((address_space(3))) u16* lptr16;
        auto scatter_y = [&](u32 xl, u32 y01, u32 y3) __attribute__((always_inline)) {
            *(lptr32)(uintptr_t)sc_2 = y01;
            *(lptr16)(uintptr_t)sc_l = (u16)y3; *(lptr16)(uintptr_t)sc_h = (u16)(y3 >> 16);
            const u32 v0 = __builtin_amdgcn_readlane(y01, 63), v1 = __builtin_amdgcn_readlane(y3, 0), v2 = __builtin_amdgcn_readlane(xl, 63);
            *(lptr16)(uintptr_t)sc_p = (u16)((pcls == 0) ? v0 : (pcls == 1) ? v1 : v2);
            WAVE_SYNC();
        };
        const rsrc_t krs = mk_rsrc(ka0[4], ka0[5], WS_K, SD * 2048);
        u32 ktag = 0;
        if (KX) { lds_wait(cnt + 0, 1); ktag = __builtin_amdgcn_readfirstlane(cnt[28]); }
        if (!gen) {
            const u32 vk = d * 2048 + lane * 16;
            __builtin_amdgcn_s_sleep(K_PRE);
            lds_wait<4>(cnt + 20, 4);
            __builtin_amdgcn_s_sleep(K_DELAY);
            STAMP(8);
            u4 kv[2];
            poll_pieces<2, G_SLEEP>(kv, [&](int i) { return BLD128(krs, vk + 1024 * i, 0, 16); }, [&](u4 v_) { return v_.w != ktag; }, [&]() {});
            STAMP(2);
            put4(ch, sc_x + 128, kv[0].x, kv[0].y);
            put4(ch, sc_x, kv[0].z, kv[1].x);
            scatter_y(kv[0].z, kv[1].y, kv[1].z);
            STAMP(3);
        } else {
            typedef __attribute__((address_space(3))) const u4* lq;
            const cplx E1 = cexp2(ar, ai, 1.0f), E2 = csq(E1), E4 = csq(E2), E8 = csq(E4);
            const hcx z1 = toh(E1), z2 = toh(E2), z4 = toh(E4), z8 = toh(E8);
            {
                const cplx E16 = csq(E8), E32 = csq(E16), E64 = csq(E32);
                hcx c[8];
                c[1] = z8; c[2] = toh(E16); c[4] = toh(E32);
                const hcx p64 = toh(E64), p128 = toh(csq(E64));
                c[3] = hmul(c[1], c[2]);
                stg_put<1>(c[1]); stg_put<2>(c[2]); stg_put<4>(c[4]); stg_put<8>(p64);
                c[5] = hmul(c[1], c[4]); c[6] = hmul(c[2], c[4]); c[7] = hmul(c[3], c[4]);
                stg_put<16>(p128); stg_put<3>(c[3]);
                const hcx t9 = hmul(c[1], p64), t10 = hmul(c[2], p64);
                stg_put<5>(c[5]); stg_put<6>(c[6]);
                const hcx t11 = hmul(c[3], p64), t12 = hmul(c[4], p64);
                stg_put<7>(c[7]); stg_put<9>(t9);
                const hcx t13 = hmul(c[5], p64), t14 = hmul(c[6], p64);
                stg_put<10>(t10); stg_put<11>(t11);
                const hcx t15 = hmul(c[7], p64);
                stg_put<12>(t12); stg_put<13>(t13); stg_put<14>(t14); stg_put<15>(t15);
            }
            WAVE_SYNC();
            u4 B0[8], B1[8], Af[8];
#pragma unroll
            for (int s = 0; s < 8; ++s) B0[s] = *(lq)(uintptr_t)(fr_b + s * 64);
#pragma unroll
            for (int s = 0; s < 8; ++s) B1[s] = *(lq)(uintptr_t)(fr_z + s * 64);
            WAVE_SYNC();
            STAMP(8);
            if (!KX && (KG_SW & 1) && cw < 4) __builtin_amdgcn_s_setprio(1);
            hcx J[4];
            {
                const cplx F1 = cexp2(ar, ai, 256.0f), F2 = csq(F1);
                const hcx f1 = toh(F1), f2_ = toh(F2), f4_ = toh(csq(F2));
                const hcx g0 = toh(G0), g1 = hmul_conj(g0, z1), g2 = hmul_conj(g0, z2), g4 = hmul_conj(g0, z4), g8 = hmul_conj(g0, z8);
                stg_put<0>(g0); stg_put<1>(g1);
                const hcx g3 = hmul_conj(g1, z2), g5 = hmul_conj(g1, z4);
                stg_put<2>(g2); stg_put<4>(g4);
                const hcx g6 = hmul_conj(g2, z4), g7 = hmul_conj(g3, z4);
                stg_put<3>(g3); stg_put<5>(g5);
                const hcx h0 = hmul_conj(g0, f1), h2_ = hmul_conj(g2, f1);
                stg_put<6>(g6); stg_put<7>(g7);
                const hcx h4 = hmul_conj(g4, f1), h6 = hmul_conj(g6, f1);
                stg_put<8>(h0); stg_put<9>(h2_);
                const hcx j1 = hmul_conj(g8, f1), k2 = hmul_conj(g4, f2_);
                stg_put<10>(h4); stg_put<11>(h6);
                const hcx j2 = hmul_conj(g8, f2_), k3 = hmul_conj(h4, f2_);
                stg_put<12>(j1); stg_put<13>(k2);
                const hcx j3 = hmul_conj(j1, f2_);
                J[0] = hmul_conj(g8, f4_);
                stg_put<14>(j2); stg_put<15>(k3);
                J[1] = hmul_conj(j1, f4_); J[2] = hmul_conj(j2, f4_);
                stg_put<16>(j3);
                J[3] = hmul_conj(j3, f4_);
            }
            WAVE_SYNC();
#pragma unroll
            for (int s = 0; s < 8; ++s) Af[s] = *(lq)(uintptr_t)(fr_x + s * 64);
            WAVE_SYNC();
            stg_put<0>(J[0]); stg_put<1>(J[1]); stg_put<2>(J[2]); stg_put<3>(J[3]);
            STAMP(9);
            if (!KX && KG_SW == 2 && cw < 4) __builtin_amdgcn_s_setprio(1);
            if (!KX && KG_SW == 3 && cw >= 4) __builtin_amdgcn_s_setprio(1);
            f4 X0 = {0, 0, 0, 0}, X1 = {0, 0, 0, 0}, Y0 = {0, 0, 0, 0}, Y1 = {0, 0, 0, 0};
            X0[0] = skip;
            auto mf = [](u4 a, u4 b, f4 c) { return mfma16(__builtin_bit_cast(h8, a), __builtin_bit_cast(h8, b), c); };
#pragma unroll
            for (int s = 0; s < 4; ++s) {
#pragma unroll
                for (int i = 0; i < 4; ++i) {
                    u32 zr = B1[s][i], zi = B1[s + 4][i], t;
                    asm volatile("v_pk_mul_f16 %2, %4, %1\n\tv_pk_mul_f16 %1, %3, %1\n\tv_pk_fma_f16 %1, %4, %0, %1\n\tv_pk_fma_f16 %0, %3, %0, %2 neg_lo:[0,0,1] neg_hi:[0,0,1]"
                                 : "+v"(zr), "+v"(zi), "=&v"(t) : "v"(B0[s][i]), "v"(B0[s + 4][i]));
                    B1[s][i] = zr; B1[s + 4][i] = zi;
                }
                X0 = mf(Af[2 * s], B0[2 * s], X0); X0 = mf(Af[2 * s + 1], B0[2 * s + 1], X0);
                asm volatile("" :: "v"(X0));
                SCHED_FENCE();
            }
            STAMP(10);
            WAVE_SYNC();
#pragma unroll
            for (int s = 0; s < 8; ++s) { X1 = mf(Af[s], B1[s], X1); asm volatile("" :: "v"(X1) : "memory"); Af[s] = *(lq)(uintptr_t)(fr_y + s * 64); SCHED_FENCE(); }
            u4 k0, k1;
            k0.x = pkh_r(X0[3], X0[2]); k0.y = pkh_r(X0[1], X0[0]);
            put4(ch, sc_x + 128, k0.x, k0.y);
            STAMP(11);
#pragma unroll
            for (int s = 0; s < 8; ++s) { Y0 = mf(Af[s], B0[s], Y0); Y1 = mf(Af[s], B1[s], Y1); }
            k0.z = pkh_r(X1[3], X1[2]); k1.x = pkh_r(X1[1], X1[0]);
            put4(ch, sc_x, k0.z, k1.x);
            STAMP(2);
            k1.y = __builtin_amdgcn_permlane32_swap(pkh_r(Y0[1], Y0[0]), pkh_r(Y1[1], Y1[0]), false, false)[0];
            k1.z = pkh(Y0[2], Y1[2]);
            if (KX) {
                u32 ln;
                asm volatile("v_mbcnt_lo_u32_b32 %0, -1, 0\n\tv_mbcnt_hi_u32_b32 %0, -1, %0" : "=v"(ln));
                const u32 vk = d * 2048 + ln * 16;
                k0.w = k1.w = ktag;
                __builtin_amdgcn_raw_buffer_store_b128(k0, krs, vk, 0, 16);
                __builtin_amdgcn_raw_buffer_store_b128(k1, krs, vk + 1024, 0, 16);
                MEM_FENCE();
                lds_add1(cnt + 20);
            }
            scatter_y(k0.z, k1.y, k1.z);
            STAMP(3);
        }
        __builtin_amdgcn_s_setprio(CV_P0);
        lds_wait(cnt + 22, 4);
        STAMP(5);
        conv(hf, sm0, cw, yscale);
        STAMP(7);
        if (WB_AT >= 0 && wave == WB_W && slot == 1) {
            __builtin_amdgcn_s_sleep(WB_AT < 0 ? 0 : WB_AT);
            asm volatile("buffer_wbl2 sc1" ::: "memory");
        }

    }
}

extern "C" void kernel_launch(void* const* d_in, const int* in_sizes, int n_in,
                              void* d_out, int out_size, void* d_ws, size_t ws_size,
                              hipStream_t stream) {
    char* p = (char*)d_ws + ((WS_OFF < 0 ? ws_size + (long long)WS_OFF * 1048576ll : (size_t)WS_OFF * 1048576ull) & ~(size_t)255);
    static_assert(WS_OFF >= 0 || WS_END <= (size_t)(-WS_OFF) * 1048576, "workspace");
    k_fused<<<256, 1024, 0, stream>>>((const u16*)d_in[0], (const u16*)d_in[8], p, (u16*)d_out,
                                      (const u16*)d_in[1], (const u16*)d_in[2], (const u16*)d_in[3], (const u16*)d_in[4],
                                      (const u16*)d_in[5], (const u16*)d_in[6], (const u16*)d_in[9], (const u16*)d_in[10], (const u16*)d_in[11]);
}
```

```cpp
#include <hip/hip_runtime.h>
#include <stdint.h>
#include <type_traits>

typedef uint16_t u16;
typedef uint32_t u32;
typedef _Float16 f16;
typedef __attribute__((ext_vector_type(8))) _Float16 h8;
typedef __attribute__((ext_vector_type(2))) _Float16 h2;
typedef __attribute__((ext_vector_type(2))) __bf16 b2;
typedef __attribute__((ext_vector_type(8))) __bf16 b8;
typedef __attribute__((ext_vector_type(4))) float f4;
typedef __attribute__((ext_vector_type(2))) float f2;
typedef __attribute__((ext_vector_type(4))) u32 u4;
typedef __attribute__((ext_vector_type(2))) u32 u2;

constexpr int SL = 2048, SD = 1024, SN = 128, SF = 3584;
constexpr int GU_WORDS = 128 * 2 * 4 * 2 * 8 * 4;
constexpr int DN_WORDS = 32 * 8 * 64 * 4;
#define SPIN_LOG 20
constexpr int SPIN_MAX = 1 << SPIN_LOG;

#define DEV __device__ __forceinline__
#define STAMP_DECL
#define STAMP(k)
#define STAMP_FLUSH(kn)
#define STAMP_ARG
#define STAMP_PAR
#define WAVE_SYNC() do { __builtin_amdgcn_fence(__ATOMIC_ACQ_REL, "wavefront"); __builtin_amdgcn_wave_barrier(); } while (0)
#define PIN(v) asm volatile("" : "+v"(v))
#define SCHED_FENCE() __builtin_amdgcn_sched_barrier(0)
#define MEM_FENCE() asm volatile("" ::: "memory")

DEV float bflo(u32 v) { return __uint_as_float(v << 16); }
DEV float bfhi(u32 v) { return __uint_as_float(v & 0xffff0000u); }
DEV f2 bfup(u32 v) { f2 r; r.x = bflo(v); r.y = bfhi(v); return r; }
DEV u32 pkbf(f2 v) { b2 t = __builtin_convertvector(v, b2); return __builtin_bit_cast(u32, t); }
DEV u32 pkbf(float a, float b) { f2 v; v.x = a; v.y = b; return pkbf(v); }
DEV u32 pkh(f2 v) { h2 t = __builtin_convertvector(v, h2); return __builtin_bit_cast(u32, t); }
DEV u32 pkh(float a, float b) { f2 v; v.x = a; v.y = b; return pkh(v); }
DEV u32 pkh_r(float a, float b) { PIN(a); return pkh(a, b); }
DEV float rbf(float a) { return __uint_as_float(pkbf(0.0f, a)); }
DEV f2 rbf2(f2 a) { f2 r; r.x = rbf(a.x); r.y = rbf(a.y); return r; }
#define KNOB(name, value) constexpr int name = value
KNOB(NREP, 16);
KNOB(WS_OFF, -8);
KNOB(CODE_B, 24000);
KNOB(W_A, 4);
KNOB(W_B, 2);
KNOB(W_E, 5);
KNOB(W_PACE, 6);
KNOB(W_PACE2, 4);
KNOB(WD_A, 2);
KNOB(G_DELAY, 12);
KNOB(G_SLEEP, 2);
KNOB(U_DELAY, 22);
KNOB(A_DELAY, 12);
KNOB(A_SLEEP, 4);
KNOB(B_DELAY, 20);
KNOB(B_SLEEP, 2);
KNOB(K_S1, 12);
KNOB(PACE, 3);
KNOB(E_UP, 1);
constexpr float LOG2E = 1.4426950408889634f;
DEV float silu_fast(float a) { return a * __builtin_amdgcn_rcpf(1.0f + __builtin_amdgcn_exp2f(-1.4426950408889634f * a)); }

template <int CTRL, int RMASK>
DEV float dpp_add(float v) {
    int t = __builtin_amdgcn_update_dpp(0, __float_as_int(v), CTRL, RMASK, 0xf, false);
    return v + __int_as_float(t);
}
DEV float wave_sum(float v) {
    v = dpp_add<0x111, 0xf>(v);
    v = dpp_add<0x112, 0xf>(v);
    v = dpp_add<0x114, 0xf>(v);
    v = dpp_add<0x118, 0xf>(v);
    v = dpp_add<0x142, 0xa>(v);
    v = dpp_add<0x143, 0xc>(v);
    return __int_as_float(__builtin_amdgcn_readlane(__float_as_int(v), 63));
}
DEV float fold4(float a, float b, float c, float d) {
    const auto s0 = __builtin_amdgcn_permlane32_swap(__float_as_uint(a), __float_as_uint(b), false, false);
    const auto s1 = __builtin_amdgcn_permlane32_swap(__float_as_uint(c), __float_as_uint(d), false, false);
    const u32 w0 = __float_as_uint(__uint_as_float(s0[0]) + __uint_as_float(s0[1]));
    const u32 w1 = __float_as_uint(__uint_as_float(s1[0]) + __uint_as_float(s1[1]));
    const auto s2 = __builtin_amdgcn_permlane16_swap(w0, w1, false, false);
    return __uint_as_float(s2[0]) + __uint_as_float(s2[1]);
}
DEV float row_sum(float v) {
    v = dpp_add<0x128, 0xf>(v);
    v = dpp_add<0x124, 0xf>(v);
    v = dpp_add<0x122, 0xf>(v);
    return dpp_add<0x121, 0xf>(v);
}
#define TOP2(ctl) "v_max_u32_dpp %2, %1, %1 " ctl "\n\tv_min_u32_dpp %3, %0, %0 " ctl "\n\tv_max_u32_dpp %0, %0, %0 " ctl "\n\tv_max_u32 %1, %2, %3\n\ts_nop 1\n\t"
DEV void wave_top2(u32& m1, u32& m2) {
    u32 t, s;
    asm volatile("s_nop 1\n\t"
                 TOP2("row_shr:1 row_mask:0xf bank_mask:0xf bound_ctrl:1") TOP2("row_shr:2 row_mask:0xf bank_mask:0xf bound_ctrl:1")
                 TOP2("row_shr:4 row_mask:0xf bank_mask:0xf bound_ctrl:1") TOP2("row_shr:8 row_mask:0xf bank_mask:0xf bound_ctrl:1")
                 TOP2("row_bcast:15 row_mask:0xa bank_mask:0xf") TOP2("row_bcast:31 row_mask:0xc bank_mask:0xf")
                 : "+v"(m1), "+v"(m2), "=&v"(t), "=&v"(s));
}
DEV void top2_of8(u32& m1, u32& m2) {
    u32 t, s;
    asm volatile("s_nop 1\n\t"
                 TOP2("row_shr:1 row_mask:0xf bank_mask:0xf bound_ctrl:1") TOP2("row_shr:2 row_mask:0xf bank_mask:0xf bound_ctrl:1")
                 TOP2("row_shr:4 row_mask:0xf bank_mask:0xf bound_ctrl:1")
                 : "+v"(m1), "+v"(m2), "=&v"(t), "=&v"(s));
}
DEV u32 med3u(u32 a, u32 b, u32 c) { return max(min(a, b), min(max(a, b), c)); }
DEV float dot2(u32 a, u32 b, float c) { return __builtin_amdgcn_fdot2_f32_bf16(__builtin_bit_cast(b2, a), __builtin_bit_cast(b2, b), c, false); }

template <int CTRL> DEV float dpp_mov(float v) { return __int_as_float(__builtin_amdgcn_mov_dpp(__float_as_int(v), CTRL, 0xf, 0xf, true)); }
DEV u32 bfi(u32 m, u32 a, u32 b) { u32 r; asm("v_bfi_b32 %0, %1, %2, %3" : "=v"(r) : "v"(m), "v"(a), "v"(b)); return r; }
DEV void gate4(u32 (&g)[4], u32 u0, u32 u1, u32 u2, u32 u3) {
#define LO "dst_sel:WORD_0 dst_unused:UNUSED_PRESERVE src0_sel:WORD_0\n\t"
#define HI "dst_sel:WORD_1 dst_unused:UNUSED_PRESERVE src0_sel:WORD_1\n\t"
    asm volatile("v_exp_f16_sdwa %0, -%4 " LO "v_exp_f16_sdwa %1, -%5 " LO "v_exp_f16_sdwa %2, -%6 " LO "v_exp_f16_sdwa %3, -%7 " LO
                 "v_exp_f16_sdwa %0, -%4 " HI "v_exp_f16_sdwa %1, -%5 " HI "v_exp_f16_sdwa %2, -%6 " HI "v_exp_f16_sdwa %3, -%7 " HI
                 "v_pk_add_f16 %0, %0, 1.0 op_sel_hi:[1,0]\n\tv_pk_add_f16 %1, %1, 1.0 op_sel_hi:[1,0]\n\tv_pk_add_f16 %2, %2, 1.0 op_sel_hi:[1,0]\n\tv_pk_add_f16 %3, %3, 1.0 op_sel_hi:[1,0]\n\t"
                 "v_rcp_f16_sdwa %0, %0 " LO "v_rcp_f16_sdwa %1, %1 " LO "v_rcp_f16_sdwa %2, %2 " LO "v_rcp_f16_sdwa %3, %3 " LO
                 "v_rcp_f16_sdwa %0, %0 " HI "v_rcp_f16_sdwa %1, %1 " HI "v_rcp_f16_sdwa %2, %2 " HI "v_rcp_f16_sdwa %3, %3 " HI
                 "v_pk_mul_f16 %0, %0, %4\n\tv_pk_mul_f16 %1, %1, %5\n\tv_pk_mul_f16 %2, %2, %6\n\tv_pk_mul_f16 %3, %3, %7"
                 : "=&v"(g[0]), "=&v"(g[1]), "=&v"(g[2]), "=&v"(g[3]) : "v"(u0), "v"(u1), "v"(u2), "v"(u3));
#undef LO
#undef HI
}
struct cplx { f2 r, i; };
DEV cplx cexp2(f2 ar, f2 ai, float p) {
    cplx z;
    const f2 am = ar * p, rv = ai * p;
    f2 mag, c, s;
#pragma unroll
    for (int k = 0; k < 2; ++k) {
        mag[k] = __builtin_amdgcn_exp2f(am[k]);
        const float rev = __builtin_amdgcn_fractf(rv[k]);
        c[k] = __builtin_amdgcn_cosf(rev);
        s[k] = __builtin_amdgcn_sinf(rev);
    }
    z.r = mag * c; z.i = mag * s;
    return z;
}
DEV float exp_acc(float x) {
    x = __builtin_amdgcn_fmed3f(x, -150.0f, 150.0f);
    const float t = x * 1.4426950408889634f;
    const float r = __builtin_fmaf(x, 1.925963033500011e-08f, __builtin_fmaf(x, 1.4426950408889634f, -t));
    return __builtin_amdgcn_exp2f(t) * __builtin_fmaf(r, 0.6931471805599453f, 1.0f);
}
DEV f4 mfma16(h8 a, h8 b, f4 c) { return __builtin_amdgcn_mfma_f32_16x16x32_f16(a, b, c, 0, 0, 0); }
DEV f4 mfma16b(u4 a, u4 b, f4 c) {
    return __builtin_amdgcn_mfma_f32_16x16x32_bf16(__builtin_bit_cast(b8, a), __builtin_bit_cast(b8, b), c, 0, 0, 0);
}
typedef __attribute__((ext_vector_type(16))) __bf16 b16v;
DEV f4 smfma(u4 a, u4 b0, u4 b1, f4 c, u32 idx) {
    return __builtin_amdgcn_smfmac_f32_16x16x64_bf16(__builtin_bit_cast(b8, a), __builtin_bit_cast(b16v, __builtin_shufflevector(b0, b1, 0, 1, 2, 3, 4, 5, 6, 7)), c, (int)idx, 0, 0);
}
DEV void lds_add1(u32* p) {
    const u32 a = (u32)(uintptr_t)(__attribute__((address_space(3))) u32*)p, one = 1u;
    asm volatile("s_mov_b64 exec, 1\n\tds_add_u32 %0, %1\n\ts_mov_b64 exec, -1" :: "v"(a), "v"(one) : "memory");
}
template <int SLP = 1>
DEV void lds_wait(u32* cnt, u32 n) {
    while (__hip_atomic_load(cnt, __ATOMIC_RELAXED, __HIP_MEMORY_SCOPE_WORKGROUP) < n) __builtin_amdgcn_s_sleep(SLP);
    __builtin_amdgcn_fence(__ATOMIC_ACQUIRE, "wavefront");
    __builtin_amdgcn_wave_barrier();
}

constexpr int KREC = 1024;
constexpr int R_KA = 0, ZA = 543, R_K2 = 560, R_K3 = 812, R_KP = 972;
constexpr int STG_ROW = 544;
constexpr int STG_BUF = 37 * 256;
constexpr int REC_B = KREC * 2 + 64;
constexpr int REC_SZ = 2 * KREC * 2 + 128;
constexpr int UPAD = 256;
constexpr int C_U = REC_SZ;
constexpr int CH_SZ = C_U + (UPAD + SL) * 2 + 16;
static_assert(CH_SZ % 32 == 16, "bank offset of the channel regions");
constexpr int L_STG = 0;
constexpr int L_CH = L_STG + 8 * STG_BUF;
constexpr int L_BOX = L_CH + 8 * CH_SZ;
constexpr int XN_S = 512 + 64, IMG = 4 * XN_S;
constexpr int L_Z = L_BOX + 5 * 1024;
constexpr int L_XN = L_Z, Z_SZ = 4 * IMG;
constexpr int L_CNT = L_Z + Z_SZ;
constexpr int L_P12 = L_CNT + 128, P12_S = 80;
constexpr int L_P3 = L_P12 + 8 * P12_S, P3_S = 48;
constexpr int L_NUL = L_P3 + 8 * P3_S;
constexpr int L_TOTAL = L_NUL + 256;
static_assert(L_TOTAL <= 160 * 1024 && Z_SZ % 16 == 0, "LDS");

DEV h8 box_frag(int al, int q, int off, int hw) {
    const u32 m = (u32)((((2ull << (2 * hw)) - 1ull) << (al - 8 * q + off - hw + 32)) >> 32) & 0xffu;
    const u32 t = m | (m << 15);
    u4 r;
#pragma unroll
    for (int i = 0; i < 4; ++i) r[i] = ((t >> (2 * i)) & 0x10001u) * 0x3c00u;
    return __builtin_bit_cast(h8, r);
}
DEV u4 tag4(f4 v, u32 vtag) {
    u4 b = __builtin_bit_cast(u4, v);
#pragma unroll
    for (int i = 0; i < 4; ++i) b[i] = __builtin_amdgcn_perm(b[i], vtag, 0x07060500u + i);
    return b;
}
DEV u32 tagof(u4 v) { return __builtin_amdgcn_perm(v.y, v.x, 0x0c0c0400u) | __builtin_amdgcn_perm(v.w, v.z, 0x04000c0cu); }
DEV u4 untag4(u4 v) { return v & 0xffffff00u; }
template <int N, int SLEEP, class LD, class BAD>
DEV void poll_pieces(u4 (&v)[N], LD ld, BAD bad) {
#pragma unroll
    for (int p = 0; p < N; ++p) v[p] = ld(p);
    MEM_FENCE(); SCHED_FENCE();
    {
        bool any = false;
#pragma unroll
        for (int p = 0; p < N; ++p) any |= bad(v[p]);
        if (__builtin_expect(!__any(any), 1)) return;
    }
    u32 miss = 0;
#pragma unroll
    for (int p = 0; p < N; ++p) if (!__all(!bad(v[p]))) miss |= 1u << p;
#pragma unroll 1
    for (int spin = 0; miss != 0 && spin < SPIN_MAX; ++spin) {
        MEM_FENCE();
        __builtin_amdgcn_s_sleep(SLEEP);
#pragma unroll
        for (int p = 0; p < N; ++p) if ((miss >> p) & 1u) v[p] = ld(p);
#pragma unroll
        for (int p = 0; p < N; ++p) if (((miss >> p) & 1u) && __all(!bad(v[p]))) miss &= ~(1u << p);
        __builtin_amdgcn_s_waitcnt(0x0f70);
    }
}
struct hcx { h2 r, i; };
DEV hcx toh(cplx v) { hcx z; z.r = __builtin_convertvector(v.r, h2); z.i = __builtin_convertvector(v.i, h2); return z; }
DEV hcx hmul(hcx a, hcx b) { hcx z; z.r = __builtin_elementwise_fma(a.r, b.r, -(a.i * b.i)); z.i = __builtin_elementwise_fma(a.r, b.i, a.i * b.r); return z; }
DEV hcx hmul_conj(hcx g, hcx e) { hcx z; z.r = __builtin_elementwise_fma(g.r, e.r, g.i * e.i); z.i = __builtin_elementwise_fma(g.i, e.r, -(g.r * e.i)); return z; }
DEV cplx csq(cplx a) { cplx z; z.r = a.r * a.r - a.i * a.i; z.i = (a.r + a.r) * a.i; return z; }
template <int ROW>
DEV void stg_put(hcx v) {
    asm volatile("ds_write_addtid_b32 %0 offset:%2\n\tds_write_addtid_b32 %1 offset:%3"
                 :: "v"(v.r), "v"(v.i), "n"(ROW * STG_ROW), "n"(ROW * STG_ROW + 256) : "memory");
}
DEV void lds_add1a(u32 a) {
    const u32 one = 1u;
    asm volatile("s_mov_b64 exec, 1\n\tds_add_u32 %0, %1\n\ts_mov_b64 exec, -1" :: "v"(a), "v"(one) : "memory");
}
#define CONV_ASM_0 \
    "v_add_u32 %7, -64, %0\n\tds_read2_b32 v[8:9], %0 offset0:128 offset1:129\n\tds_read2_b32 v[10:11], %0 offset0:130 offset1:131\n\tds_read2_b32 v[12:13], %0 offset0:0 offset1:1\n\t" \
    "ds_read2_b32 v[14:15], %0 offset0:2 offset1:3\n\tds_read2_b32 v[16:17], %0 offset0:144 offset1:145\n\tds_read2_b32 v[18:19], %0 offset0:146 offset1:147\n\t" \
    "ds_read2_b32 v[20:21], %0 offset0:16 offset1:17\n\tds_read2_b32 v[22:23], %0 offset0:18 offset1:19\n\tds_read2_b32 v[24:25], %0 offset0:160 offset1:161\n\t" \
    "ds_read2_b32 v[26:27], %0 offset0:162 offset1:163\n\tds_read2_b32 v[28:29], %0 offset0:32 offset1:33\n\tds_read2_b32 v[30:31], %0 offset0:34 offset1:35\n\t" \
    "ds_read2_b32 v[32:33], %0 offset0:176 offset1:177\n\tds_read2_b32 v[34:35], %0 offset0:178 offset1:179\n\tds_read2_b32 v[36:37], %0 offset0:48 offset1:49\n\t" \
    "ds_read2_b32 v[38:39], %0 offset0:50 offset1:51\n\tds_read2_b32 v[40:41], %0 offset0:192 offset1:193\n\tds_read2_b32 v[42:43], %0 offset0:194 offset1:195\n\t" \
    "ds_read2_b32 v[44:45], %0 offset0:64 offset1:65\n\tds_read2_b32 v[46:47], %0 offset0:66 offset1:67\n\tds_read2_b32 v[48:49], %0 offset0:208 offset1:209\n\t" \
    "ds_read2_b32 v[50:51], %0 offset0:210 offset1:211\n\tds_read2_b32 v[52:53], %0 offset0:80 offset1:81\n\tds_read2_b32 v[54:55], %0 offset0:82 offset1:83\n\t" \
    "ds_read2_b32 v[56:57], %0 offset0:224 offset1:225\n\tds_read2_b32 v[58:59], %0 offset0:226 offset1:227\n\tds_read2_b32 v[60:61], %0 offset0:96 offset1:97\n\t" \
    "ds_read2_b32 v[62:63], %0 offset0:98 offset1:99\n\tds_read2_b32 v[64:65], %0 offset0:240 offset1:241\n\tds_read2_b32 v[66:67], %0 offset0:242 offset1:243\n\t" \
    "ds_read2_b32 v[68:69], %0 offset0:112 offset1:113\n\tds_read2_b32 v[70:71], %0 offset0:114 offset1:115\n\tds_read2_b32 v[72:73], %7 offset0:0 offset1:1\n\t" \
    "ds_read2_b32 v[74:75], %7 offset0:2 offset1:3\n\t" \
    CONV_WAIT \
    "ds_read_b128 v[76:79], %1 offset:32\n\tds_read_b128 v[80:83], %1 offset:96\n\tds_read_b128 v[84:87], %1 offset:160\n\tds_read_b128 v[88:91], %1 offset:224\n\t" \
    "s_waitcnt lgkmcnt(3)\n\tv_mfma_f32_16x16x32_f16 v[92:95], v[8:11], v[76:79], 0\n\tds_read_b128 v[96:99], %1 offset:288\n\tv_mfma_f32_16x16x32_f16 v[100:103], v[12:15], v[76:79], 0\n\t" \
    "s_waitcnt lgkmcnt(3)\n\tv_mfma_f32_16x16x32_f16 v[92:95], v[16:19], v[80:83], v[92:95]\n\tds_read_b128 v[104:107], %1 offset:352\n\tv_mfma_f32_16x16x32_f16 v[100:103], v[20:23], v[80:83], v[100:103]\n\t" \
    "s_waitcnt lgkmcnt(2)\n\tv_mfma_f32_16x16x32_f16 v[92:95], v[24:27], v[84:87], v[92:95]\n\tds_read_b128 v[108:111], %1 offset:416\n\tv_mfma_f32_16x16x32_f16 v[100:103], v[28:31], v[84:87], v[100:103]\n\t" \
    "v_mfma_f32_16x16x32_f16 v[92:95], v[32:35], v[88:91], v[92:95]\n\tds_read_b128 v[112:115], %1 offset:480\n\tv_mfma_f32_16x16x32_f16 v[100:103], v[36:39], v[88:91], v[100:103]\n\t" \
    "s_waitcnt lgkmcnt(2)\n\tv_mfma_f32_16x16x32_f16 v[92:95], v[40:43], v[96:99], v[92:95]\n\tds_read_b128 v[116:119], %1 offset:544\n\tv_mfma_f32_16x16x32_f16 v[100:103], v[44:47], v[96:99], v[100:103]\n\t" \
    "ds_read_b128 v[120:123], %1 offset:608\n\tv_mfma_f32_16x16x32_f16 v[92:95], v[48:51], v[104:107], v[92:95]\n\tds_read_b128 v[124:127], %1 offset:2528\n\t" \
    "v_mfma_f32_16x16x32_f16 v[100:103], v[52:55], v[104:107], v[100:103]\n\tds_read_b128 v[76:79], %1 offset:672\n\ts_waitcnt lgkmcnt(4)\n\tv_mfma_f32_16x16x32_f16 v[92:95], v[56:59], v[108:111], v[92:95]\n\t" \
    "ds_read_b128 v[80:83], %1 offset:2592\n\tv_mfma_f32_16x16x32_f16 v[100:103], v[60:63], v[108:111], v[100:103]\n\tds_read_b128 v[84:87], %1 offset:736\n\t" \
    "v_mfma_f32_16x16x32_f16 v[92:95], v[64:67], v[112:115], v[92:95]\n\tds_read_b128 v[88:91], %1 offset:2656\n\tv_mfma_f32_16x16x32_f16 v[100:103], v[68:71], v[112:115], v[100:103]\n\t" \
    "ds_read_b128 v[96:99], %1 offset:800\n\tds_read_b128 v[104:107], %1 offset:2784\n\ts_waitcnt lgkmcnt(5)\n\tv_mfma_f32_16x16x32_f16 v[100:103], v[8:11], v[116:119], v[100:103]\n\t" \
    "ds_read_b128 v[108:111], %1 offset:2720\n\tds_read_b128 v[112:115], %1 offset:864\n\tv_mfma_f32_16x16x32_f16 v[100:103], v[16:19], v[120:123], v[100:103]\n\t" \
    "v_mul_f32 v92, %8, v92\n\tv_mul_f32 v93, %8, v93\n\tv_mfma_f32_16x16x32_f16 v[116:119], v[72:75], v[124:127], 0\n\tv_mul_f32 v94, %8, v94\n\tv_mul_f32 v95, %8, v95\n\t" \
    "v_mfma_f32_16x16x32_f16 v[100:103], v[24:27], v[76:79], v[100:103]\n\tds_write_b128 %2, v[92:95] offset:0\n\tds_write_b8 %6, %6 offset:0\n\t" \
    CONV_P0 \
    "ds_read_b128 v[120:123], %1 offset:928\n\ts_waitcnt lgkmcnt(6)\n\tv_mfma_f32_16x16x32_f16 v[116:119], v[12:15], v[80:83], v[116:119]\n\tds_read_b128 v[124:127], %1 offset:2848\n\t" \
    "v_mfma_f32_16x16x32_f16 v[100:103], v[32:35], v[84:87], v[100:103]\n\tds_read_b128 v[76:79], %1 offset:992\n\tv_mfma_f32_16x16x32_f16 v[116:119], v[20:23], v[88:91], v[116:119]\n\t" \
    "ds_read_b128 v[80:83], %1 offset:2912\n\tv_mfma_f32_16x16x32_f16 v[100:103], v[40:43], v[96:99], v[100:103]\n\tds_read_b128 v[92:95], %1 offset:2976\n\t" \
    "s_waitcnt lgkmcnt(4)\n\tv_mfma_f32_16x16x32_f16 v[116:119], v[28:31], v[108:111], v[116:119]\n\tds_read_b128 v[84:87], %1 offset:3040\n\tv_mfma_f32_16x16x32_f16 v[100:103], v[48:51], v[112:115], v[100:103]\n\t" \
    "v_mfma_f32_16x16x32_f16 v[116:119], v[36:39], v[104:107], v[116:119]\n\tds_read_b128 v[88:91], %1 offset:3104\n\tv_mfma_f32_16x16x32_f16 v[100:103], v[56:59], v[120:123], v[100:103]\n\t" \
    "s_waitcnt lgkmcnt(2)\n\tv_mfma_f32_16x16x32_f16 v[116:119], v[44:47], v[124:127], v[116:119]\n\tds_read_b128 v[96:99], %1 offset:3168\n\tv_mfma_f32_16x16x32_f16 v[100:103], v[64:67], v[76:79], v[100:103]\n\t" \
    "v_mfma_f32_16x16x32_f16 v[116:119], v[52:55], v[80:83], v[116:119]\n\tds_read_b128 v[108:111], %1 offset:3232\n\tv_mfma_f32_16x16x32_f16 v[116:119], v[60:63], v[92:95], v[116:119]\n\t" \
    "ds_read_b128 v[104:107], %1 offset:3296\n\tds_read_b128 v[112:115], %1 offset:3360\n\ts_waitcnt lgkmcnt(4)\n\tv_mfma_f32_16x16x32_f16 v[116:119], v[68:71], v[84:87], v[116:119]\n\t" \
    "s_nop 0\n\tv_mul_f32 v100, %8, v100\n\tv_mul_f32 v101, %8, v101\n\tv_mfma_f32_16x16x32_f16 v[120:123], v[72:75], v[84:87], 0\n\tv_mul_f32 v102, %8, v102\n\t" \
    "v_mul_f32 v103, %8, v103\n\tv_mfma_f32_16x16x32_f16 v[116:119], v[8:11], v[88:91], v[116:119]\n\tds_write_b128 %2, v[100:103] offset:1024\n\tds_write_b8 %6, %6 offset:8\n\t" \
    CONV_P1 \
    "v_mfma_f32_16x16x32_f16 v[120:123], v[12:15], v[88:91], v[120:123]\n\ts_waitcnt lgkmcnt(4)\n\tv_mfma_f32_16x16x32_f16 v[116:119], v[16:19], v[96:99], v[116:119]\n\t" \
    "ds_read_b128 v[124:127], %1 offset:3424\n\tv_mfma_f32_16x16x32_f16 v[120:123], v[20:23], v[96:99], v[120:123]\n\tv_mfma_f32_16x16x32_f16 v[116:119], v[24:27], v[108:111], v[116:119]\n\t" \
    "ds_read_b128 v[76:79], %1 offset:3488\n\tv_mfma_f32_16x16x32_f16 v[120:123], v[28:31], v[108:111], v[120:123]\n\ts_waitcnt lgkmcnt(4)\n\tv_mfma_f32_16x16x32_f16 v[116:119], v[32:35], v[104:107], v[116:119]\n\t" \
    "ds_read_b128 v[80:83], %1 offset:3552\n\tv_mfma_f32_16x16x32_f16 v[120:123], v[36:39], v[104:107], v[120:123]\n\tv_mfma_f32_16x16x32_f16 v[116:119], v[40:43], v[112:115], v[116:119]\n\t" \
    "ds_read_b128 v[92:95], %1 offset:3616\n\tv_mfma_f32_16x16x32_f16 v[120:123], v[44:47], v[112:115], v[120:123]\n\tds_read_b128 v[12:15], %1 offset:3680\n\t" \
    "s_waitcnt lgkmcnt(3)\n\tv_mfma_f32_16x16x32_f16 v[116:119], v[48:51], v[124:127], v[116:119]\n\tds_read_b128 v[72:75], %1 offset:3744\n\tv_mfma_f32_16x16x32_f16 v[120:123], v[52:55], v[124:127], v[120:123]\n\t" \
    "ds_read_b128 v[100:103], %1 offset:3808\n\tv_mfma_f32_16x16x32_f16 v[116:119], v[56:59], v[76:79], v[116:119]\n\tds_read_b128 v[84:87], %1 offset:464\n\t" \
    "ds_read_b128 v[88:91], %3 offset:1024\n\tv_mfma_f32_16x16x32_f16 v[120:123], v[60:63], v[76:79], v[120:123]\n\tds_read_b128 v[20:23], %1 offset:480\n\t" \
    "ds_read_b128 v[96:99], %3 offset:4096\n\ts_waitcnt lgkmcnt(6)\n\tv_mfma_f32_16x16x32_f16 v[116:119], v[64:67], v[80:83], v[116:119]\n\tds_read_b128 v[28:31], %1 offset:3872\n\t" \
    "v_mfma_f32_16x16x32_f16 v[120:123], v[68:71], v[80:83], v[120:123]\n\tds_read_b128 v[108:111], %1 offset:976\n\tv_mfma_f32_16x16x32_f16 v[120:123], v[8:11], v[92:95], v[120:123]\n\t" \
    "ds_read_b128 v[36:39], %1 offset:992\n\tv_mfma_f32_16x16x32_f16 v[120:123], v[16:19], v[12:15], v[120:123]\n\tds_read_b128 v[104:107], %1 offset:3936\n\t" \
    "s_waitcnt lgkmcnt(4)\n\tv_mfma_f32_16x16x32_f16 v[120:123], v[24:27], v[72:75], v[120:123]\n\tds_read_b128 v[44:47], %1 offset:1488\n\tv_mfma_f32_16x16x32_f16 v[120:123], v[32:35], v[100:103], v[120:123]\n\t" \
    "ds_read_b128 v[112:115], %1 offset:1504\n\tv_mfma_f32_16x16x32_f16 v[52:55], v[84:87], v[88:91], 0\n\tds_read_b128 v[124:127], %1 offset:4000\n\tv_mfma_f32_16x16x32_f16 v[60:63], v[20:23], v[96:99], 0\n\t" \
    "ds_read_b128 v[76:79], %3 offset:0\n\ts_waitcnt lgkmcnt(4)\n\tv_mfma_f32_16x16x32_f16 v[120:123], v[40:43], v[28:31], v[120:123]\n\tds_read_b128 v[68:71], %3 offset:2048\n\t" \
    "v_mfma_f32_16x16x32_f16 v[80:83], v[108:111], v[88:91], 0\n\tds_read_b128 v[8:11], %3 offset:3072\n\tds_read_b128 v[92:95], %1 offset:4064\n\tv_mfma_f32_16x16x32_f16 v[16:19], v[36:39], v[96:99], 0\n\t" \
    "v_cvt_pk_f16_f32 v12, v52, v60\n\tv_cvt_pk_f16_f32 v13, v53, v61\n\tv_mfma_f32_16x16x32_f16 v[120:123], v[48:51], v[104:107], v[120:123]\n\tv_cvt_pk_f16_f32 v14, v54, v62\n\t" \
    "v_cvt_pk_f16_f32 v15, v55, v63\n\ts_waitcnt lgkmcnt(3)\n\tv_mfma_f32_16x16x32_f16 v[24:27], v[44:47], v[88:91], 0\n\tds_read2_b32 v[72:73], %5 offset0:16 offset1:17\n\t" \
    "ds_read2_b32 v[74:75], %5 offset0:18 offset1:19\n\tv_mfma_f32_16x16x32_f16 v[32:35], v[112:115], v[96:99], 0\n\tv_cvt_pk_f16_f32 v100, v80, v16\n\tv_cvt_pk_f16_f32 v101, v81, v17\n\t" \
    "ds_read2_b32 v[84:85], %5 offset0:0 offset1:1\n\tds_read2_b32 v[86:87], %5 offset0:2 offset1:3\n\tv_mfma_f32_16x16x32_f16 v[120:123], v[56:59], v[124:127], v[120:123]\n\t" \
    "v_cvt_pk_f16_f32 v102, v82, v18\n\tv_cvt_pk_f16_f32 v103, v83, v19\n\tv_add_u32 %7, 1880, %0\n\tds_read2_b32 v[20:21], %7 offset0:0 offset1:1\n\tds_read2_b32 v[22:23], %7 offset0:2 offset1:3\n\t" \
    "v_mfma_f32_16x16x32_f16 v[40:43], v[44:47], v[76:79], 0\n\tds_read_b128 v[28:31], %1 offset:2000\n\tds_read_b128 v[108:111], %1 offset:2016\n\ts_waitcnt lgkmcnt(8)\n\t" \
    "v_mfma_f32_16x16x32_f16 v[36:39], v[112:115], v[68:71], 0\n\tv_cvt_pk_f16_f32 v48, v24, v32\n\tv_cvt_pk_f16_f32 v49, v25, v33\n\tv_mfma_f32_16x16x32_f16 v[104:107], v[112:115], v[76:79], 0\n\t" \
    "v_cvt_pk_f16_f32 v50, v26, v34\n\tv_cvt_pk_f16_f32 v51, v27, v35\n\tv_mfma_f32_16x16x32_f16 v[52:55], v[112:115], v[8:11], 0\n\tds_read2_b32 v[60:61], %5 offset0:32 offset1:33\n\t" \
    "ds_read2_b32 v[62:63], %5 offset0:34 offset1:35\n\tv_mfma_f32_16x16x32_f16 v[120:123], v[64:67], v[92:95], v[120:123]\n\tv_cvt_pk_f16_f32 v56, v40, v36\n\t" \
    "v_cvt_pk_f16_f32 v58, v41, v37\n\ts_waitcnt lgkmcnt(3)\n\tv_mfma_f32_16x16x32_f16 v[116:119], v[12:15], v[72:75], v[116:119]\n\tv_cvt_pk_f16_f32 v124, v42, v38\n\t" \
    "v_cvt_pk_f16_f32 v126, v43, v39\n\tv_mfma_f32_16x16x32_f16 v[120:123], v[12:15], v[84:87], v[120:123]\n\tv_cvt_pk_f16_f32 v57, v104, v52\n\tv_cvt_pk_f16_f32 v59, v105, v53\n\t" \
    "ds_read2_b32 v[80:81], %5 offset0:48 offset1:49\n\tds_read2_b32 v[82:83], %5 offset0:50 offset1:51\n\tv_mfma_f32_16x16x32_f16 v[116:119], v[20:23], v[112:115], v[116:119]\n\t" \
    "v_cvt_pk_f16_f32 v125, v106, v54\n\tv_cvt_pk_f16_f32 v127, v107, v55\n\tv_mfma_f32_16x16x32_f16 v[16:19], v[28:31], v[76:79], 0\n\tds_read_b128 v[44:47], %1 offset:2512\n\t" \
    "s_waitcnt lgkmcnt(3)\n\tv_mfma_f32_16x16x32_f16 v[24:27], v[108:111], v[68:71], 0\n\tds_read_b128 v[32:35], %1 offset:2528\n\tv_mfma_f32_16x16x32_f16 v[64:67], v[108:111], v[76:79], 0\n\t" \
    "v_mfma_f32_16x16x32_f16 v[92:95], v[108:111], v[8:11], 0\n\tds_read2_b64 v[84:87], %4 offset0:0 offset1:1\n\tds_read2_b64 v[40:43], %4 offset0:2 offset1:3\n\t" \
    "v_mfma_f32_16x16x32_f16 v[116:119], v[100:103], v[60:63], v[116:119]\n\ts_nop 1\n\tv_cvt_pk_f16_f32 v36, v16, v24\n\tv_cvt_pk_f16_f32 v38, v17, v25\n\t" \
    "v_mfma_f32_16x16x32_f16 v[120:123], v[100:103], v[72:75], v[120:123]\n\tv_cvt_pk_f16_f32 v112, v18, v26\n\tv_cvt_pk_f16_f32 v114, v19, v27\n\ts_waitcnt lgkmcnt(2)\n\t" \
    "v_mfma_f32_16x16x32_f16 v[116:119], v[48:51], v[80:83], v[116:119]\n\tv_cvt_pk_f16_f32 v37, v64, v92\n\tv_cvt_pk_f16_f32 v39, v65, v93\n\tv_mfma_f32_16x16x32_f16 v[120:123], v[48:51], v[60:63], v[120:123]\n\t" \
    "v_cvt_pk_f16_f32 v113, v66, v94\n\tv_cvt_pk_f16_f32 v115, v67, v95\n\tv_mfma_f32_16x16x32_f16 v[12:15], v[44:47], v[76:79], 0\n\tv_mfma_f32_16x16x32_f16 v[104:107], v[32:35], v[68:71], 0\n\t" \
    "ds_read2_b64 v[52:55], %4 offset0:16 offset1:17\n\tv_mfma_f32_16x16x32_f16 v[72:75], v[32:35], v[76:79], 0\n\tds_read2_b64 v[100:103], %4 offset0:18 offset1:19\n\t" \
    "v_mfma_f32_16x16x32_f16 v[60:63], v[32:35], v[8:11], 0\n\tds_read_b128 v[16:19], %1 offset:3024\n\tds_read_b128 v[24:27], %1 offset:3040\n\ts_waitcnt lgkmcnt(4)\n\t" \
    "v_mfma_f32_16x16x32_f16 v[116:119], v[56:59], v[84:87], v[116:119]\n\tv_cvt_pk_f16_f32 v64, v12, v104\n\tv_cvt_pk_f16_f32 v66, v13, v105\n\tv_mfma_f32_16x16x32_f16 v[116:119], v[124:127], v[40:43], v[116:119]\n\t" \
    "v_cvt_pk_f16_f32 v92, v14, v106\n\tv_cvt_pk_f16_f32 v94, v15, v107\n\tv_mfma_f32_16x16x32_f16 v[48:51], v[28:31], v[88:91], 0\n\tv_cvt_pk_f16_f32 v65, v72, v60\n\t" \
    "v_cvt_pk_f16_f32 v67, v73, v61\n\tv_mfma_f32_16x16x32_f16 v[44:47], v[108:111], v[96:99], 0\n\tv_cvt_pk_f16_f32 v93, v74, v62\n\tv_cvt_pk_f16_f32 v95, v75, v63\n\t" \
    "ds_read2_b64 v[32:35], %4 offset0:32 offset1:33\n\tv_mfma_f32_16x16x32_f16 v[120:123], v[20:23], v[108:111], v[120:123]\n\tds_read2_b64 v[56:59], %4 offset0:34 offset1:35\n\t" \
    "s_waitcnt lgkmcnt(2)\n\tv_mfma_f32_16x16x32_f16 v[116:119], v[36:39], v[52:55], v[116:119]\n\tv_mfma_f32_16x16x32_f16 v[116:119], v[112:115], v[100:103], v[116:119]\n\t" \
    "v_cvt_pk_f16_f32 v12, v48, v44\n\tv_cvt_pk_f16_f32 v13, v49, v45\n\tv_mfma_f32_16x16x32_f16 v[104:107], v[16:19], v[76:79], 0\n\tv_cvt_pk_f16_f32 v14, v50, v46\n\t" \
    "v_cvt_pk_f16_f32 v15, v51, v47\n\tv_mfma_f32_16x16x32_f16 v[88:91], v[24:27], v[68:71], 0\n\tv_mfma_f32_16x16x32_f16 v[28:31], v[24:27], v[76:79], 0\n\t" \
    "v_mfma_f32_16x16x32_f16 v[124:127], v[24:27], v[8:11], 0\n\ts_waitcnt lgkmcnt(0)\n\tv_mfma_f32_16x16x32_f16 v[116:119], v[64:67], v[32:35], v[116:119]\n\t" \
    "s_nop 3\n\tv_cvt_pk_f16_f32 v96, v104, v88\n\tv_cvt_pk_f16_f32 v98, v105, v89\n\tv_mfma_f32_16x16x32_f16 v[116:119], v[92:95], v[56:59], v[116:119]\n\t" \
    "v_cvt_pk_f16_f32 v20, v106, v90\n\tv_cvt_pk_f16_f32 v22, v107, v91\n\tv_mfma_f32_16x16x32_f16 v[120:123], v[36:39], v[84:87], v[120:123]\n\tv_cvt_pk_f16_f32 v97, v28, v124\n\t" \
    "v_cvt_pk_f16_f32 v99, v29, v125\n\tv_mfma_f32_16x16x32_f16 v[120:123], v[112:115], v[40:43], v[120:123]\n\tv_cvt_pk_f16_f32 v21, v30, v126\n\tv_cvt_pk_f16_f32 v23, v31, v127\n\t" \
    "v_mfma_f32_16x16x32_f16 v[120:123], v[12:15], v[80:83], v[120:123]\n\tv_mul_f32 v116, %8, v116\n\tv_mul_f32 v117, %8, v117\n\tv_mfma_f32_16x16x32_f16 v[120:123], v[64:67], v[52:55], v[120:123]\n\t" \
    "v_mul_f32 v118, %8, v118\n\tv_mul_f32 v119, %8, v119\n\tv_mfma_f32_16x16x32_f16 v[120:123], v[92:95], v[100:103], v[120:123]\n\tds_write_b128 %2, v[116:119] offset:2048\n\t" \
    "ds_write_b8 %6, %6 offset:16\n\t" \
    CONV_P2 \
    "v_mfma_f32_16x16x32_f16 v[120:123], v[96:99], v[32:35], v[120:123]\n\tv_mfma_f32_16x16x32_f16 v[120:123], v[20:23], v[56:59], v[120:123]\n\ts_nop 7\n\t" \
    "v_mul_f32 v120, %8, v120\n\tv_mul_f32 v121, %8, v121\n\tv_mul_f32 v122, %8, v122\n\tv_mul_f32 v123, %8, v123\n\tds_write_b128 %2, v[120:123] offset:3072\n\t" \
    "ds_write_b8 %6, %6 offset:24\n\t"
#define CONV_ASM_1 \
    "v_add_u32 %7, -64, %0\n\tds_read_b128 v[8:11], %3 offset:0\n\tds_read_b128 v[12:15], %3 offset:2048\n\tds_read_b128 v[16:19], %3 offset:3072\n\tds_read2_b64 v[24:27], %4 offset0:32 offset1:33\n\t" \
    "ds_read2_b64 v[36:39], %4 offset0:34 offset1:35\n\tds_read2_b64 v[28:31], %4 offset0:16 offset1:17\n\tds_read2_b64 v[40:43], %4 offset0:18 offset1:19\n\t" \
    "ds_read2_b64 v[32:35], %4 offset0:0 offset1:1\n\tds_read2_b64 v[44:47], %4 offset0:2 offset1:3\n\tds_read2_b32 v[20:21], %7 offset0:0 offset1:1\n\tds_read2_b32 v[22:23], %7 offset0:2 offset1:3\n\t" \
    "ds_read2_b32 v[48:49], %0 offset0:0 offset1:1\n\tds_read2_b32 v[50:51], %0 offset0:2 offset1:3\n\tds_read2_b32 v[52:53], %0 offset0:16 offset1:17\n\tds_read2_b32 v[54:55], %0 offset0:18 offset1:19\n\t" \
    "ds_read2_b32 v[56:57], %0 offset0:32 offset1:33\n\tds_read2_b32 v[58:59], %0 offset0:34 offset1:35\n\tds_read2_b32 v[60:61], %0 offset0:48 offset1:49\n\t" \
    "ds_read2_b32 v[62:63], %0 offset0:50 offset1:51\n\tds_read2_b32 v[64:65], %0 offset0:64 offset1:65\n\tds_read2_b32 v[66:67], %0 offset0:66 offset1:67\n\t" \
    "ds_read2_b32 v[68:69], %0 offset0:80 offset1:81\n\tds_read2_b32 v[70:71], %0 offset0:82 offset1:83\n\tds_read2_b32 v[72:73], %0 offset0:96 offset1:97\n\t" \
    "ds_read2_b32 v[74:75], %0 offset0:98 offset1:99\n\tds_read2_b32 v[76:77], %0 offset0:112 offset1:113\n\tds_read2_b32 v[78:79], %0 offset0:114 offset1:115\n\t" \
    "ds_read2_b32 v[80:81], %0 offset0:128 offset1:129\n\tds_read2_b32 v[82:83], %0 offset0:130 offset1:131\n\tds_read2_b32 v[84:85], %0 offset0:144 offset1:145\n\t" \
    "ds_read2_b32 v[86:87], %0 offset0:146 offset1:147\n\tds_read2_b32 v[88:89], %0 offset0:160 offset1:161\n\tds_read2_b32 v[90:91], %0 offset0:162 offset1:163\n\t" \
    CONV_WAIT \
    "ds_read_b128 v[92:95], %1 offset:464\n\tds_read_b128 v[96:99], %1 offset:480\n\tds_read_b128 v[100:103], %1 offset:976\n\tds_read_b128 v[104:107], %1 offset:992\n\t" \
    "s_waitcnt lgkmcnt(3)\n\tv_mfma_f32_16x16x32_f16 v[108:111], v[92:95], v[8:11], 0\n\ts_waitcnt lgkmcnt(2)\n\tv_mfma_f32_16x16x32_f16 v[112:115], v[96:99], v[12:15], 0\n\t" \
    "v_mfma_f32_16x16x32_f16 v[116:119], v[96:99], v[8:11], 0\n\tv_mfma_f32_16x16x32_f16 v[120:123], v[96:99], v[16:19], 0\n\ts_waitcnt lgkmcnt(0)\n\tv_mfma_f32_16x16x32_f16 v[124:127], v[100:103], v[8:11], 0\n\t" \
    "s_nop 3\n\tv_cvt_pk_f16_f32 v92, v108, v112\n\tv_cvt_pk_f16_f32 v94, v109, v113\n\tv_mfma_f32_16x16x32_f16 v[100:103], v[104:107], v[12:15], 0\n\tv_cvt_pk_f16_f32 v12, v110, v114\n\t" \
    "v_cvt_pk_f16_f32 v14, v111, v115\n\tv_mfma_f32_16x16x32_f16 v[108:111], v[104:107], v[8:11], 0\n\tv_cvt_pk_f16_f32 v93, v116, v120\n\tv_cvt_pk_f16_f32 v95, v117, v121\n\t" \
    "v_mfma_f32_16x16x32_f16 v[112:115], v[104:107], v[16:19], 0\n\tv_cvt_pk_f16_f32 v13, v118, v122\n\tv_cvt_pk_f16_f32 v15, v119, v123\n\tv_mfma_f32_16x16x32_f16 v[8:11], v[20:23], v[96:99], 0\n\t" \
    "v_cvt_pk_f16_f32 v16, v124, v100\n\tv_cvt_pk_f16_f32 v18, v125, v101\n\tv_mfma_f32_16x16x32_f16 v[116:119], v[20:23], v[104:107], 0\n\tv_cvt_pk_f16_f32 v120, v126, v102\n\t" \
    "v_cvt_pk_f16_f32 v122, v127, v103\n\tv_mfma_f32_16x16x32_f16 v[8:11], v[92:95], v[24:27], v[8:11]\n\tv_cvt_pk_f16_f32 v17, v108, v112\n\tv_cvt_pk_f16_f32 v19, v109, v113\n\t" \
    "v_mfma_f32_16x16x32_f16 v[116:119], v[92:95], v[28:31], v[116:119]\n\tv_cvt_pk_f16_f32 v121, v110, v114\n\tv_cvt_pk_f16_f32 v123, v111, v115\n\tv_mfma_f32_16x16x32_f16 v[96:99], v[92:95], v[32:35], 0\n\t" \
    "v_mfma_f32_16x16x32_f16 v[8:11], v[12:15], v[36:39], v[8:11]\n\tv_mfma_f32_16x16x32_f16 v[116:119], v[12:15], v[40:43], v[116:119]\n\tds_read_b128 v[104:107], %1 offset:544\n\t" \
    "v_mfma_f32_16x16x32_f16 v[96:99], v[12:15], v[44:47], v[96:99]\n\tds_read_b128 v[124:127], %1 offset:608\n\tv_mfma_f32_16x16x32_f16 v[116:119], v[16:19], v[24:27], v[116:119]\n\t" \
    "ds_read_b128 v[100:103], %1 offset:672\n\tv_mfma_f32_16x16x32_f16 v[96:99], v[16:19], v[28:31], v[96:99]\n\tds_read_b128 v[108:111], %1 offset:736\n\tv_mfma_f32_16x16x32_f16 v[112:115], v[16:19], v[32:35], 0\n\t" \
    "ds_read_b128 v[92:95], %1 offset:800\n\tv_mfma_f32_16x16x32_f16 v[116:119], v[120:123], v[36:39], v[116:119]\n\tds_read_b128 v[24:27], %1 offset:864\n\t" \
    "v_mfma_f32_16x16x32_f16 v[96:99], v[120:123], v[40:43], v[96:99]\n\tds_read_b128 v[12:15], %1 offset:928\n\tv_mfma_f32_16x16x32_f16 v[112:115], v[120:123], v[44:47], v[112:115]\n\t" \
    "ds_read_b128 v[28:31], %1 offset:992\n\ts_waitcnt lgkmcnt(4)\n\tv_mfma_f32_16x16x32_f16 v[8:11], v[48:51], v[104:107], v[8:11]\n\tds_read_b128 v[32:35], %1 offset:1056\n\t" \
    "v_mfma_f32_16x16x32_f16 v[8:11], v[52:55], v[124:127], v[8:11]\n\tv_mfma_f32_16x16x32_f16 v[8:11], v[56:59], v[100:103], v[8:11]\n\tds_read_b128 v[36:39], %1 offset:1120\n\t" \
    "v_mfma_f32_16x16x32_f16 v[8:11], v[60:63], v[108:111], v[8:11]\n\ts_waitcnt lgkmcnt(2)\n\tv_mfma_f32_16x16x32_f16 v[8:11], v[64:67], v[92:95], v[8:11]\n\t" \
    "ds_read_b128 v[16:19], %1 offset:1184\n\tv_mfma_f32_16x16x32_f16 v[8:11], v[68:71], v[24:27], v[8:11]\n\tds_read2_b32 v[40:41], %0 offset0:176 offset1:177\n\t" \
    "ds_read2_b32 v[42:43], %0 offset0:178 offset1:179\n\tv_mfma_f32_16x16x32_f16 v[8:11], v[72:75], v[12:15], v[8:11]\n\tds_read_b128 v[44:47], %1 offset:1248\n\t" \
    "v_mfma_f32_16x16x32_f16 v[8:11], v[76:79], v[28:31], v[8:11]\n\tds_read2_b32 v[120:121], %0 offset0:192 offset1:193\n\tds_read2_b32 v[122:123], %0 offset0:194 offset1:195\n\t" \
    "s_waitcnt lgkmcnt(6)\n\tv_mfma_f32_16x16x32_f16 v[8:11], v[80:83], v[32:35], v[8:11]\n\tds_read_b128 v[104:107], %1 offset:1312\n\tv_mfma_f32_16x16x32_f16 v[116:119], v[48:51], v[32:35], v[116:119]\n\t" \
    "ds_read2_b32 v[124:125], %0 offset0:208 offset1:209\n\tds_read2_b32 v[126:127], %0 offset0:210 offset1:211\n\tv_mfma_f32_16x16x32_f16 v[8:11], v[84:87], v[36:39], v[8:11]\n\t" \
    "ds_read_b128 v[100:103], %1 offset:1376\n\tv_mfma_f32_16x16x32_f16 v[116:119], v[52:55], v[36:39], v[116:119]\n\tds_read2_b32 v[108:109], %0 offset0:224 offset1:225\n\t" \
    "ds_read2_b32 v[110:111], %0 offset0:226 offset1:227\n\ts_waitcnt lgkmcnt(8)\n\tv_mfma_f32_16x16x32_f16 v[8:11], v[88:91], v[16:19], v[8:11]\n\tds_read_b128 v[92:95], %1 offset:1440\n\t" \
    "v_mfma_f32_16x16x32_f16 v[116:119], v[56:59], v[16:19], v[116:119]\n\tds_read2_b32 v[24:25], %0 offset0:240 offset1:241\n\tds_read2_b32 v[26:27], %0 offset0:242 offset1:243\n\t" \
    "v_mfma_f32_16x16x32_f16 v[8:11], v[40:43], v[44:47], v[8:11]\n\tds_read_b128 v[12:15], %1 offset:1504\n\tv_mfma_f32_16x16x32_f16 v[116:119], v[60:63], v[44:47], v[116:119]\n\t" \
    "s_waitcnt lgkmcnt(6)\n\tv_mfma_f32_16x16x32_f16 v[8:11], v[120:123], v[104:107], v[8:11]\n\tv_mfma_f32_16x16x32_f16 v[116:119], v[64:67], v[104:107], v[116:119]\n\t" \
    "ds_read_b128 v[28:31], %1 offset:1568\n\tv_mfma_f32_16x16x32_f16 v[8:11], v[124:127], v[100:103], v[8:11]\n\tv_mfma_f32_16x16x32_f16 v[116:119], v[68:71], v[100:103], v[116:119]\n\t" \
    "ds_read_b128 v[32:35], %1 offset:1632\n\ts_waitcnt lgkmcnt(2)\n\tv_mfma_f32_16x16x32_f16 v[8:11], v[108:111], v[92:95], v[8:11]\n\tv_mfma_f32_16x16x32_f16 v[116:119], v[72:75], v[92:95], v[116:119]\n\t" \
    "ds_read_b128 v[36:39], %1 offset:1696\n\tv_mfma_f32_16x16x32_f16 v[8:11], v[24:27], v[12:15], v[8:11]\n\tv_mfma_f32_16x16x32_f16 v[116:119], v[76:79], v[12:15], v[116:119]\n\t" \
    "ds_read_b128 v[16:19], %1 offset:1760\n\tv_mfma_f32_16x16x32_f16 v[96:99], v[20:23], v[12:15], v[96:99]\n\tds_read_b128 v[44:47], %1 offset:1824\n\tds_read_b128 v[104:107], %1 offset:1888\n\t" \
    "s_waitcnt lgkmcnt(4)\n\tv_mfma_f32_16x16x32_f16 v[116:119], v[80:83], v[28:31], v[116:119]\n\ts_nop 0\n\tv_mul_f32 v8, %8, v8\n\tv_mul_f32 v9, %8, v9\n\t" \
    "v_mfma_f32_16x16x32_f16 v[96:99], v[48:51], v[28:31], v[96:99]\n\tv_mul_f32 v10, %8, v10\n\tv_mul_f32 v11, %8, v11\n\tv_mfma_f32_16x16x32_f16 v[116:119], v[84:87], v[32:35], v[116:119]\n\t" \
    "ds_write_b128 %2, v[8:11] offset:0\n\tds_write_b8 %6, %6 offset:0\n\t" \
    CONV_P0 \
    "v_mfma_f32_16x16x32_f16 v[96:99], v[52:55], v[32:35], v[96:99]\n\ts_waitcnt lgkmcnt(4)\n\tv_mfma_f32_16x16x32_f16 v[116:119], v[88:91], v[36:39], v[116:119]\n\t" \
    "ds_read_b128 v[100:103], %1 offset:1952\n\tv_mfma_f32_16x16x32_f16 v[96:99], v[56:59], v[36:39], v[96:99]\n\tv_mfma_f32_16x16x32_f16 v[116:119], v[40:43], v[16:19], v[116:119]\n\t" \
    "ds_read_b128 v[92:95], %1 offset:2016\n\tv_mfma_f32_16x16x32_f16 v[96:99], v[60:63], v[16:19], v[96:99]\n\ts_waitcnt lgkmcnt(4)\n\tv_mfma_f32_16x16x32_f16 v[116:119], v[120:123], v[44:47], v[116:119]\n\t" \
    "v_mfma_f32_16x16x32_f16 v[96:99], v[64:67], v[44:47], v[96:99]\n\tds_read_b128 v[12:15], %1 offset:2080\n\tv_mfma_f32_16x16x32_f16 v[116:119], v[124:127], v[104:107], v[116:119]\n\t" \
    "v_mfma_f32_16x16x32_f16 v[96:99], v[68:71], v[104:107], v[96:99]\n\tds_read_b128 v[8:11], %1 offset:2144\n\ts_waitcnt lgkmcnt(2)\n\tv_mfma_f32_16x16x32_f16 v[116:119], v[108:111], v[100:103], v[116:119]\n\t" \
    "v_mfma_f32_16x16x32_f16 v[96:99], v[72:75], v[100:103], v[96:99]\n\tds_read_b128 v[28:31], %1 offset:2208\n\tv_mfma_f32_16x16x32_f16 v[116:119], v[24:27], v[92:95], v[116:119]\n\t" \
    "v_mfma_f32_16x16x32_f16 v[96:99], v[76:79], v[92:95], v[96:99]\n\tds_read_b128 v[32:35], %1 offset:2272\n\tv_mfma_f32_16x16x32_f16 v[112:115], v[20:23], v[92:95], v[112:115]\n\t" \
    "ds_read_b128 v[36:39], %1 offset:2336\n\tds_read_b128 v[16:19], %1 offset:2400\n\ts_waitcnt lgkmcnt(4)\n\tv_mfma_f32_16x16x32_f16 v[96:99], v[80:83], v[12:15], v[96:99]\n\t" \
    "s_nop 0\n\tv_mul_f32 v116, %8, v116\n\tv_mul_f32 v117, %8, v117\n\tv_mfma_f32_16x16x32_f16 v[112:115], v[48:51], v[12:15], v[112:115]\n\tv_mul_f32 v118, %8, v118\n\t" \
    "v_mul_f32 v119, %8, v119\n\tv_mfma_f32_16x16x32_f16 v[96:99], v[84:87], v[8:11], v[96:99]\n\tds_write_b128 %2, v[116:119] offset:1024\n\tds_write_b8 %6, %6 offset:8\n\t" \
    CONV_P1 \
    "v_mfma_f32_16x16x32_f16 v[112:115], v[52:55], v[8:11], v[112:115]\n\ts_waitcnt lgkmcnt(4)\n\tv_mfma_f32_16x16x32_f16 v[96:99], v[88:91], v[28:31], v[96:99]\n\t" \
    "ds_read_b128 v[44:47], %1 offset:2464\n\tv_mfma_f32_16x16x32_f16 v[112:115], v[56:59], v[28:31], v[112:115]\n\tv_mfma_f32_16x16x32_f16 v[96:99], v[40:43], v[32:35], v[96:99]\n\t" \
    "ds_read_b128 v[104:107], %1 offset:2528\n\tv_mfma_f32_16x16x32_f16 v[112:115], v[60:63], v[32:35], v[112:115]\n\ts_waitcnt lgkmcnt(4)\n\tv_mfma_f32_16x16x32_f16 v[96:99], v[120:123], v[36:39], v[96:99]\n\t" \
    "ds_read_b128 v[100:103], %1 offset:2592\n\tv_mfma_f32_16x16x32_f16 v[112:115], v[64:67], v[36:39], v[112:115]\n\tds_read_b128 v[20:23], %1 offset:2656\n\t" \
    "v_mfma_f32_16x16x32_f16 v[96:99], v[124:127], v[16:19], v[96:99]\n\tds_read_b128 v[92:95], %1 offset:2720\n\tv_mfma_f32_16x16x32_f16 v[112:115], v[68:71], v[16:19], v[112:115]\n\t" \
    "ds_read_b128 v[48:51], %1 offset:464\n\tds_read_b128 v[52:55], %3 offset:1024\n\ts_waitcnt lgkmcnt(5)\n\tv_mfma_f32_16x16x32_f16 v[96:99], v[108:111], v[44:47], v[96:99]\n\t" \
    "ds_read_b128 v[116:119], %1 offset:480\n\tds_read_b128 v[12:15], %3 offset:4096\n\tv_mfma_f32_16x16x32_f16 v[112:115], v[72:75], v[44:47], v[112:115]\n\t" \
    "ds_read_b128 v[8:11], %1 offset:2784\n\tv_mfma_f32_16x16x32_f16 v[96:99], v[24:27], v[104:107], v[96:99]\n\tds_read_b128 v[56:59], %1 offset:1488\n\tds_read_b128 v[28:31], %3 offset:0\n\t" \
    "v_mfma_f32_16x16x32_f16 v[112:115], v[76:79], v[104:107], v[112:115]\n\tds_read_b128 v[60:63], %1 offset:1504\n\tds_read_b128 v[32:35], %3 offset:2048\n\t" \
    "s_waitcnt lgkmcnt(7)\n\tv_mfma_f32_16x16x32_f16 v[112:115], v[80:83], v[100:103], v[112:115]\n\tv_mfma_f32_16x16x32_f16 v[112:115], v[84:87], v[20:23], v[112:115]\n\t" \
    "ds_read_b128 v[64:67], %3 offset:3072\n\tv_mfma_f32_16x16x32_f16 v[112:115], v[88:91], v[92:95], v[112:115]\n\tds_read_b128 v[36:39], %1 offset:2848\n\t" \
    "v_mfma_f32_16x16x32_f16 v[68:71], v[48:51], v[52:55], 0\n\tds_read_b128 v[16:19], %1 offset:976\n\ts_waitcnt lgkmcnt(3)\n\tv_mfma_f32_16x16x32_f16 v[72:75], v[116:119], v[12:15], 0\n\t" \
    "ds_read_b128 v[44:47], %1 offset:992\n\tv_mfma_f32_16x16x32_f16 v[112:115], v[40:43], v[8:11], v[112:115]\n\tv_add_u32 %7, 1880, %0\n\tds_read2_b32 v[76:77], %7 offset0:0 offset1:1\n\t" \
    "ds_read2_b32 v[78:79], %7 offset0:2 offset1:3\n\tv_mfma_f32_16x16x32_f16 v[104:107], v[56:59], v[28:31], 0\n\tds_read_b128 v[80:83], %1 offset:2000\n\t" \
    "ds_read_b128 v[84:87], %1 offset:2016\n\tv_mfma_f32_16x16x32_f16 v[100:103], v[60:63], v[32:35], 0\n\tv_cvt_pk_f16_f32 v20, v68, v72\n\tv_cvt_pk_f16_f32 v21, v69, v73\n\t" \
    "v_mfma_f32_16x16x32_f16 v[88:91], v[60:63], v[28:31], 0\n\tv_cvt_pk_f16_f32 v22, v70, v74\n\tv_cvt_pk_f16_f32 v23, v71, v75\n\tds_read2_b32 v[92:93], %5 offset0:48 offset1:49\n\t" \
    "ds_read2_b32 v[94:95], %5 offset0:50 offset1:51\n\ts_waitcnt lgkmcnt(6)\n\tv_mfma_f32_16x16x32_f16 v[48:51], v[60:63], v[64:67], 0\n\tds_read2_b64 v[40:43], %4 offset0:32 offset1:33\n\t" \
    "ds_read2_b64 v[8:11], %4 offset0:34 offset1:35\n\tv_mfma_f32_16x16x32_f16 v[112:115], v[120:123], v[36:39], v[112:115]\n\tv_cvt_pk_f16_f32 v56, v104, v100\n\t" \
    "v_cvt_pk_f16_f32 v58, v105, v101\n\tv_mfma_f32_16x16x32_f16 v[68:71], v[16:19], v[52:55], 0\n\tv_cvt_pk_f16_f32 v72, v106, v102\n\tv_cvt_pk_f16_f32 v74, v107, v103\n\t" \
    "v_mfma_f32_16x16x32_f16 v[60:63], v[44:47], v[12:15], 0\n\tv_cvt_pk_f16_f32 v57, v88, v48\n\tv_cvt_pk_f16_f32 v59, v89, v49\n\ts_waitcnt lgkmcnt(2)\n\t" \
    "v_mfma_f32_16x16x32_f16 v[96:99], v[76:79], v[116:119], v[96:99]\n\tv_cvt_pk_f16_f32 v73, v90, v50\n\tv_cvt_pk_f16_f32 v75, v91, v51\n\tds_read_b128 v[120:123], %1 offset:2912\n\t" \
    "v_mfma_f32_16x16x32_f16 v[36:39], v[80:83], v[28:31], 0\n\tds_read2_b32 v[52:53], %5 offset0:32 offset1:33\n\tds_read2_b32 v[54:55], %5 offset0:34 offset1:35\n\t" \
    "v_mfma_f32_16x16x32_f16 v[16:19], v[84:87], v[32:35], 0\n\tv_cvt_pk_f16_f32 v104, v68, v60\n\tv_cvt_pk_f16_f32 v105, v69, v61\n\tv_mfma_f32_16x16x32_f16 v[96:99], v[20:23], v[92:95], v[96:99]\n\t" \
    "v_cvt_pk_f16_f32 v106, v70, v62\n\tv_cvt_pk_f16_f32 v107, v71, v63\n\tds_read_b128 v[100:103], %1 offset:2976\n\tv_mfma_f32_16x16x32_f16 v[116:119], v[84:87], v[28:31], 0\n\t" \
    "ds_read2_b64 v[12:15], %4 offset0:16 offset1:17\n\tds_read2_b64 v[88:91], %4 offset0:18 offset1:19\n\tv_mfma_f32_16x16x32_f16 v[48:51], v[84:87], v[64:67], 0\n\t" \
    "v_cvt_pk_f16_f32 v80, v36, v16\n\tv_cvt_pk_f16_f32 v82, v37, v17\n\ts_waitcnt lgkmcnt(5)\n\tv_mfma_f32_16x16x32_f16 v[96:99], v[56:59], v[40:43], v[96:99]\n\t" \
    "v_cvt_pk_f16_f32 v32, v38, v18\n\tv_cvt_pk_f16_f32 v34, v39, v19\n\tv_mfma_f32_16x16x32_f16 v[96:99], v[72:75], v[8:11], v[96:99]\n\tds_read_b128 v[68:71], %1 offset:3040\n\t" \
    "v_mfma_f32_16x16x32_f16 v[112:115], v[124:127], v[120:123], v[112:115]\n\tv_cvt_pk_f16_f32 v81, v116, v48\n\tv_cvt_pk_f16_f32 v83, v117, v49\n\tv_mfma_f32_16x16x32_f16 v[112:115], v[76:79], v[44:47], v[112:115]\n\t" \
    "v_cvt_pk_f16_f32 v33, v118, v50\n\tv_cvt_pk_f16_f32 v35, v119, v51\n\ts_waitcnt lgkmcnt(1)\n\tv_mfma_f32_16x16x32_f16 v[112:115], v[20:23], v[52:55], v[112:115]\n\t" \
    "v_mul_f32 v96, %8, v96\n\tv_mul_f32 v97, %8, v97\n\tv_mfma_f32_16x16x32_f16 v[112:115], v[108:111], v[100:103], v[112:115]\n\tv_mul_f32 v98, %8, v98\n\t" \
    "v_mul_f32 v99, %8, v99\n\tv_mfma_f32_16x16x32_f16 v[112:115], v[56:59], v[12:15], v[112:115]\n\tds_write_b128 %2, v[96:99] offset:2048\n\tds_write_b8 %6, %6 offset:16\n\t" \
    CONV_P2 \
    "v_mfma_f32_16x16x32_f16 v[112:115], v[72:75], v[88:91], v[112:115]\n\tv_mfma_f32_16x16x32_f16 v[112:115], v[104:107], v[92:95], v[112:115]\n\ts_waitcnt lgkmcnt(2)\n\t" \
    "v_mfma_f32_16x16x32_f16 v[112:115], v[24:27], v[68:71], v[112:115]\n\tv_mfma_f32_16x16x32_f16 v[112:115], v[80:83], v[40:43], v[112:115]\n\tv_mfma_f32_16x16x32_f16 v[112:115], v[32:35], v[8:11], v[112:115]\n\t" \
    "s_nop 7\n\tv_mul_f32 v112, %8, v112\n\tv_mul_f32 v113, %8, v113\n\tv_mul_f32 v114, %8, v114\n\tv_mul_f32 v115, %8, v115\n\tds_write_b128 %2, v[112:115] offset:3072\n\t" \
    "ds_write_b8 %6, %6 offset:24\n\t"
#define CONV_SPIN(lab, next, off, slp) lab ":\n\tv_and_b32 %7, -8, %6\n\tds_read_b32 %7, %7 offset:" off "\n\ts_waitcnt lgkmcnt(0)\n\tv_readfirstlane_b32 %9, %7\n\t" \
    "s_cmp_ge_u32 %9, 4\n\ts_cbranch_scc1 " next "f\n\ts_sleep " slp "\n\ts_branch " lab "b\n"
#define CONV_WAIT "s_setprio 1\n" CONV_SPIN("3", "1", "36", "6") CONV_SPIN("1", "2", "32", "1") "2:\n\ts_setprio 2\n\t"
#define CONV_P0 "s_nop 0\n\t"
#define CONV_P1 "s_setprio 1\n\t"
#define CONV_P2 "s_nop 0\n\t"
#define CONV_CLOBBERS \
    "v8", "v9", "v10", "v11", "v12", "v13", "v14", "v15", "v16", "v17", "v18", "v19", "v20", "v21", "v22", "v23", "v24", "v25", "v26", "v27", "v28", "v29", "v30", "v31", \
    "v32", "v33", "v34", "v35", "v36", "v37", "v38", "v39", "v40", "v41", "v42", "v43", "v44", "v45", "v46", "v47", "v48", "v49", "v50", "v51", "v52", "v53", "v54", "v55", \
    "v56", "v57", "v58", "v59", "v60", "v61", "v62", "v63", "v64", "v65", "v66", "v67", "v68", "v69", "v70", "v71", "v72", "v73", "v74", "v75", "v76", "v77", "v78", "v79", \
    "v80", "v81", "v82", "v83", "v84", "v85", "v86", "v87", "v88", "v89", "v90", "v91", "v92", "v93", "v94", "v95", "v96", "v97", "v98", "v99", "v100", "v101", "v102", \
    "v103", "v104", "v105", "v106", "v107", "v108", "v109", "v110", "v111", "v112", "v113", "v114", "v115", "v116", "v117", "v118", "v119", "v120", "v121", "v122", "v123", \
    "v124", "v125", "v126", "v127"
DEV void conv(int hf, u32 sm0, int cw, float yscale) {
    int lane;
    asm volatile("v_mbcnt_lo_u32_b32 %0, -1, 0\n\tv_mbcnt_hi_u32_b32 %0, -1, %0" : "=v"(lane));
    __builtin_assume((unsigned)lane < 64u);
    const int al = lane & 15, q = lane >> 4;
    const u32 chb = sm0 + L_CH + cw * CH_SZ, kpar = chb + ((al & 1) ? 0 : REC_B + 2);
    u32 ka = kpar + (R_KA + ZA - al - 16 + 8 * q) * 2 - 960, ub0 = chb + C_U + (16 * al + 8 * q) * 2;
    u32 yb = sm0 + L_STG + cw * STG_BUF + 64 * al + 16 * (q ^ ((al >> 1) & 3));
    u32 bxp = sm0 + L_BOX + lane * 16, k2b = chb + (R_K2 + 192 - 4 * al - 4 + 16 * q - 128) * 2, k3b = chb + (R_K3 + 128 - 2 * al - 2 + 8 * q) * 2 - 192;
    u32 fl = sm0 + L_CNT + 8 + cw, t0, t1;
    u32 ys = __builtin_amdgcn_readfirstlane(__float_as_uint(yscale));
    static_assert(UPAD == 256 && (R_KP + 15 - (R_KA + ZA - 16)) * 2 + 960 == 1880 && L_CNT % 16 == 0, "offsets in the streams");
    asm volatile("s_cmp_eq_u32 %10, 0\n\ts_cbranch_scc1 8f\n\t" CONV_ASM_1 "s_branch 9f\n8:\n\t" CONV_ASM_0 "\n9:"
                 : "+v"(ka), "+v"(ub0), "+v"(yb), "+v"(bxp), "+v"(k2b), "+v"(k3b), "+v"(fl), "=&v"(t0), "+s"(ys), "=&s"(t1) : "s"(hf) : "memory", "scc", CONV_CLOBBERS);
}

constexpr int G_KEY = 8 * SL * 8, G_SZ = G_KEY + 256 * NREP * 16;
constexpr size_t WS_EPOCH = 0, WS_GRAN = 4096, WS_HAND = WS_GRAN + G_SZ, WS_U = WS_HAND + (size_t)(GU_WORDS + DN_WORDS) * 4,
                 WS_END = WS_U + (size_t)SL * SD * 2;
typedef __attribute__((ext_vector_type(8))) u32 u8v;
typedef __amdgpu_buffer_rsrc_t rsrc_t;
DEV rsrc_t mk_rsrc(u32 lo, u32 hi, size_t off, u32 bytes) {
    return __builtin_amdgcn_make_buffer_rsrc((__attribute__((address_space(1))) void*)((((unsigned long long)hi << 32) | lo) + off), 0, bytes, 0x00020000);
}
#define BLD128(rs, vo, so, aux) __builtin_amdgcn_raw_buffer_load_b128(rs, vo, so, aux)
DEV u32 lds_addr(const void* p) { return (u32)(uintptr_t)(__attribute__((address_space(3))) const void*)p; }

extern "C" __global__ __launch_bounds__(1024, 4) void k_fused(
    const u16* x_, const u16* wr_, char* ws_, u16* out_,
    const u16* logA_, const u16* Aim_, const u16* Cre_, const u16* Cim_,
    const u16* logdt_, const u16* dskip_, const u16* wg_, const u16* wu_, const u16* wd_) {
    __shared__ __attribute__((aligned(16))) char smem[L_TOTAL];
    const int tid = threadIdx.x, lane = tid & 63;
    const int wave = __builtin_amdgcn_readfirstlane(tid >> 6);
    const int xcd = blockIdx.x & 7, slot = blockIdx.x >> 3;
    const int g = xcd * 16 + (slot >> 1), hf = slot & 1;
    STAMP_DECL;
    STAMP(0);
    u32* cnt = reinterpret_cast<u32*>(smem + L_CNT);
    const u32 sm0 = lds_addr(smem);
    if (wave == 0) {
        if (lane < 32) cnt[lane] = 0;
        __builtin_amdgcn_s_waitcnt(0xc07f);
    }
    MEM_FENCE(); SCHED_FENCE();
    u32 kw0, kw1;
    asm volatile("s_load_dword %0, %2, 0x0\n\ts_load_dword %1, %2, 0x40" : "=&s"(kw0), "=&s"(kw1) : "s"(__builtin_amdgcn_kernarg_segment_ptr()) : "memory");
#define ARGS_LOAD() u8v ka0, ka1, ka2; u2 ka3; \
    asm volatile("s_load_dwordx8 %0, %4, 0x0\n\ts_load_dwordx8 %1, %4, 0x20\n\ts_load_dwordx8 %2, %4, 0x40\n\ts_load_dwordx2 %3, %4, 0x60" \
                 : "=&s"(ka0), "=&s"(ka1), "=&s"(ka2), "=&s"(ka3) : "s"(__builtin_amdgcn_kernarg_segment_ptr()) : "memory"); SCHED_FENCE()
#define ARGS_WAIT() do { SCHED_FENCE(); asm volatile("s_waitcnt lgkmcnt(0)" : "+s"(ka0), "+s"(ka1), "+s"(ka2), "+s"(ka3) : "s"(kw0), "s"(kw1) : "memory"); SCHED_FENCE(); } while (0)
#define LANE_IDS() int lane; asm volatile("v_mbcnt_lo_u32_b32 %0, -1, 0\n\tv_mbcnt_hi_u32_b32 %0, -1, %0" : "=v"(lane)); __builtin_assume((unsigned)lane < 64u); \
    const int tid = wave * 64 + lane, al = lane & 15, q = lane >> 4
#define XRS mk_rsrc(ka0[0], ka0[1], 0, SL * SD * 2)
#define GRS mk_rsrc(ka0[4], ka0[5], WS_GRAN, G_SZ)
#define URS mk_rsrc(ka0[4], ka0[5], WS_U, SL * SD * 2)
#define HRS mk_rsrc(ka0[4], ka0[5], WS_HAND, (GU_WORDS + DN_WORDS) * 4)
#define ORS mk_rsrc(ka0[6], ka0[7], 0, SL * SD * 2)
    u32 code_pf = 0;
    if (wave == 8 && slot * 2048 < CODE_B) {
        unsigned long long pc;
        asm volatile("s_getpc_b64 %0" : "=s"(pc));
        code_pf = *(const __attribute__((address_space(1))) u32*)((pc & ~255ull) + (unsigned)min(slot * 2048 + lane * 32, CODE_B));
    }
    MEM_FENCE(); SCHED_FENCE();
    __builtin_amdgcn_s_barrier();
    MEM_FENCE(); SCHED_FENCE();
#define M0_ADD(n) "s_add_u32 m0, m0, " #n "\n\ts_nop 0\n\t"
    if (wave == 6) {
        unsigned long long pe;
        u32 ep;
        asm volatile("s_load_dwordx2 %0, %2, 0x10\n\ts_waitcnt lgkmcnt(0)\n\ts_load_dword %1, %0, %3\n\ts_waitcnt lgkmcnt(0)"
                     : "=&s"(pe), "=s"(ep) : "s"(__builtin_amdgcn_kernarg_segment_ptr()), "n"(WS_EPOCH) : "memory");
        const u32 tg = ep + 1u;
        STAMP(1);
        if (lane < 5) cnt[24 + lane] = lane == 4 ? tg : ((tg >> (6 * lane)) & 7u) | (((tg >> (6 * lane + 3)) & 7u) << 16);
        MEM_FENCE();
        lds_add1(cnt + 0);
    }
    MEM_FENCE(); SCHED_FENCE();
#define URGENT_DONE() do { MEM_FENCE(); SCHED_FENCE(); lds_add1(cnt + 17); MEM_FENCE(); SCHED_FENCE(); } while (0)
    const int b = xcd * 32 + slot;
    const unsigned long long all8 = 0x0706050403020100ull + ((sm0 + L_CNT + 8) & 0xffu) * 0x0101010101010101ull;
    typedef __attribute__((address_space(3))) u32* lptr32;
    typedef __attribute__((address_space(3))) u4* lptr128;
    auto stats = [&](const u4 (&v)[4], float& mb, float& den, float& rd) __attribute__((always_inline)) {
        f2 p[8];
#pragma unroll
        for (int i = 0; i < 8; ++i) { p[i].x = __uint_as_float(v[i >> 1][2 * (i & 1)]); p[i].y = __uint_as_float(v[i >> 1][2 * (i & 1) + 1]); }
        const f2 t = (((p[0] + p[1]) + (p[2] + p[3])) + ((p[4] + p[5]) + (p[6] + p[7]))) * (1.0f / SD);
        const float var = fmaxf(__builtin_fmaf(-t.x, t.x, t.y), 0.0f);
        mb = rbf(t.x);
        den = rbf(__builtin_amdgcn_sqrtf(rbf(rbf(var) + 1e-5f)));
        rd = __builtin_amdgcn_rcpf(den);
    };
    auto e_role = [&]() __attribute__((always_inline)) {
        ARGS_LOAD();
        __builtin_amdgcn_s_setprio(3);
        LANE_IDS();
        u32 vw = wave * 512 + (al >> 3) * 64 + q * 16;
        u32 vrow = (b * 8 + (lane >> 3)) * (SD * 2) + wave * 512 + (((lane & 7) ^ ((lane >> 3) & 2)) << 4);
        const u32 l_row = sm0 + L_CH + wave * CH_SZ + C_U + UPAD * 2;
        u32 a_x = l_row + (al & 7) * 128 + (((4 * (al >> 3) + q) ^ (al & 2)) << 4);
        PIN(vrow);
        u4 xa[4], wa[4];
        ARGS_WAIT();
        asm volatile("s_mov_b32 m0, %0\n\ts_nop 0\n\tglobal_load_lds_dwordx4 %1, %2\n\t" M0_ADD(896) "global_load_lds_dwordx4 %1, %2 offset:128\n\t"
                     M0_ADD(896) "global_load_lds_dwordx4 %1, %2 offset:256\n\t" M0_ADD(896) "global_load_lds_dwordx4 %1, %2 offset:384"
                     :: "s"(l_row), "v"(vrow), "s"(((unsigned long long)ka0[1] << 32) | ka0[0]) : "memory", "scc");
        MEM_FENCE(); SCHED_FENCE();
#pragma unroll
        for (int j = 0; j < 4; ++j) { wa[j] = u4{0x3f803f80u, 0x3f803f80u, 0x3f803f80u, 0x3f803f80u}; PIN(wa[j]); }
        PIN(vw); PIN(a_x);
        if (!(al & 4)) {
            const rsrc_t rrs = mk_rsrc(ka0[2], ka0[3], 0, SD * 2);
#pragma unroll
            for (int j = 0; j < 4; ++j) wa[j] = BLD128(rrs, vw + 128 * j, 0, 0);
        }
        URGENT_DONE();
        u4 sel[2];
#pragma unroll
        for (int h = 0; h < 2; ++h) {
#pragma unroll
            for (int i = 0; i < 4; ++i) sel[h][i] = (q == 2 * h + (al >> 3) && i == ((al & 7) >> 1)) ? (0x3f80u << (16 * (al & 1))) : 0u;
            PIN(sel[h]);
        }
        const int part = 2 * wave + (al >> 3);
        u32 a_p2 = sm0 + L_P12 + (al & 7) * P12_S + part * 8;
        u32 a_p1 = (q == 1) ? a_p2 : (q == (al >> 3) * 2) ? sm0 + L_P3 + (al & 7) * P3_S + part * 4 : sm0 + L_NUL + lane * 4;
        a_p2 = (q == (al >> 2)) ? a_p2 + 4 : sm0 + L_NUL + lane * 4;
        u32 a_pr = sm0 + L_P12 + (4 * (q & 1) + (al & 3)) * P12_S, a_cnt = sm0 + L_CNT;
        u32 vu[4], m7 = 0x00070007u;
#pragma unroll
        for (int j = 0; j < 4; ++j) { vu[j] = ((32 * wave + 8 * j + (lane >> 3)) * 256 + b) * 128 + (lane & 7) * 16; PIN(vu[j]); }
        u32 vp = g * 32768 + tid * 16, vs = (xcd * SL + tid) * 8;
        u32 a_u = sm0 + L_CH + (lane & 7) * CH_SZ + C_U + (UPAD + 64 * wave + 8 * (lane >> 3)) * 2;
        PIN(a_p1); PIN(a_p2); PIN(a_pr); PIN(a_cnt); PIN(m7); PIN(vp); PIN(vs); PIN(a_u);
        const rsrc_t urs = URS, grs = GRS;
        bool c1 = al & 1, c2_ = al & 2;
        lds_wait(cnt + 0, 1);
        const u32 tag = __builtin_amdgcn_readfirstlane(cnt[28]);
        u4 pat = *reinterpret_cast<const u4*>(cnt + 24);
        PIN(pat);
        MEM_FENCE(); SCHED_FENCE();
        {
            const f4 z4 = {0, 0, 0, 0};
            f4 d2 = z4, d1 = z4, xt[4][2];
            asm volatile("s_waitcnt vmcnt(0)\n\tds_read_b128 %0, %4\n\tds_read_b128 %1, %4 offset:1024\n\tds_read_b128 %2, %4 offset:2048\n\t"
                         "ds_read_b128 %3, %4 offset:3072\n\ts_waitcnt lgkmcnt(0)"
                         : "=&v"(xa[0]), "=&v"(xa[1]), "=&v"(xa[2]), "=&v"(xa[3]) : "v"(a_x) : "memory");
#pragma unroll
            for (int j = 0; j < 4; ++j) { d2 = mfma16b(xa[j], xa[j], d2); d1 = mfma16b(wa[j], xa[j], d1); }
            STAMP(1);
#pragma unroll
            for (int j = 0; j < 2; ++j) xt[j >> 1][j & 1] = mfma16b(xa[j >> 1], sel[j & 1], z4);
            SCHED_FENCE();
            const float e0 = c1 ? d2[1] : d2[0], e1 = c1 ? d2[3] : d2[2];
            *(lptr32)(uintptr_t)a_p2 = __float_as_uint(c2_ ? e1 : e0);
            *(lptr32)(uintptr_t)a_p1 = __float_as_uint(d1[0]);
            MEM_FENCE();
            lds_add1a(a_cnt + 4);
            __builtin_amdgcn_s_setprio(2);
            SCHED_FENCE();
#pragma unroll
            for (int j = 0; j < 2; ++j) asm volatile("" :: "v"(xt[j >> 1][j & 1]));
            SCHED_FENCE();
            u4 pv[4];
            for (;;) {
                u32 c;
                asm volatile("ds_read_b32 %0, %5 offset:4\n\tds_read_b128 %1, %6\n\tds_read_b128 %2, %6 offset:16\n\tds_read_b128 %3, %6 offset:32\n\t"
                             "ds_read_b128 %4, %6 offset:48\n\ts_waitcnt lgkmcnt(0)"
                             : "=&v"(c), "=&v"(pv[0]), "=&v"(pv[1]), "=&v"(pv[2]), "=&v"(pv[3]) : "v"(a_cnt), "v"(a_pr) : "memory");
                if (__builtin_amdgcn_readfirstlane(c) >= 4u) break;
            }
            STAMP(2);
#pragma unroll
            for (int j = 2; j < 8; ++j) xt[j >> 1][j & 1] = mfma16b(xa[j >> 1], sel[j & 1], z4);
            float mb, den, rd;
            stats(pv, mb, den, rd);
            const float a = LOG2E * rd, c = -mb * a;
            f2 a2[2], c2[2];
            a2[0].x = dpp_mov<0x00>(a); a2[0].y = dpp_mov<0x55>(a); a2[1].x = dpp_mov<0xaa>(a); a2[1].y = dpp_mov<0xff>(a);
            c2[0].x = dpp_mov<0x00>(c); c2[0].y = dpp_mov<0x55>(c); c2[1].x = dpp_mov<0xaa>(c); c2[1].y = dpp_mov<0xff>(c);
            STAMP(3);
#pragma unroll
            for (int jj = 0; jj < 4; jj += 2) {
                f2 v[8];
                u32 p[8];
#pragma unroll
                for (int i = 0; i < 8; ++i) {
                    const f4 t = xt[jj + (i >> 2)][(i >> 1) & 1];
                    v[i].x = t[2 * (i & 1)]; v[i].y = t[2 * (i & 1) + 1];
                    v[i] = __builtin_elementwise_fma(v[i], a2[i & 1], c2[i & 1]);
                }
                SCHED_FENCE();
#pragma unroll
                for (int i = 0; i < 8; ++i) p[i] = pkh(v[i]);
                SCHED_FENCE();
#pragma unroll
                for (int i = 0; i < 8; ++i) p[i] += 0x00040004u;
                SCHED_FENCE();
                u4 f[2];
#pragma unroll
                for (int k = 0; k < 2; ++k) {
                    const auto s0 = __builtin_amdgcn_permlane16_swap(p[4 * k], p[4 * k + 2], false, false);
                    const auto s1 = __builtin_amdgcn_permlane16_swap(p[4 * k + 1], p[4 * k + 3], false, false);
                    f[k].x = s0[0]; f[k].y = s1[0]; f[k].z = s0[1]; f[k].w = s1[1];
                }
                SCHED_FENCE();
#pragma unroll
                for (int k = 0; k < 2; ++k) {
#pragma unroll
                    for (int i = 0; i < 4; ++i) f[k][i] = bfi(m7, pat[i], f[k][i]);
                    __builtin_amdgcn_raw_buffer_store_b128(f[k], urs, vu[jj + k], 0, 16);
                }
                SCHED_FENCE();
            }
        }
        MEM_FENCE(); SCHED_FENCE();
        lds_add1a(a_cnt + 23 * 4);
        __builtin_amdgcn_s_setprio(3);
        MEM_FENCE(); SCHED_FENCE();
        STAMP(4);
        const int t01 = 2 * hf, t23 = 6 - 2 * hf;
        u32 a_g = sm0 + L_CH + ((lane >> 2) & 3) * CH_SZ + C_U + (UPAD + 64 * wave + 16 * (lane >> 4) + 4 * (lane & 3)) * 2;
        u32 a_g0 = a_g + t01 * 512, a_g1 = a_g + t23 * 512;
        u32 vp2 = hf ? 0x80000000u : vp;
        PIN(a_g0); PIN(a_g1); PIN(vp2);
        __builtin_amdgcn_s_sleep(U_DELAY);
        lds_wait(cnt + 31, 1);
        STAMP(5);
        u4 up[8];
        u2 sg[4];
        auto ld_stats = [&]() __attribute__((always_inline)) {
#pragma unroll
            for (int t = 0; t < 4; ++t) sg[t] = __builtin_amdgcn_raw_buffer_load_b64(grs, vs + (t & 1) * 2048, (t < 2 ? t01 : t23) * 2048, 16);
        };
#pragma unroll
        for (int p = 0; p < 8; ++p) up[p] = BLD128(urs, p < 6 ? vp : vp2, p * 4096, 16);
        ld_stats();
        MEM_FENCE(); SCHED_FENCE();
        lds_add1a(a_cnt + 44);
        MEM_FENCE(); SCHED_FENCE();
        {
            u32 bad = 0, bad6 = 0;
#pragma unroll
            for (int p = 0; p < 8; ++p) {
#pragma unroll
                for (int i = 0; i < 4; ++i) up[p][i] ^= pat[i];
                *(lptr128)(uintptr_t)(a_u + p * 512) = up[p];
                bad |= up[p].x | up[p].y; PIN(bad);
                bad |= up[p].z | up[p].w; PIN(bad);
                if (p == 5) bad6 = bad;
                SCHED_FENCE();
            }
            STAMP(13);
            if (__builtin_expect(__any(((hf ? bad6 : bad) & m7) != 0), 0)) {
                u32 miss = 0;
#pragma unroll
                for (int p = 0; p < 8; ++p) if (__any(((up[p].x | up[p].y | up[p].z | up[p].w) & m7) != 0)) miss |= 1u << p;
                if (hf) miss &= 0x3fu;
#pragma unroll 1
                for (int spin = 0; miss != 0 && spin < SPIN_MAX; ++spin) {
                    MEM_FENCE();
#pragma unroll
                    for (int p = 0; p < 8; ++p) if ((miss >> p) & 1u) up[p] = BLD128(urs, vp, p * 4096, 16);
#pragma unroll
                    for (int p = 0; p < 8; ++p) if ((miss >> p) & 1u) {
#pragma unroll
                        for (int i = 0; i < 4; ++i) up[p][i] ^= pat[i];
                        *(lptr128)(uintptr_t)(a_u + p * 512) = up[p];
                        if (!__any(((up[p].x | up[p].y | up[p].z | up[p].w) & m7) != 0)) miss &= ~(1u << p);
                    }
                }
            }
            STAMP(6);
            MEM_FENCE();
            lds_add1a(a_cnt + 40);
            MEM_FENCE(); SCHED_FENCE();
        }
        __builtin_amdgcn_s_setprio(0);
        u2 ut[4][2];
        asm volatile("ds_read_b64_tr_b16 %0, %8\n\tds_read_b64_tr_b16 %1, %8 offset:%10\n\tds_read_b64_tr_b16 %2, %8 offset:512\n\tds_read_b64_tr_b16 %3, %8 offset:512+%10\n\t"
                     "ds_read_b64_tr_b16 %4, %9\n\tds_read_b64_tr_b16 %5, %9 offset:%10\n\tds_read_b64_tr_b16 %6, %9 offset:512\n\tds_read_b64_tr_b16 %7, %9 offset:512+%10\n\t"
                     "s_waitcnt lgkmcnt(0)"
                     : "=&v"(ut[0][0]), "=&v"(ut[0][1]), "=&v"(ut[1][0]), "=&v"(ut[1][1]), "=&v"(ut[2][0]), "=&v"(ut[2][1]), "=&v"(ut[3][0]), "=&v"(ut[3][1])
                     : "v"(a_g0), "v"(a_g1), "n"(4 * CH_SZ) : "memory");
        if (__builtin_expect(__any(sg[0].y != tag || sg[1].y != tag || sg[2].y != tag || sg[3].y != tag), 0)) {
#pragma unroll 1
            for (int spin = 0; spin < SPIN_MAX; ++spin) {
                MEM_FENCE();
                ld_stats();
                if (!__any(sg[0].y != tag || sg[1].y != tag || sg[2].y != tag || sg[3].y != tag)) break;
                __builtin_amdgcn_s_sleep(G_SLEEP);
            }
        }
        STAMP(7);
        u32 gh[4][4];
        float xf[4][8];
        auto gate = [&](int t) __attribute__((always_inline)) {
            gate4(gh[t], ut[t][0].x, ut[t][0].y, ut[t][1].x, ut[t][1].y);
            const float mt = bflo(sg[t].x), dt = bfhi(sg[t].x) * (1.0f / LOG2E);
#pragma unroll
            for (int c = 0; c < 8; ++c) {
                const u32 uw = ut[t][c >> 2][(c >> 1) & 1];
                float xx = __builtin_fmaf((float)__builtin_bit_cast(h2, uw)[c & 1], dt, mt);
                PIN(xx);
                xf[t][c] = xx;
            }
            MEM_FENCE(); SCHED_FENCE();
        };
        gate(0); gate(1); gate(2);
        lds_wait(cnt + 13, 1);
        u32 vo[4];
        {
            const int i0 = 0xffff - (int)(__builtin_amdgcn_readfirstlane(cnt[12]) & 0xffffu);
            const int i1 = 0xffff - (int)(__builtin_amdgcn_readfirstlane(cnt[13]) & 0xffffu);
#pragma unroll
            for (int j = 0; j < 4; ++j) {
                const int l = 256 * ((j < 2 ? t01 : t23) + (j & 1)) + tid;
                vo[j] = (l == i0 || l == i1) ? 0x80000000u : l * (SD * 2) + g * 16;
                PIN(vo[j]);
            }
        }
        const char* yp = smem + L_STG + 64 * (tid >> 4) + 16 * (((tid >> 2) & 3) ^ ((tid >> 5) & 3)) + 4 * (tid & 3);
        const rsrc_t ors = ORS;
        STAMP(8);
        auto epi = [&](int t) __attribute__((always_inline)) {
            if (t >= E_UP) __builtin_amdgcn_s_setprio(3);
            while (__hip_atomic_load(reinterpret_cast<unsigned long long*>(cnt + 2 + 2 * t), __ATOMIC_RELAXED, __HIP_MEMORY_SCOPE_WORKGROUP) != all8) __builtin_amdgcn_s_sleep(1);
            __builtin_amdgcn_fence(__ATOMIC_ACQUIRE, "wavefront");
            __builtin_amdgcn_wave_barrier();
            if (t == 0) STAMP(9);
            if (t == 2) STAMP(14);
            if (t == 3) STAMP(11);
            float yv[8];
#pragma unroll
            for (int c = 0; c < 8; ++c) yv[c] = *reinterpret_cast<const float*>(yp + c * STG_BUF + 1024 * t);
            u4 o;
#pragma unroll
            for (int h = 0; h < 4; ++h) {
                const h2 gg = __builtin_bit_cast(h2, gh[t][h]);
                float o0 = __builtin_fmaf(yv[2 * h], (float)gg[0], xf[t][2 * h]);
                float o1 = __builtin_fmaf(yv[2 * h + 1], (float)gg[1], xf[t][2 * h + 1]);
                PIN(o0); PIN(o1);
                o[h] = pkbf(o0, o1);
            }
            if (t < PACE) { __builtin_amdgcn_s_waitcnt(0x0f70); MEM_FENCE(); SCHED_FENCE(); }
            __builtin_amdgcn_raw_buffer_store_b128(o, ors, vo[t], 0, 0);
            MEM_FENCE(); SCHED_FENCE();
            if (t == 1) STAMP(10);
            if (t == 3) STAMP(12);
        };
        epi(0);
        gate(3);
        epi(1); epi(2); epi(3);
    };
    auto f_role = [&]() __attribute__((always_inline)) {
        constexpr int WE = W_A;
        static_assert(W_A >= 2 && W_A + W_B + W_E <= 16, "weight loads");
        ARGS_LOAD();
        __builtin_amdgcn_s_setprio(3);
        LANE_IDS();
        const int hw = wave - 4;
        const bool has_gu = slot < 28;
        const int s7 = (slot * 37) >> 8, mat = s7 & 1, quart = 2 * (xcd >> 2) + ((s7 >> 1) & 1), cblk = 7 * ((xcd + 3) & 3) + slot - 7 * s7, ksl = 4 * quart + hw;
        const int sup = (xcd & 3) * 8 + (slot >> 2), chk = 4 * sup + hw, dblk = (xcd < 4 ? 4 : 0) + (slot & 3);
        auto f_box = [&]() __attribute__((always_inline)) {
            const int off = (hw == 2) ? 4 : 8, bw = (hw == 0) ? 2 : (hw == 1) ? 4 : (hw == 2) ? 1 : 3;
            *reinterpret_cast<h8*>(smem + L_BOX + (hw == 3 ? 4 : hw) * 1024 + lane * 16) = box_frag(al, q, off, bw);
            if (hw == 2) *reinterpret_cast<h8*>(smem + L_BOX + 3072 + lane * 16) = box_frag(al, q, 12, 1);
            const u4 z = {0, 0, 0, 0};
#pragma unroll
            for (int i = 0; i < 3; ++i) *reinterpret_cast<u4*>(smem + L_Z + hw * IMG + min(i * 1024 + lane * 16, IMG - 16)) = z;
            MEM_FENCE();
            SCHED_FENCE();
        };
        u32 wvo = ((64 * ksl + q) * SF + 128 * cblk + 8 * al) * 2;
        u32 a_cnt = sm0 + L_CNT, v16 = (hw == 0) ? lane * 16 : 0x80000000u;
        PIN(wvo); PIN(a_cnt); PIN(v16);
        ARGS_WAIT();
        u4 wr[2], wv[16], wdv[8];
        {
            const rsrc_t rrs = mk_rsrc(ka0[2], ka0[3], 0, SD * 2);
            wr[0] = BLD128(rrs, v16, 0, 0); wr[1] = BLD128(rrs, v16, 1024, 0);
        }
        MEM_FENCE(); SCHED_FENCE();
        lds_wait(cnt + 17, 12);
        const rsrc_t wrs = mk_rsrc(mat ? ka2[6] : ka2[4], mat ? ka2[7] : ka2[5], 0, has_gu ? SD * SF * 2 : 0);
        auto load_w = [&](int j) __attribute__((always_inline)) {
            wv[j] = BLD128(wrs, wvo, 4 * j * (SF * 2), 2);
            MEM_FENCE(); SCHED_FENCE();

        };
        STAMP(8);
        load_w(0); load_w(1);
        f_box();
        lds_add1a(a_cnt + 22 * 4);
#pragma unroll
        for (int j = 2; j < WE; ++j) { load_w(j); if (j + 1 < WE) __builtin_amdgcn_s_sleep(W_PACE); }
        MEM_FENCE(); SCHED_FENCE();
        STAMP(10);
        const rsrc_t grs = GRS;
        u32 tag;
        {
            float sw = 0.f;
            u32 a_pr = sm0 + L_P12 + (lane & 7) * P12_S, a_p3 = sm0 + L_P3 + (lane & 7) * P3_S;
            u32 vpub = (hw == 0) ? (lane < NREP ? G_KEY + (lane * 256 + blockIdx.x) * 16 : 0x80000000u) : ((lane & 1) ? 0x80000000u : ((lane >> 3) * SL + b * 8 + (lane & 7)) * 8);
            u32 ck = 0xffff - (b * 8 + (lane & 7));
            PIN(a_pr); PIN(a_p3); PIN(vpub); PIN(ck);
            lds_wait(cnt + 0, 1);
            tag = __builtin_amdgcn_readfirstlane(cnt[28]);
            MEM_FENCE(); SCHED_FENCE();
            if (hw == 0) {
#pragma unroll
                for (int j = 0; j < 8; ++j) sw = dot2(wr[j >> 2][j & 3], 0x3f803f80u, sw);
                sw = wave_sum(sw);
            }
            MEM_FENCE(); SCHED_FENCE();
            if (hw < 2) {
                u4 pv[4];
                f4 s0, s1;
                lds_wait(cnt + 1, 3);
                for (;;) {
                    u32 c;
                    asm volatile("ds_read_b32 %0, %7 offset:4\n\tds_read_b128 %1, %8\n\tds_read_b128 %2, %8 offset:16\n\tds_read_b128 %3, %8 offset:32\n\t"
                                 "ds_read_b128 %4, %8 offset:48\n\tds_read_b128 %5, %9\n\tds_read_b128 %6, %9 offset:16\n\ts_waitcnt lgkmcnt(0)"
                                 : "=&v"(c), "=&v"(pv[0]), "=&v"(pv[1]), "=&v"(pv[2]), "=&v"(pv[3]), "=&v"(s0), "=&v"(s1) : "v"(a_cnt), "v"(a_pr), "v"(a_p3) : "memory");
                    const u32 cc = __builtin_amdgcn_readfirstlane(c);
                    if (cc >= 4u) break;
                    if (cc == 0u) __builtin_amdgcn_s_sleep(1);
                }
                float mb, den, rd;
                stats(pv, mb, den, rd);
                u4 f; f.y = tag; f.w = tag;
                if (hw == 0) {
                    const f4 sv = s0 + s1;
                    const u32 rb = __float_as_uint(rbf(__builtin_fmaf(-mb, sw, (sv[0] + sv[1]) + (sv[2] + sv[3])) * rd));
                    u32 m1 = ((rb ^ ((u32)((int)rb >> 31) | 0x80000000u)) & 0xffff0000u) | ck, m2 = 0;
                    top2_of8(m1, m2);
                    f.x = __builtin_amdgcn_readlane(m1, 7); f.z = __builtin_amdgcn_readlane(m2, 7);
                } else {
                    f.x = __builtin_amdgcn_perm(__float_as_uint(den), __float_as_uint(mb), 0x07060302u);
                    f.z = __builtin_amdgcn_mov_dpp(f.x, 0xf5, 0xf, 0xf, true);
                }
                __builtin_amdgcn_raw_buffer_store_b128(f, grs, vpub, 0, 16);
            }
            MEM_FENCE(); SCHED_FENCE();
        }
        STAMP(4);
#pragma unroll
        for (int j = WE; j < WE + W_B; ++j) { load_w(j); if (hw >= 2) __builtin_amdgcn_s_sleep(W_PACE2); }
        u32 vtag = tag, dvo = (q * SD + 128 * dblk + 8 * al) * 2;
        const u32 img = sm0 + L_XN + hw * IMG;
        u32 aoff = img + (al >> 2) * XN_S + 16 * q, a_red = sm0 + L_XN + q * XN_S + al * 16;
        u32 idx = ((al & 3) * 0x4444u) | (((al & 3) ^ 1) * 0x1111u);
        const u32 Q = 32 * cblk + (lane & 31), cj = __umul24(Q, 9363u) >> 16;
        u32 gu_off = has_gu ? (__umul24(cj, 121u) + Q + ((q >> 1) * 8 + quart * 2 + mat) * 8) * 16 : 0x80000000u;
        u32 aoffs = (chk * 128 + lane - ((lane & 7) == 7 ? 1 : 0)) * 16;
        u32 h_st = img + ((lane >> 3) & 1) * (2 * XN_S) + (4 * (lane & 7) + q) * 8;
        u32 dn_off = (GU_WORDS * 4) + ((sup * 8 + dblk) * 64 + lane) * 16;
        PIN(vtag); PIN(dvo); PIN(aoff); PIN(a_red); PIN(idx); PIN(gu_off); PIN(aoffs); PIN(h_st); PIN(dn_off);
        MEM_FENCE(); SCHED_FENCE();
#pragma unroll
        for (int j = WE + W_B; j < 16 - W_E; ++j) { load_w(j); __builtin_amdgcn_s_sleep(W_PACE2); }
        STAMP(5);
        int dso[7];
#pragma unroll
        for (int j = 0; j < 7; ++j) {
            const int Qd = 7 * chk + j;
            dso[j] = (128 * (Qd >> 5) + 8 * (Qd & 15) + 4 * ((Qd >> 4) & 1)) * (SD * 2);
            asm volatile("" : "+s"(dso[j]));
        }
        u32 k1, k2;
        if (hw == 3) {
            u32 vk = G_KEY + ((blockIdx.x % NREP) * 256 + lane) * 16;
            PIN(vk);
            lds_wait(cnt + 23, 4);
            __builtin_amdgcn_s_sleep(G_DELAY);
            u4 kg[4];
            poll_pieces<4, G_SLEEP>(kg, [&](int p) { return BLD128(grs, vk + 1024 * p, 0, 16); }, [&](u4 v_) { return v_.w != tag; });
            lds_add1a(a_cnt + 31 * 4);
            STAMP(2);
            u32 m1 = max(kg[0].x, kg[1].x), m2 = min(kg[0].x, kg[1].x);
            m2 = med3u(m1, m2, kg[2].x); m1 = max(m1, kg[2].x); m2 = med3u(m1, m2, kg[3].x); m1 = max(m1, kg[3].x);
            m2 = max(max(m2, max(kg[0].z, kg[1].z)), max(kg[2].z, kg[3].z));
            wave_top2(m1, m2);
            u2 m; m.x = m1; m.y = m2;
            asm volatile("s_mov_b32 exec_lo, 0\n\ts_brev_b32 exec_hi, 1\n\tds_write_b64 %0, %1 offset:48\n\ts_mov_b64 exec, -1" :: "v"(a_cnt), "v"(m) : "memory");
            k1 = __builtin_amdgcn_readlane(m1, 63); k2 = __builtin_amdgcn_readlane(m2, 63);
        } else {
            __builtin_amdgcn_s_sleep(G_DELAY);
            for (;;) {
                u2 m;
                asm volatile("ds_read_b64 %0, %1 offset:48\n\ts_waitcnt lgkmcnt(0)" : "=v"(m) : "v"(a_cnt) : "memory");
                k1 = __builtin_amdgcn_readfirstlane(m.x); k2 = __builtin_amdgcn_readfirstlane(m.y);
                if (k2) break;
                __builtin_amdgcn_s_sleep(1);
            }
        }
        MEM_FENCE(); SCHED_FENCE();
        const u32 ts = (0xffffu - ((lane & 32) ? k2 : k1)) & (SL - 1), osr = (xcd * SL + ts) * 8;
        const u32 xr = __builtin_amdgcn_raw_buffer_load_b32(XRS, ts * (SD * 2) + 128 * ksl + 4 * (lane & 31), 0, 0);
        u2 sr = __builtin_amdgcn_raw_buffer_load_b64(grs, osr, 0, 16);
        MEM_FENCE(); SCHED_FENCE();
        lds_wait(cnt + 11, 4);
#pragma unroll
        for (int j = 16 - W_E; j < 16; ++j) load_w(j);
        MEM_FENCE(); SCHED_FENCE();
        {
            u32 a_xn = img + (lane >> 5) * (2 * XN_S) + (lane & 31) * 16;
            PIN(a_xn);
            if (__builtin_expect(!__all(sr.y == tag), 0)) {
#pragma unroll 1
                for (int spin = 0; spin < SPIN_MAX && !__all(sr.y == tag); ++spin) { MEM_FENCE(); sr = __builtin_amdgcn_raw_buffer_load_b64(grs, osr, 0, 16); __builtin_amdgcn_s_waitcnt(0x0f70); }
            }
            const float mb2 = bflo(sr.x), rc2 = __builtin_amdgcn_rcpf(bfhi(sr.x));
            float e0 = bflo(xr) - mb2, e1 = bfhi(xr) - mb2;
            PIN(e0); PIN(e1);
            e0 = rbf(e0) * rc2; e1 = rbf(e1) * rc2;
            PIN(e0); PIN(e1);
            f2 o0; o0.x = rbf(e0); o0.y = rbf(e1);
#pragma unroll
            for (int h = 0; h < 2; ++h) {
                *(lptr32)(uintptr_t)(a_xn + h * (XN_S + 4)) = __float_as_uint(o0.x); *(lptr32)(uintptr_t)(a_xn + h * (XN_S + 4) + 8) = __float_as_uint(o0.y);
            }
            WAVE_SYNC();
            MEM_FENCE(); SCHED_FENCE();
            STAMP(3);
        }
        const u32 stf = __builtin_amdgcn_readlane(sr.x, 32 * (hw >> 1));
        auto reduce_send = [&](f4 a0, auto off_c, int ci, u32 voff) __attribute__((always_inline)) {
            constexpr int OFF = decltype(off_c)::value;
            *(__attribute__((address_space(3))) f4*)(uintptr_t)(a_red + hw * IMG + OFF) = a0;
            u32 old; f4 r[4];
            const u32 one = 1u;
            asm volatile("s_mov_b64 exec, 1\n\tds_add_rtn_u32 %0, %5, %6\n\ts_mov_b64 exec, -1\n\tds_read_b128 %1, %7 offset:%8\n\tds_read_b128 %2, %7 offset:%8+%9\n\t"
                         "ds_read_b128 %3, %7 offset:%8+2*%9\n\tds_read_b128 %4, %7 offset:%8+3*%9\n\ts_waitcnt lgkmcnt(0)"
                         : "=&v"(old), "=&v"(r[0]), "=&v"(r[1]), "=&v"(r[2]), "=&v"(r[3]) : "v"(a_cnt + 4 * ci), "v"(one), "v"(a_red), "n"(OFF), "n"(IMG) : "memory");
            if (__builtin_amdgcn_readfirstlane(old) == 3u) {
                f4 t;
#pragma unroll
                for (int i = 0; i < 4; ++i) { float t0 = r[0][i] + r[1][i], t1 = r[2][i] + r[3][i]; PIN(t0); PIN(t1); t0 += t1; PIN(t0); t[i] = t0; }
                __builtin_amdgcn_raw_buffer_store_b128(tag4(t, vtag), HRS, voff, 0, 16);
                MEM_FENCE(); SCHED_FENCE();
                lds_add1a(a_cnt + 4 * ci);
            }
        };
        auto load_wd = [&](int j0, int j1) __attribute__((always_inline)) {
            const rsrc_t drs = mk_rsrc(ka3[0], ka3[1], 0, SD * SF * 2);
            MEM_FENCE(); SCHED_FENCE();
#pragma unroll
            for (int j = j0; j < j1; ++j) {
                wdv[j] = BLD128(drs, dvo, dso[j], 2);
            }
            wdv[7] = u4{0, 0, 0, 0};
            MEM_FENCE(); SCHED_FENCE();
        };
        load_wd(0, WD_A);
        STAMP(6);
        const rsrc_t hrs = HRS;
        {
            f4 a0 = {0, 0, 0, 0};
            u4 Af[4], Ag[4];
#pragma unroll
            for (int t = 0; t < 4; ++t) Ag[t] = *(__attribute__((address_space(3))) const u4*)(uintptr_t)(aoff + 64 * t);
#pragma unroll
            for (int t = 0; t < 4; ++t) Af[t] = *(__attribute__((address_space(3))) const u4*)(uintptr_t)(aoff + 256 + 64 * t);
#pragma unroll
            for (int t = 0; t < 4; ++t) a0 = smfma(Ag[t], wv[2 * t], wv[2 * t + 1], a0, idx);
#pragma unroll
            for (int t = 0; t < 4; ++t) a0 = smfma(Af[t], wv[8 + 2 * t], wv[9 + 2 * t], a0, idx);
            STAMP(7);
            reduce_send(a0, std::integral_constant<int, 288>{}, 19, gu_off);
        }
        MEM_FENCE(); SCHED_FENCE();
        STAMP(9);
        lds_wait(cnt + 19, 5);
        load_wd(WD_A, 7);
        if (blockIdx.x == 0 && tid == 256)
            __hip_atomic_store((__attribute__((address_space(1))) u32*)((((unsigned long long)ka0[5] << 32) | ka0[4]) + WS_EPOCH), tag, __ATOMIC_RELAXED, __HIP_MEMORY_SCOPE_AGENT);
        const u32 keyf = hw < 2 ? k1 : k2;
        const int tokf = (0xffff - (int)(keyf & 0xffffu)) & (SL - 1), ftile = tokf >> 8;
        const bool mine = hf ? (ftile >= 2 && ftile <= 5) : (ftile < 2 || ftile > 5);
        const int chf = 4 * (hw & 1) + q;
        const u32 eoff = (tokf * SD + g * 8 + chf) * 2;
        const u32 xe = __builtin_amdgcn_raw_buffer_load_b16(XRS, mine ? eoff : 0x80000000u, 0, 0);
        MEM_FENCE(); SCHED_FENCE();
        __builtin_amdgcn_s_sleep(A_DELAY);
        STAMP(11);
        {
            u4 pv[2];
            poll_pieces<2, A_SLEEP>(pv, [&](int i) { return BLD128(hrs, aoffs + 1024 * i, 0, 16); }, [&](u4 v) { return tagof(v) != tag; });
            STAMP(12);
            int r[2];
#pragma unroll
            for (int i = 0; i < 2; ++i) {
                const u4 p = untag4(pv[i]);
                r[i] = __float_as_int(rbf(fold4(__uint_as_float(p[0]), __uint_as_float(p[2]), __uint_as_float(p[1]), __uint_as_float(p[3]))));
            }
            const float gv = __int_as_float(__builtin_amdgcn_update_dpp(r[0], r[1], 0x128, 0xf, 0xc, false));
            const float up = __int_as_float(__builtin_amdgcn_update_dpp(r[1], r[0], 0x128, 0xf, 0x3, false));
            const u32 hv = __float_as_uint(rbf(rbf(silu_fast(gv)) * up));
            *(lptr32)(uintptr_t)h_st = hv; *(lptr32)(uintptr_t)(h_st + XN_S + 4) = hv;
            WAVE_SYNC();
            u4 Ah[4];
#pragma unroll
            for (int t = 0; t < 4; ++t) Ah[t] = *(__attribute__((address_space(3))) const u4*)(uintptr_t)(aoff + 64 * t);
            f4 a0 = {0, 0, 0, 0};
#pragma unroll
            for (int t = 0; t < 4; ++t) a0 = smfma(Ah[t], wdv[2 * t], wdv[2 * t + 1], a0, idx);
            WAVE_SYNC();
            reduce_send(a0, std::integral_constant<int, 0>{}, 18, dn_off);
        }
        MEM_FENCE(); SCHED_FENCE();
        STAMP(13);
        if (mine) {
            u32 boff = lane < 32 ? (GU_WORDS * 4) + ((lane * 8 + (g >> 4)) * 64 + hw * 16 + (g & 15)) * 16 : 0x80000000u;
            const u32 rb = (keyf & 0x80000000u) ? (keyf & 0x7fff0000u) : (~keyf & 0xffff0000u);
            float rs = rbf(__builtin_amdgcn_rcpf(1.0f + __builtin_amdgcn_exp2f(-LOG2E * __uint_as_float(rb))));
            const int oi = hf ? ftile - 2 : (ftile < 2 ? ftile : ftile - 4);
            float xd = bflo(xe);
            PIN(xd);
            xd -= bflo(stf);
            PIN(xd);
            float xnv = rbf(rbf(xd) * __builtin_amdgcn_rcpf(bfhi(stf)));
            const int tt = tokf & 255;
            u32 ya = sm0 + L_STG + chf * STG_BUF + 1024 * oi + 64 * (tt >> 4) + 16 * (((tt >> 2) & 3) ^ ((tt >> 5) & 3)) + 4 * (tt & 3);
            u32 yc = a_cnt + 8 + 8 * oi + chf;
            u32 eo = (al == 0) ? eoff : 0x80000000u;
            PIN(boff); PIN(rs); PIN(xnv); PIN(ya); PIN(yc); PIN(eo);
            MEM_FENCE(); SCHED_FENCE();
            __builtin_amdgcn_s_sleep(B_DELAY);
            u4 pv = BLD128(hrs, boff, 0, 16);
#pragma unroll 1
            for (int spin = 0; spin < SPIN_MAX; ++spin) {
                const bool b0 = tagof(pv) != tag && lane < 32;
                if (!__any(b0)) break;
                MEM_FENCE();
                __builtin_amdgcn_s_sleep(B_SLEEP);
                if (b0) pv = BLD128(hrs, boff, 0, 16);
            }
            STAMP(14);
            float sv; u32 c;
            asm volatile("ds_read_u8 %0, %2\n\tds_read_b32 %1, %3" : "=&v"(c), "=&v"(sv) : "v"(yc), "v"(ya) : "memory");
            SCHED_FENCE();
            pv = untag4(pv);
            const float tot = row_sum(fold4(__uint_as_float(pv[0]), __uint_as_float(pv[2]), __uint_as_float(pv[1]), __uint_as_float(pv[3])));
            float gt = LOG2E * silu_fast(rbf(xnv + rbf(rbf(tot) * rs)));
            asm volatile("s_waitcnt lgkmcnt(0)" : "+v"(c), "+v"(sv), "+v"(gt) :: "memory");
            while (__any(c == 0u)) {
                __builtin_amdgcn_s_sleep(1);
                asm volatile("ds_read_u8 %0, %2\n\tds_read_b32 %1, %3\n\ts_waitcnt lgkmcnt(0)" : "=&v"(c), "=&v"(sv) : "v"(yc), "v"(ya) : "memory");
            }
            const float ov = __builtin_fmaf(sv, gt, bflo(xe));
            __builtin_amdgcn_raw_buffer_store_b16((u16)pkbf(ov, ov), ORS, eo, 0, 0);
        }
    };
    if (wave < 4) {
        e_role();
    } else if (wave < 8) {
        f_role();
    } else {
        ARGS_LOAD();
        const int cw = wave - 8;
        __builtin_amdgcn_s_setprio(2);
        const int d = g * 8 + cw;
        char* ch = smem + L_CH + cw * CH_SZ;
        u32 vp = d * (SN * 2) + (tid & 63) * 4, vd = d * 2;
        PIN(vp); PIN(vd);
        ARGS_WAIT();
        const u32 pDT = __builtin_amdgcn_raw_buffer_load_b16(mk_rsrc(ka2[0], ka2[1], 0, SD * 2), vd, 0, 0);
        const u32 pLA = __builtin_amdgcn_raw_buffer_load_b32(mk_rsrc(ka1[0], ka1[1], 0, SD * SN * 2), vp, 0, 2);
        const u32 pAI = __builtin_amdgcn_raw_buffer_load_b32(mk_rsrc(ka1[2], ka1[3], 0, SD * SN * 2), vp, 0, 2);
        const u32 pCR = __builtin_amdgcn_raw_buffer_load_b32(mk_rsrc(ka1[4], ka1[5], 0, SD * SN * 2), vp, 0, 2);
        const u32 pCI = __builtin_amdgcn_raw_buffer_load_b32(mk_rsrc(ka1[6], ka1[7], 0, SD * SN * 2), vp, 0, 2);
        const u32 pDS = __builtin_amdgcn_raw_buffer_load_b16(mk_rsrc(ka2[2], ka2[3], 0, SD * 2), vd, 0, 0);
        STAMP(4);
        URGENT_DONE();
        int lane = tid & 63;
        PIN(lane);
        const int al = lane & 15, q = lane >> 4;
        auto clear = [&](char* c, int n) {
            const u4 z = {0, 0, 0, 0};
#pragma unroll
            for (int i = 0; i < 4; ++i) *reinterpret_cast<u4*>(c + i * 1024 + lane * 16) = z;
            if (lane < n) *reinterpret_cast<u4*>(c + 4096 + lane * 16) = z;
        };
        auto put4 = [&](char* c, int idx, u32 lo, u32 hi) {
            u16* rec = reinterpret_cast<u16*>(c);
            u16* rec1 = reinterpret_cast<u16*>(c + REC_B + 2);
            u2 o; o.x = lo; o.y = hi;
            *reinterpret_cast<u2*>(rec + idx) = o;
            rec1[idx] = (u16)lo;
            *reinterpret_cast<u32*>(rec1 + idx + 1) = (lo >> 16) | (hi << 16);
            rec1[idx + 3] = (u16)(hi >> 16);
        };
        const u32 chb = sm0 + L_CH + cw * CH_SZ, nul = sm0 + L_NUL + lane * 4;
        u32 sc_x = R_KA + ZA - 3 - 128 - 256 * (q >> 1) - 4 * (q & 1) - 8 * al;
        u32 sc_2 = chb + (R_K2 + 190 - 32 * (q >> 1) - 64 * (q & 1) - 2 * al) * 2, sc_h = chb + (R_K3 + 127 - 16 - 32 * q - al) * 2, sc_l = lane ? sc_h + 32 : nul;
        const int pj = (lane < 18) ? (lane >= 9 ? lane - 9 : lane) : (lane >= 21 ? lane - 21 : lane - 18), pcopy = (lane < 18) ? (lane >= 9) : (lane >= 21);
        u32 sc_p = lane < 24 ? chb + ((lane < 18) ? (R_KP + 1055 - 1027 - pj) : (R_KA + ZA - 514 + pj)) * 2 + (pcopy ? REC_B + 2 : 0) : nul;
        int pcls = (lane < 18) ? (pj < 2 ? 0 : 1) : 2;
        const u32 stg = sm0 + L_STG + cw * STG_BUF + q * 16;
        u32 fr_b = stg + al * STG_ROW, fr_z = stg + 16 * STG_ROW;
        u32 fr_x = stg + (al < 8 ? al : 8 + (((al & 3) == 2) ? ((al - 8) >> 1) : (2 * ((al - 7) >> 2)))) * STG_ROW;
        u32 fr_y = stg + ((al & 10) == 0 ? 13 + (al >> 1) + (al & 1) - ((al >> 1) & 1) : (al >> 2)) * STG_ROW;
        asm volatile("s_mov_b32 m0, %0\n\ts_nop 0" :: "s"(sm0 + L_STG + cw * STG_BUF) : "memory");
        {
            hcx one; one.r = h2{(f16)1.0f, (f16)1.0f}; one.i = h2{(f16)0.0f, (f16)0.0f};
            stg_put<0>(one);
        }
        clear(ch, 40);
        PIN(sc_x); PIN(sc_2); PIN(sc_h); PIN(sc_l); PIN(sc_p); PIN(pcls); PIN(fr_b); PIN(fr_z); PIN(fr_x); PIN(fr_y);
        MEM_FENCE(); SCHED_FENCE();
        const float dt = rbf(exp_acc(bflo(pDT)));
        f2 ar, ai, cr, ci;
        {
            f2 la = bfup(pLA);
            f2 ex; ex.x = __builtin_amdgcn_exp2f(la.x * LOG2E); ex.y = __builtin_amdgcn_exp2f(la.y * LOG2E);
            ar = (-rbf2(ex) * dt) * LOG2E;
            ai = (bfup(pAI) * dt) * 0.15915494309189535f;
            cr = bfup(pCR) * dt;
            ci = bfup(pCI) * dt;
        }
        asm volatile("" :: "v"(code_pf));
        STAMP(1);
        const float bound = __builtin_fmaf(2.0f, wave_sum(fabsf(cr.x) + fabsf(cr.y) + fabsf(ci.x) + fabsf(ci.y)), fabsf(bflo(pDS)));
        int ke = 14 - ((int)((__float_as_uint(bound) >> 23) & 0xff) - 127);
        ke = __builtin_amdgcn_readfirstlane(min(max(ke, -100), 100));
        const float sig2 = __uint_as_float((u32)(127 + ke + 1) << 23);
        const float yscale = __uint_as_float((u32)(127 - ke) << 23) * (1.0f / (LOG2E * LOG2E));
        cplx G0; G0.r = cr * sig2; G0.i = -ci * sig2;
        float skip = (lane == 0) ? 0.5f * sig2 * bflo(pDS) : 0.0f;
        PIN(skip);
        typedef __attribute__((address_space(3))) u16* lptr16;
        {
            typedef __attribute__((address_space(3))) const u4* lq;
            const cplx E1 = cexp2(ar, ai, 1.0f), E2 = csq(E1), E4 = csq(E2), E8 = csq(E4);
            const hcx z1 = toh(E1), z2 = toh(E2), z4 = toh(E4), z8 = toh(E8);
            {
                const cplx E16 = csq(E8), E32 = csq(E16), E64 = csq(E32);
                hcx c[8];
                c[1] = z8; c[2] = toh(E16); c[4] = toh(E32);
                const hcx p64 = toh(E64), p128 = toh(csq(E64));
                c[3] = hmul(c[1], c[2]);
                stg_put<1>(c[1]); stg_put<2>(c[2]); stg_put<4>(c[4]); stg_put<8>(p64);
                c[5] = hmul(c[1], c[4]); c[6] = hmul(c[2], c[4]); c[7] = hmul(c[3], c[4]);
                stg_put<16>(p128); stg_put<3>(c[3]);
                const hcx t9 = hmul(c[1], p64), t10 = hmul(c[2], p64);
                stg_put<5>(c[5]); stg_put<6>(c[6]);
                const hcx t11 = hmul(c[3], p64), t12 = hmul(c[4], p64);
                stg_put<7>(c[7]); stg_put<9>(t9);
                const hcx t13 = hmul(c[5], p64), t14 = hmul(c[6], p64);
                stg_put<10>(t10); stg_put<11>(t11);
                const hcx t15 = hmul(c[7], p64);
                stg_put<12>(t12); stg_put<13>(t13); stg_put<14>(t14); stg_put<15>(t15);
            }
            WAVE_SYNC();
            u4 B0[8], B1[8], Af[8];
#pragma unroll
            for (int s = 0; s < 8; ++s) B0[s] = *(lq)(uintptr_t)(fr_b + s * 64);
#pragma unroll
            for (int s = 0; s < 8; ++s) B1[s] = *(lq)(uintptr_t)(fr_z + s * 64);
            WAVE_SYNC();
            STAMP(8);
            __builtin_amdgcn_s_sleep(K_S1);
            if (cw < 4) __builtin_amdgcn_s_setprio(1);
            hcx J[4];
            {
                const cplx F1 = cexp2(ar, ai, 256.0f), F2 = csq(F1);
                const hcx f1 = toh(F1), f2_ = toh(F2), f4_ = toh(csq(F2));
                const hcx g0 = toh(G0), g1 = hmul_conj(g0, z1), g2 = hmul_conj(g0, z2), g4 = hmul_conj(g0, z4), g8 = hmul_conj(g0, z8);
                stg_put<0>(g0); stg_put<1>(g1);
                const hcx g3 = hmul_conj(g1, z2), g5 = hmul_conj(g1, z4);
                stg_put<2>(g2); stg_put<4>(g4);
                const hcx g6 = hmul_conj(g2, z4), g7 = hmul_conj(g3, z4);
                stg_put<3>(g3); stg_put<5>(g5);
                const hcx h0 = hmul_conj(g0, f1), h2_ = hmul_conj(g2, f1);
                stg_put<6>(g6); stg_put<7>(g7);
                const hcx h4 = hmul_conj(g4, f1), h6 = hmul_conj(g6, f1);
                stg_put<8>(h0); stg_put<9>(h2_);
                const hcx j1 = hmul_conj(g8, f1), k2 = hmul_conj(g4, f2_);
                stg_put<10>(h4); stg_put<11>(h6);
                const hcx j2 = hmul_conj(g8, f2_), k3 = hmul_conj(h4, f2_);
                stg_put<12>(j1); stg_put<13>(k2);
                const hcx j3 = hmul_conj(j1, f2_);
                J[0] = hmul_conj(g8, f4_);
                stg_put<14>(j2); stg_put<15>(k3);
                J[1] = hmul_conj(j1, f4_); J[2] = hmul_conj(j2, f4_);
                stg_put<16>(j3);
                J[3] = hmul_conj(j3, f4_);
            }
            WAVE_SYNC();
#pragma unroll
            for (int s = 0; s < 8; ++s) Af[s] = *(lq)(uintptr_t)(fr_x + s * 64);
            WAVE_SYNC();
            stg_put<0>(J[0]); stg_put<1>(J[1]); stg_put<2>(J[2]); stg_put<3>(J[3]);
            STAMP(9);
            __builtin_amdgcn_s_setprio(1);
            f4 X0 = {0, 0, 0, 0}, X1 = {0, 0, 0, 0}, Y0 = {0, 0, 0, 0}, Y1 = {0, 0, 0, 0};
            X0[0] = skip;
            auto mf = [](u4 a, u4 b, f4 c) { return mfma16(__builtin_bit_cast(h8, a), __builtin_bit_cast(h8, b), c); };
#pragma unroll
            for (int s = 0; s < 4; ++s) {
#pragma unroll
                for (int i = 0; i < 4; ++i) {
                    u32 zr = B1[s][i], zi = B1[s + 4][i], t;
                    asm volatile("v_pk_mul_f16 %2, %4, %1\n\tv_pk_mul_f16 %1, %3, %1\n\tv_pk_fma_f16 %1, %4, %0, %1\n\tv_pk_fma_f16 %0, %3, %0, %2 neg_lo:[0,0,1] neg_hi:[0,0,1]"
                                 : "+v"(zr), "+v"(zi), "=&v"(t) : "v"(B0[s][i]), "v"(B0[s + 4][i]));
                    B1[s][i] = zr; B1[s + 4][i] = zi;
                }
                X0 = mf(Af[2 * s], B0[2 * s], X0); X0 = mf(Af[2 * s + 1], B0[2 * s + 1], X0);
                asm volatile("" :: "v"(X0));
                SCHED_FENCE();
            }
            STAMP(10);
            WAVE_SYNC();
#pragma unroll
            for (int s = 0; s < 8; ++s) { X1 = mf(Af[s], B1[s], X1); asm volatile("" :: "v"(X1) : "memory"); Af[s] = *(lq)(uintptr_t)(fr_y + s * 64); SCHED_FENCE(); }
            put4(ch, sc_x + 128, pkh_r(X0[3], X0[2]), pkh_r(X0[1], X0[0]));
            STAMP(11);
#pragma unroll
            for (int s = 0; s < 8; ++s) { Y0 = mf(Af[s], B0[s], Y0); Y1 = mf(Af[s], B1[s], Y1); }
            const u32 xl = pkh_r(X1[3], X1[2]);
            put4(ch, sc_x, xl, pkh_r(X1[1], X1[0]));
            STAMP(2);
            const u32 y01 = __builtin_amdgcn_permlane32_swap(pkh_r(Y0[1], Y0[0]), pkh_r(Y1[1], Y1[0]), false, false)[0];
            const u32 y3 = pkh(Y0[2], Y1[2]);
            *(lptr32)(uintptr_t)sc_2 = y01;
            *(lptr16)(uintptr_t)sc_l = (u16)y3; *(lptr16)(uintptr_t)sc_h = (u16)(y3 >> 16);
            const u32 v0 = __builtin_amdgcn_readlane(y01, 63), v1 = __builtin_amdgcn_readlane(y3, 0), v2 = __builtin_amdgcn_readlane(xl, 63);
            *(lptr16)(uintptr_t)sc_p = (u16)((pcls == 0) ? v0 : (pcls == 1) ? v1 : v2);
            WAVE_SYNC();
            STAMP(3);
        }
        __builtin_amdgcn_s_setprio(2);
        lds_wait(cnt + 22, 4);
        STAMP(5);
        conv(hf, sm0, cw, yscale);
        STAMP(7);

    }
}

extern "C" void kernel_launch(void* const* d_in, const int* in_sizes, int n_in,
                              void* d_out, int out_size, void* d_ws, size_t ws_size,
                              hipStream_t stream) {
    char* p = (char*)d_ws + ((WS_OFF < 0 ? ws_size + (long long)WS_OFF * 1048576ll : (size_t)WS_OFF * 1048576ull) & ~(size_t)255);
    static_assert(WS_OFF >= 0 || WS_END <= (size_t)(-WS_OFF) * 1048576, "workspace");
    k_fused<<<256, 1024, 0, stream>>>((const u16*)d_in[0], (const u16*)d_in[8], p, (u16*)d_out,
                                      (const u16*)d_in[1], (const u16*)d_in[2], (const u16*)d_in[3], (const u16*)d_in[4],
                                      (const u16*)d_in[5], (const u16*)d_in[6], (const u16*)d_in[9], (const u16*)d_in[10], (const u16*)d_in[11]);
}
```

```cpp
#include <hip/hip_runtime.h>
#include <stdint.h>
#include <type_traits>

typedef uint16_t u16;
typedef uint32_t u32;
typedef _Float16 f16;
typedef __attribute__((ext_vector_type(8))) _Float16 h8;
typedef __attribute__((ext_vector_type(2))) _Float16 h2;
typedef __attribute__((ext_vector_type(2))) __bf16 b2;
typedef __attribute__((ext_vector_type(8))) __bf16 b8;
typedef __attribute__((ext_vector_type(4))) float f4;
typedef __attribute__((ext_vector_type(2))) float f2;
typedef __attribute__((ext_vector_type(4))) u32 u4;
typedef __attribute__((ext_vector_type(2))) u32 u2;

constexpr int SL = 2048, SD = 1024, SN = 128, SF = 3584;
constexpr int GU_WORDS = 128 * 2 * 4 * 2 * 8 * 4;
constexpr int DN_WORDS = 32 * 8 * 64 * 4;
#define SPIN_LOG 20
constexpr int SPIN_MAX = 1 << SPIN_LOG;

#define DEV __device__ __forceinline__
#define STAMP_DECL
#define STAMP(k)
#define STAMP_FLUSH(kn)
#define STAMP_ARG
#define STAMP_PAR
#define WAVE_SYNC() do { __builtin_amdgcn_fence(__ATOMIC_ACQ_REL, "wavefront"); __builtin_amdgcn_wave_barrier(); } while (0)
#define PIN(v) asm volatile("" : "+v"(v))
#define SCHED_FENCE() __builtin_amdgcn_sched_barrier(0)
#define MEM_FENCE() asm volatile("" ::: "memory")

DEV float bflo(u32 v) { return __uint_as_float(v << 16); }
DEV float bfhi(u32 v) { return __uint_as_float(v & 0xffff0000u); }
DEV f2 bfup(u32 v) { f2 r; r.x = bflo(v); r.y = bfhi(v); return r; }
DEV u32 pkbf(f2 v) { b2 t = __builtin_convertvector(v, b2); return __builtin_bit_cast(u32, t); }
DEV u32 pkbf(float a, float b) { f2 v; v.x = a; v.y = b; return pkbf(v); }
DEV u32 pkh(f2 v) { h2 t = __builtin_convertvector(v, h2); return __builtin_bit_cast(u32, t); }
DEV u32 pkh(float a, float b) { f2 v; v.x = a; v.y = b; return pkh(v); }
DEV u32 pkh_r(float a, float b) { PIN(a); return pkh(a, b); }
DEV float rbf(float a) { return __uint_as_float(pkbf(0.0f, a)); }
DEV f2 rbf2(f2 a) { f2 r; r.x = rbf(a.x); r.y = rbf(a.y); return r; }
#define KNOB(name, value) constexpr int name = value
KNOB(NREP, 16);
KNOB(WS_OFF, -8);
KNOB(CODE_B, 24000);
KNOB(W_A, 4);
KNOB(W_B, 2);
KNOB(W_E, 5);
KNOB(W_PACE, 6);
KNOB(W_PACE2, 4);
KNOB(WD_A, 2);
KNOB(G_DELAY, 12);
KNOB(G_SLEEP, 2);
KNOB(U_DELAY, 22);
KNOB(A_DELAY, 12);
KNOB(A_SLEEP, 4);
KNOB(B_DELAY, 20);
KNOB(B_SLEEP, 2);
KNOB(K_S1, 12);
KNOB(PACE, 4);
KNOB(E_UP, 1);
constexpr float LOG2E = 1.4426950408889634f;
DEV float silu_fast(float a) { return a * __builtin_amdgcn_rcpf(1.0f + __builtin_amdgcn_exp2f(-1.4426950408889634f * a)); }

template <int CTRL, int RMASK>
DEV float dpp_add(float v) {
    int t = __builtin_amdgcn_update_dpp(0, __float_as_int(v), CTRL, RMASK, 0xf, false);
    return v + __int_as_float(t);
}
DEV float wave_sum(float v) {
    v = dpp_add<0x111, 0xf>(v);
    v = dpp_add<0x112, 0xf>(v);
    v = dpp_add<0x114, 0xf>(v);
    v = dpp_add<0x118, 0xf>(v);
    v = dpp_add<0x142, 0xa>(v);
    v = dpp_add<0x143, 0xc>(v);
    return __int_as_float(__builtin_amdgcn_readlane(__float_as_int(v), 63));
}
DEV float fold4(float a, float b, float c, float d) {
    const auto s0 = __builtin_amdgcn_permlane32_swap(__float_as_uint(a), __float_as_uint(b), false, false);
    const auto s1 = __builtin_amdgcn_permlane32_swap(__float_as_uint(c), __float_as_uint(d), false, false);
    const u32 w0 = __float_as_uint(__uint_as_float(s0[0]) + __uint_as_float(s0[1]));
    const u32 w1 = __float_as_uint(__uint_as_float(s1[0]) + __uint_as_float(s1[1]));
    const auto s2 = __builtin_amdgcn_permlane16_swap(w0, w1, false, false);
    return __uint_as_float(s2[0]) + __uint_as_float(s2[1]);
}
DEV float row_sum(float v) {
    v = dpp_add<0x128, 0xf>(v);
    v = dpp_add<0x124, 0xf>(v);
    v = dpp_add<0x122, 0xf>(v);
    return dpp_add<0x121, 0xf>(v);
}
#define TOP2(ctl) "v_max_u32_dpp %2, %1, %1 " ctl "\n\tv_min_u32_dpp %3, %0, %0 " ctl "\n\tv_max_u32_dpp %0, %0, %0 " ctl "\n\tv_max_u32 %1, %2, %3\n\ts_nop 1\n\t"
DEV void wave_top2(u32& m1, u32& m2) {
    u32 t, s;
    asm volatile("s_nop 1\n\t"
                 TOP2("row_shr:1 row_mask:0xf bank_mask:0xf bound_ctrl:1") TOP2("row_shr:2 row_mask:0xf bank_mask:0xf bound_ctrl:1")
                 TOP2("row_shr:4 row_mask:0xf bank_mask:0xf bound_ctrl:1") TOP2("row_shr:8 row_mask:0xf bank_mask:0xf bound_ctrl:1")
                 TOP2("row_bcast:15 row_mask:0xa bank_mask:0xf") TOP2("row_bcast:31 row_mask:0xc bank_mask:0xf")
                 : "+v"(m1), "+v"(m2), "=&v"(t), "=&v"(s));
}
DEV void top2_of8(u32& m1, u32& m2) {
    u32 t, s;
    asm volatile("s_nop 1\n\t"
                 TOP2("row_shr:1 row_mask:0xf bank_mask:0xf bound_ctrl:1") TOP2("row_shr:2 row_mask:0xf bank_mask:0xf bound_ctrl:1")
                 TOP2("row_shr:4 row_mask:0xf bank_mask:0xf bound_ctrl:1")
                 : "+v"(m1), "+v"(m2), "=&v"(t), "=&v"(s));
}
DEV u32 med3u(u32 a, u32 b, u32 c) { return max(min(a, b), min(max(a, b), c)); }
DEV float dot2(u32 a, u32 b, float c) { return __builtin_amdgcn_fdot2_f32_bf16(__builtin_bit_cast(b2, a), __builtin_bit_cast(b2, b), c, false); }

template <int CTRL> DEV float dpp_mov(float v) { return __int_as_float(__builtin_amdgcn_mov_dpp(__float_as_int(v), CTRL, 0xf, 0xf, true)); }
DEV u32 bfi(u32 m, u32 a, u32 b) { u32 r; asm("v_bfi_b32 %0, %1, %2, %3" : "=v"(r) : "v"(m), "v"(a), "v"(b)); return r; }
DEV void gate4(u32 (&g)[4], u32 u0, u32 u1, u32 u2, u32 u3) {
#define LO "dst_sel:WORD_0 dst_unused:UNUSED_PRESERVE src0_sel:WORD_0\n\t"
#define HI "dst_sel:WORD_1 dst_unused:UNUSED_PRESERVE src0_sel:WORD_1\n\t"
    asm volatile("v_exp_f16_sdwa %0, -%4 " LO "v_exp_f16_sdwa %1, -%5 " LO "v_exp_f16_sdwa %2, -%6 " LO "v_exp_f16_sdwa %3, -%7 " LO
                 "v_exp_f16_sdwa %0, -%4 " HI "v_exp_f16_sdwa %1, -%5 " HI "v_exp_f16_sdwa %2, -%6 " HI "v_exp_f16_sdwa %3, -%7 " HI
                 "v_pk_add_f16 %0, %0, 1.0 op_sel_hi:[1,0]\n\tv_pk_add_f16 %1, %1, 1.0 op_sel_hi:[1,0]\n\tv_pk_add_f16 %2, %2, 1.0 op_sel_hi:[1,0]\n\tv_pk_add_f16 %3, %3, 1.0 op_sel_hi:[1,0]\n\t"
                 "v_rcp_f16_sdwa %0, %0 " LO "v_rcp_f16_sdwa %1, %1 " LO "v_rcp_f16_sdwa %2, %2 " LO "v_rcp_f16_sdwa %3, %3 " LO
                 "v_rcp_f16_sdwa %0, %0 " HI "v_rcp_f16_sdwa %1, %1 " HI "v_rcp_f16_sdwa %2, %2 " HI "v_rcp_f16_sdwa %3, %3 " HI
                 "v_pk_mul_f16 %0, %0, %4\n\tv_pk_mul_f16 %1, %1, %5\n\tv_pk_mul_f16 %2, %2, %6\n\tv_pk_mul_f16 %3, %3, %7"
                 : "=&v"(g[0]), "=&v"(g[1]), "=&v"(g[2]), "=&v"(g[3]) : "v"(u0), "v"(u1), "v"(u2), "v"(u3));
#undef LO
#undef HI
}
struct cplx { f2 r, i; };
DEV cplx cexp2(f2 ar, f2 ai, float p) {
    cplx z;
    const f2 am = ar * p, rv = ai * p;
    f2 mag, c, s;
#pragma unroll
    for (int k = 0; k < 2; ++k) {
        mag[k] = __builtin_amdgcn_exp2f(am[k]);
        const float rev = __builtin_amdgcn_fractf(rv[k]);
        c[k] = __builtin_amdgcn_cosf(rev);
        s[k] = __builtin_amdgcn_sinf(rev);
    }
    z.r = mag * c; z.i = mag * s;
    return z;
}
DEV float exp_acc(float x) {
    x = __builtin_amdgcn_fmed3f(x, -150.0f, 150.0f);
    const float t = x * 1.4426950408889634f;
    const float r = __builtin_fmaf(x, 1.925963033500011e-08f, __builtin_fmaf(x, 1.4426950408889634f, -t));
    return __builtin_amdgcn_exp2f(t) * __builtin_fmaf(r, 0.6931471805599453f, 1.0f);
}
DEV f4 mfma16(h8 a, h8 b, f4 c) { return __builtin_amdgcn_mfma_f32_16x16x32_f16(a, b, c, 0, 0, 0); }
DEV f4 mfma16b(u4 a, u4 b, f4 c) {
    return __builtin_amdgcn_mfma_f32_16x16x32_bf16(__builtin_bit_cast(b8, a), __builtin_bit_cast(b8, b), c, 0, 0, 0);
}
typedef __attribute__((ext_vector_type(16))) __bf16 b16v;
DEV f4 smfma(u4 a, u4 b0, u4 b1, f4 c, u32 idx) {
    return __builtin_amdgcn_smfmac_f32_16x16x64_bf16(__builtin_bit_cast(b8, a), __builtin_bit_cast(b16v, __builtin_shufflevector(b0, b1, 0, 1, 2, 3, 4, 5, 6, 7)), c, (int)idx, 0, 0);
}
DEV void lds_add1(u32* p) {
    const u32 a = (u32)(uintptr_t)(__attribute__((address_space(3))) u32*)p, one = 1u;
    asm volatile("s_mov_b64 exec, 1\n\tds_add_u32 %0, %1\n\ts_mov_b64 exec, -1" :: "v"(a), "v"(one) : "memory");
}
template <int SLP = 1>
DEV void lds_wait(u32* cnt, u32 n) {
    while (__hip_atomic_load(cnt, __ATOMIC_RELAXED, __HIP_MEMORY_SCOPE_WORKGROUP) < n) __builtin_amdgcn_s_sleep(SLP);
    __builtin_amdgcn_fence(__ATOMIC_ACQUIRE, "wavefront");
    __builtin_amdgcn_wave_barrier();
}

constexpr int KREC = 1024;
constexpr int R_KA = 0, ZA = 543, R_K2 = 560, R_K3 = 812, R_KP = 972;
constexpr int STG_ROW = 544;
constexpr int STG_BUF = 37 * 256;
constexpr int REC_B = KREC * 2 + 64;
constexpr int REC_SZ = 2 * KREC * 2 + 128;
constexpr int UPAD = 256;
constexpr int C_U = REC_SZ;
constexpr int CH_SZ = C_U + (UPAD + SL) * 2 + 16;
static_assert(CH_SZ % 32 == 16, "bank offset of the channel regions");
constexpr int L_STG = 0;
constexpr int L_CH = L_STG + 8 * STG_BUF;
constexpr int L_BOX = L_CH + 8 * CH_SZ;
constexpr int XN_S = 512 + 64, IMG = 4 * XN_S;
constexpr int L_Z = L_BOX + 5 * 1024;
constexpr int L_XN = L_Z, Z_SZ = 4 * IMG;
constexpr int L_CNT = L_Z + Z_SZ;
constexpr int L_P12 = L_CNT + 128, P12_S = 80;
constexpr int L_P3 = L_P12 + 8 * P12_S, P3_S = 48;
constexpr int L_NUL = L_P3 + 8 * P3_S;
constexpr int L_TOTAL = L_NUL + 256;
static_assert(L_TOTAL <= 160 * 1024 && Z_SZ % 16 == 0, "LDS");

DEV h8 box_frag(int al, int q, int off, int hw) {
    const u32 m = (u32)((((2ull << (2 * hw)) - 1ull) << (al - 8 * q + off - hw + 32)) >> 32) & 0xffu;
    const u32 t = m | (m << 15);
    u4 r;
#pragma unroll
    for (int i = 0; i < 4; ++i) r[i] = ((t >> (2 * i)) & 0x10001u) * 0x3c00u;
    return __builtin_bit_cast(h8, r);
}
DEV u4 tag4(f4 v, u32 vtag) {
    u4 b = __builtin_bit_cast(u4, v);
#pragma unroll
    for (int i = 0; i < 4; ++i) b[i] = __builtin_amdgcn_perm(b[i], vtag, 0x07060500u + i);
    return b;
}
DEV u32 tagof(u4 v) { return __builtin_amdgcn_perm(v.y, v.x, 0x0c0c0400u) | __builtin_amdgcn_perm(v.w, v.z, 0x04000c0cu); }
DEV u4 untag4(u4 v) { return v & 0xffffff00u; }
template <int N, int SLEEP, class LD, class BAD>
DEV void poll_pieces(u4 (&v)[N], LD ld, BAD bad) {
#pragma unroll
    for (int p = 0; p < N; ++p) v[p] = ld(p);
    MEM_FENCE(); SCHED_FENCE();
    {
        bool any = false;
#pragma unroll
        for (int p = 0; p < N; ++p) any |= bad(v[p]);
        if (__builtin_expect(!__any(any), 1)) return;
    }
    u32 miss = 0;
#pragma unroll
    for (int p = 0; p < N; ++p) if (!__all(!bad(v[p]))) miss |= 1u << p;
#pragma unroll 1
    for (int spin = 0; miss != 0 && spin < SPIN_MAX; ++spin) {
        MEM_FENCE();
        __builtin_amdgcn_s_sleep(SLEEP);
#pragma unroll
        for (int p = 0; p < N; ++p) if ((miss >> p) & 1u) v[p] = ld(p);
#pragma unroll
        for (int p = 0; p < N; ++p) if (((miss >> p) & 1u) && __all(!bad(v[p]))) miss &= ~(1u << p);
        __builtin_amdgcn_s_waitcnt(0x0f70);
    }
}
struct hcx { h2 r, i; };
DEV hcx toh(cplx v) { hcx z; z.r = __builtin_convertvector(v.r, h2); z.i = __builtin_convertvector(v.i, h2); return z; }
DEV hcx hmul(hcx a, hcx b) { hcx z; z.r = __builtin_elementwise_fma(a.r, b.r, -(a.i * b.i)); z.i = __builtin_elementwise_fma(a.r, b.i, a.i * b.r); return z; }
DEV hcx hmul_conj(hcx g, hcx e) { hcx z; z.r = __builtin_elementwise_fma(g.r, e.r, g.i * e.i); z.i = __builtin_elementwise_fma(g.i, e.r, -(g.r * e.i)); return z; }
DEV cplx csq(cplx a) { cplx z; z.r = a.r * a.r - a.i * a.i; z.i = (a.r + a.r) * a.i; return z; }
template <int ROW>
DEV void stg_put(hcx v) {
    asm volatile("ds_write_addtid_b32 %0 offset:%2\n\tds_write_addtid_b32 %1 offset:%3"
                 :: "v"(v.r), "v"(v.i), "n"(ROW * STG_ROW), "n"(ROW * STG_ROW + 256) : "memory");
}
DEV void lds_add1a(u32 a) {
    const u32 one = 1u;
    asm volatile("s_mov_b64 exec, 1\n\tds_add_u32 %0, %1\n\ts_mov_b64 exec, -1" :: "v"(a), "v"(one) : "memory");
}
#define CONV_ASM_0 \
    "v_add_u32 %7, -64, %0\n\tds_read2_b32 v[8:9], %0 offset0:128 offset1:129\n\tds_read2_b32 v[10:11], %0 offset0:130 offset1:131\n\tds_read2_b32 v[12:13], %0 offset0:0 offset1:1\n\t" \
    "ds_read2_b32 v[14:15], %0 offset0:2 offset1:3\n\tds_read2_b32 v[16:17], %0 offset0:144 offset1:145\n\tds_read2_b32 v[18:19], %0 offset0:146 offset1:147\n\t" \
    "ds_read2_b32 v[20:21], %0 offset0:16 offset1:17\n\tds_read2_b32 v[22:23], %0 offset0:18 offset1:19\n\tds_read2_b32 v[24:25], %0 offset0:160 offset1:161\n\t" \
    "ds_read2_b32 v[26:27], %0 offset0:162 offset1:163\n\tds_read2_b32 v[28:29], %0 offset0:32 offset1:33\n\tds_read2_b32 v[30:31], %0 offset0:34 offset1:35\n\t" \
    "ds_read2_b32 v[32:33], %0 offset0:176 offset1:177\n\tds_read2_b32 v[34:35], %0 offset0:178 offset1:179\n\tds_read2_b32 v[36:37], %0 offset0:48 offset1:49\n\t" \
    "ds_read2_b32 v[38:39], %0 offset0:50 offset1:51\n\tds_read2_b32 v[40:41], %0 offset0:192 offset1:193\n\tds_read2_b32 v[42:43], %0 offset0:194 offset1:195\n\t" \
    "ds_read2_b32 v[44:45], %0 offset0:64 offset1:65\n\tds_read2_b32 v[46:47], %0 offset0:66 offset1:67\n\tds_read2_b32 v[48:49], %0 offset0:208 offset1:209\n\t" \
    "ds_read2_b32 v[50:51], %0 offset0:210 offset1:211\n\tds_read2_b32 v[52:53], %0 offset0:80 offset1:81\n\tds_read2_b32 v[54:55], %0 offset0:82 offset1:83\n\t" \
    "ds_read2_b32 v[56:57], %0 offset0:224 offset1:225\n\tds_read2_b32 v[58:59], %0 offset0:226 offset1:227\n\tds_read2_b32 v[60:61], %0 offset0:96 offset1:97\n\t" \
    "ds_read2_b32 v[62:63], %0 offset0:98 offset1:99\n\tds_read2_b32 v[64:65], %0 offset0:240 offset1:241\n\tds_read2_b32 v[66:67], %0 offset0:242 offset1:243\n\t" \
    "ds_read2_b32 v[68:69], %0 offset0:112 offset1:113\n\tds_read2_b32 v[70:71], %0 offset0:114 offset1:115\n\tds_read2_b32 v[72:73], %7 offset0:0 offset1:1\n\t" \
    "ds_read2_b32 v[74:75], %7 offset0:2 offset1:3\n\t" \
    CONV_WAIT \
    "ds_read_b128 v[76:79], %1 offset:32\n\tds_read_b128 v[80:83], %1 offset:96\n\tds_read_b128 v[84:87], %1 offset:160\n\tds_read_b128 v[88:91], %1 offset:224\n\t" \
    "s_waitcnt lgkmcnt(3)\n\tv_mfma_f32_16x16x32_f16 v[92:95], v[8:11], v[76:79], 0\n\tds_read_b128 v[96:99], %1 offset:288\n\tv_mfma_f32_16x16x32_f16 v[100:103], v[12:15], v[76:79], 0\n\t" \
    "s_waitcnt lgkmcnt(3)\n\tv_mfma_f32_16x16x32_f16 v[92:95], v[16:19], v[80:83], v[92:95]\n\tds_read_b128 v[104:107], %1 offset:352\n\tv_mfma_f32_16x16x32_f16 v[100:103], v[20:23], v[80:83], v[100:103]\n\t" \
    "s_waitcnt lgkmcnt(2)\n\tv_mfma_f32_16x16x32_f16 v[92:95], v[24:27], v[84:87], v[92:95]\n\tds_read_b128 v[108:111], %1 offset:416\n\tv_mfma_f32_16x16x32_f16 v[100:103], v[28:31], v[84:87], v[100:103]\n\t" \
    "v_mfma_f32_16x16x32_f16 v[92:95], v[32:35], v[88:91], v[92:95]\n\tds_read_b128 v[112:115], %1 offset:480\n\tv_mfma_f32_16x16x32_f16 v[100:103], v[36:39], v[88:91], v[100:103]\n\t" \
    "s_waitcnt lgkmcnt(2)\n\tv_mfma_f32_16x16x32_f16 v[92:95], v[40:43], v[96:99], v[92:95]\n\tds_read_b128 v[116:119], %1 offset:544\n\tv_mfma_f32_16x16x32_f16 v[100:103], v[44:47], v[96:99], v[100:103]\n\t" \
    "ds_read_b128 v[120:123], %1 offset:608\n\tv_mfma_f32_16x16x32_f16 v[92:95], v[48:51], v[104:107], v[92:95]\n\tds_read_b128 v[124:127], %1 offset:2528\n\t" \
    "v_mfma_f32_16x16x32_f16 v[100:103], v[52:55], v[104:107], v[100:103]\n\tds_read_b128 v[76:79], %1 offset:672\n\ts_waitcnt lgkmcnt(4)\n\tv_mfma_f32_16x16x32_f16 v[92:95], v[56:59], v[108:111], v[92:95]\n\t" \
    "ds_read_b128 v[80:83], %1 offset:2592\n\tv_mfma_f32_16x16x32_f16 v[100:103], v[60:63], v[108:111], v[100:103]\n\tds_read_b128 v[84:87], %1 offset:736\n\t" \
    "v_mfma_f32_16x16x32_f16 v[92:95], v[64:67], v[112:115], v[92:95]\n\tds_read_b128 v[88:91], %1 offset:2656\n\tv_mfma_f32_16x16x32_f16 v[100:103], v[68:71], v[112:115], v[100:103]\n\t" \
    "ds_read_b128 v[96:99], %1 offset:800\n\tds_read_b128 v[104:107], %1 offset:2784\n\ts_waitcnt lgkmcnt(5)\n\tv_mfma_f32_16x16x32_f16 v[100:103], v[8:11], v[116:119], v[100:103]\n\t" \
    "ds_read_b128 v[108:111], %1 offset:2720\n\tds_read_b128 v[112:115], %1 offset:864\n\tv_mfma_f32_16x16x32_f16 v[100:103], v[16:19], v[120:123], v[100:103]\n\t" \
    "v_mul_f32 v92, %8, v92\n\tv_mul_f32 v93, %8, v93\n\tv_mfma_f32_16x16x32_f16 v[116:119], v[72:75], v[124:127], 0\n\tv_mul_f32 v94, %8, v94\n\tv_mul_f32 v95, %8, v95\n\t" \
    "v_mfma_f32_16x16x32_f16 v[100:103], v[24:27], v[76:79], v[100:103]\n\tds_write_b128 %2, v[92:95] offset:0\n\tds_write_b8 %6, %6 offset:0\n\t" \
    CONV_P0 \
    "ds_read_b128 v[120:123], %1 offset:928\n\ts_waitcnt lgkmcnt(6)\n\tv_mfma_f32_16x16x32_f16 v[116:119], v[12:15], v[80:83], v[116:119]\n\tds_read_b128 v[124:127], %1 offset:2848\n\t" \
    "v_mfma_f32_16x16x32_f16 v[100:103], v[32:35], v[84:87], v[100:103]\n\tds_read_b128 v[76:79], %1 offset:992\n\tv_mfma_f32_16x16x32_f16 v[116:119], v[20:23], v[88:91], v[116:119]\n\t" \
    "ds_read_b128 v[80:83], %1 offset:2912\n\tv_mfma_f32_16x16x32_f16 v[100:103], v[40:43], v[96:99], v[100:103]\n\tds_read_b128 v[92:95], %1 offset:2976\n\t" \
    "s_waitcnt lgkmcnt(4)\n\tv_mfma_f32_16x16x32_f16 v[116:119], v[28:31], v[108:111], v[116:119]\n\tds_read_b128 v[84:87], %1 offset:3040\n\tv_mfma_f32_16x16x32_f16 v[100:103], v[48:51], v[112:115], v[100:103]\n\t" \
    "v_mfma_f32_16x16x32_f16 v[116:119], v[36:39], v[104:107], v[116:119]\n\tds_read_b128 v[88:91], %1 offset:3104\n\tv_mfma_f32_16x16x32_f16 v[100:103], v[56:59], v[120:123], v[100:103]\n\t" \
    "s_waitcnt lgkmcnt(2)\n\tv_mfma_f32_16x16x32_f16 v[116:119], v[44:47], v[124:127], v[116:119]\n\tds_read_b128 v[96:99], %1 offset:3168\n\tv_mfma_f32_16x16x32_f16 v[100:103], v[64:67], v[76:79], v[100:103]\n\t" \
    "v_mfma_f32_16x16x32_f16 v[116:119], v[52:55], v[80:83], v[116:119]\n\tds_read_b128 v[108:111], %1 offset:3232\n\tv_mfma_f32_16x16x32_f16 v[116:119], v[60:63], v[92:95], v[116:119]\n\t" \
    "ds_read_b128 v[104:107], %1 offset:3296\n\tds_read_b128 v[112:115], %1 offset:3360\n\ts_waitcnt lgkmcnt(4)\n\tv_mfma_f32_16x16x32_f16 v[116:119], v[68:71], v[84:87], v[116:119]\n\t" \
    "s_nop 0\n\tv_mul_f32 v100, %8, v100\n\tv_mul_f32 v101, %8, v101\n\tv_mfma_f32_16x16x32_f16 v[120:123], v[72:75], v[84:87], 0\n\tv_mul_f32 v102, %8, v102\n\t" \
    "v_mul_f32 v103, %8, v103\n\tv_mfma_f32_16x16x32_f16 v[116:119], v[8:11], v[88:91], v[116:119]\n\tds_write_b128 %2, v[100:103] offset:1024\n\tds_write_b8 %6, %6 offset:8\n\t" \
    CONV_P1 \
    "v_mfma_f32_16x16x32_f16 v[120:123], v[12:15], v[88:91], v[120:123]\n\ts_waitcnt lgkmcnt(4)\n\tv_mfma_f32_16x16x32_f16 v[116:119], v[16:19], v[96:99], v[116:119]\n\t" \
    "ds_read_b128 v[124:127], %1 offset:3424\n\tv_mfma_f32_16x16x32_f16 v[120:123], v[20:23], v[96:99], v[120:123]\n\tv_mfma_f32_16x16x32_f16 v[116:119], v[24:27], v[108:111], v[116:119]\n\t" \
    "ds_read_b128 v[76:79], %1 offset:3488\n\tv_mfma_f32_16x16x32_f16 v[120:123], v[28:31], v[108:111], v[120:123]\n\ts_waitcnt lgkmcnt(4)\n\tv_mfma_f32_16x16x32_f16 v[116:119], v[32:35], v[104:107], v[116:119]\n\t" \
    "ds_read_b128 v[80:83], %1 offset:3552\n\tv_mfma_f32_16x16x32_f16 v[120:123], v[36:39], v[104:107], v[120:123]\n\tv_mfma_f32_16x16x32_f16 v[116:119], v[40:43], v[112:115], v[116:119]\n\t" \
    "ds_read_b128 v[92:95], %1 offset:3616\n\tv_mfma_f32_16x16x32_f16 v[120:123], v[44:47], v[112:115], v[120:123]\n\tds_read_b128 v[12:15], %1 offset:3680\n\t" \
    "s_waitcnt lgkmcnt(3)\n\tv_mfma_f32_16x16x32_f16 v[116:119], v[48:51], v[124:127], v[116:119]\n\tds_read_b128 v[72:75], %1 offset:3744\n\tv_mfma_f32_16x16x32_f16 v[120:123], v[52:55], v[124:127], v[120:123]\n\t" \
    "ds_read_b128 v[100:103], %1 offset:3808\n\tv_mfma_f32_16x16x32_f16 v[116:119], v[56:59], v[76:79], v[116:119]\n\tds_read_b128 v[84:87], %1 offset:464\n\t" \
    "ds_read_b128 v[88:91], %3 offset:1024\n\tv_mfma_f32_16x16x32_f16 v[120:123], v[60:63], v[76:79], v[120:123]\n\tds_read_b128 v[20:23], %1 offset:480\n\t" \
    "ds_read_b128 v[96:99], %3 offset:4096\n\ts_waitcnt lgkmcnt(6)\n\tv_mfma_f32_16x16x32_f16 v[116:119], v[64:67], v[80:83], v[116:119]\n\tds_read_b128 v[28:31], %1 offset:3872\n\t" \
    "v_mfma_f32_16x16x32_f16 v[120:123], v[68:71], v[80:83], v[120:123]\n\tds_read_b128 v[108:111], %1 offset:976\n\tv_mfma_f32_16x16x32_f16 v[120:123], v[8:11], v[92:95], v[120:123]\n\t" \
    "ds_read_b128 v[36:39], %1 offset:992\n\tv_mfma_f32_16x16x32_f16 v[120:123], v[16:19], v[12:15], v[120:123]\n\tds_read_b128 v[104:107], %1 offset:3936\n\t" \
    "s_waitcnt lgkmcnt(4)\n\tv_mfma_f32_16x16x32_f16 v[120:123], v[24:27], v[72:75], v[120:123]\n\tds_read_b128 v[44:47], %1 offset:1488\n\tv_mfma_f32_16x16x32_f16 v[120:123], v[32:35], v[100:103], v[120:123]\n\t" \
    "ds_read_b128 v[112:115], %1 offset:1504\n\tv_mfma_f32_16x16x32_f16 v[52:55], v[84:87], v[88:91], 0\n\tds_read_b128 v[124:127], %1 offset:4000\n\tv_mfma_f32_16x16x32_f16 v[60:63], v[20:23], v[96:99], 0\n\t" \
    "ds_read_b128 v[76:79], %3 offset:0\n\ts_waitcnt lgkmcnt(4)\n\tv_mfma_f32_16x16x32_f16 v[120:123], v[40:43], v[28:31], v[120:123]\n\tds_read_b128 v[68:71], %3 offset:2048\n\t" \
    "v_mfma_f32_16x16x32_f16 v[80:83], v[108:111], v[88:91], 0\n\tds_read_b128 v[8:11], %3 offset:3072\n\tds_read_b128 v[92:95], %1 offset:4064\n\tv_mfma_f32_16x16x32_f16 v[16:19], v[36:39], v[96:99], 0\n\t" \
    "v_cvt_pk_f16_f32 v12, v52, v60\n\tv_cvt_pk_f16_f32 v13, v53, v61\n\tv_mfma_f32_16x16x32_f16 v[120:123], v[48:51], v[104:107], v[120:123]\n\tv_cvt_pk_f16_f32 v14, v54, v62\n\t" \
    "v_cvt_pk_f16_f32 v15, v55, v63\n\ts_waitcnt lgkmcnt(3)\n\tv_mfma_f32_16x16x32_f16 v[24:27], v[44:47], v[88:91], 0\n\tds_read2_b32 v[72:73], %5 offset0:16 offset1:17\n\t" \
    "ds_read2_b32 v[74:75], %5 offset0:18 offset1:19\n\tv_mfma_f32_16x16x32_f16 v[32:35], v[112:115], v[96:99], 0\n\tv_cvt_pk_f16_f32 v100, v80, v16\n\tv_cvt_pk_f16_f32 v101, v81, v17\n\t" \
    "ds_read2_b32 v[84:85], %5 offset0:0 offset1:1\n\tds_read2_b32 v[86:87], %5 offset0:2 offset1:3\n\tv_mfma_f32_16x16x32_f16 v[120:123], v[56:59], v[124:127], v[120:123]\n\t" \
    "v_cvt_pk_f16_f32 v102, v82, v18\n\tv_cvt_pk_f16_f32 v103, v83, v19\n\tv_add_u32 %7, 1880, %0\n\tds_read2_b32 v[20:21], %7 offset0:0 offset1:1\n\tds_read2_b32 v[22:23], %7 offset0:2 offset1:3\n\t" \
    "v_mfma_f32_16x16x32_f16 v[40:43], v[44:47], v[76:79], 0\n\tds_read_b128 v[28:31], %1 offset:2000\n\tds_read_b128 v[108:111], %1 offset:2016\n\ts_waitcnt lgkmcnt(8)\n\t" \
    "v_mfma_f32_16x16x32_f16 v[36:39], v[112:115], v[68:71], 0\n\tv_cvt_pk_f16_f32 v48, v24, v32\n\tv_cvt_pk_f16_f32 v49, v25, v33\n\tv_mfma_f32_16x16x32_f16 v[104:107], v[112:115], v[76:79], 0\n\t" \
    "v_cvt_pk_f16_f32 v50, v26, v34\n\tv_cvt_pk_f16_f32 v51, v27, v35\n\tv_mfma_f32_16x16x32_f16 v[52:55], v[112:115], v[8:11], 0\n\tds_read2_b32 v[60:61], %5 offset0:32 offset1:33\n\t" \
    "ds_read2_b32 v[62:63], %5 offset0:34 offset1:35\n\tv_mfma_f32_16x16x32_f16 v[120:123], v[64:67], v[92:95], v[120:123]\n\tv_cvt_pk_f16_f32 v56, v40, v36\n\t" \
    "v_cvt_pk_f16_f32 v58, v41, v37\n\ts_waitcnt lgkmcnt(3)\n\tv_mfma_f32_16x16x32_f16 v[116:119], v[12:15], v[72:75], v[116:119]\n\tv_cvt_pk_f16_f32 v124, v42, v38\n\t" \
    "v_cvt_pk_f16_f32 v126, v43, v39\n\tv_mfma_f32_16x16x32_f16 v[120:123], v[12:15], v[84:87], v[120:123]\n\tv_cvt_pk_f16_f32 v57, v104, v52\n\tv_cvt_pk_f16_f32 v59, v105, v53\n\t" \
    "ds_read2_b32 v[80:81], %5 offset0:48 offset1:49\n\tds_read2_b32 v[82:83], %5 offset0:50 offset1:51\n\tv_mfma_f32_16x16x32_f16 v[116:119], v[20:23], v[112:115], v[116:119]\n\t" \
    "v_cvt_pk_f16_f32 v125, v106, v54\n\tv_cvt_pk_f16_f32 v127, v107, v55\n\tv_mfma_f32_16x16x32_f16 v[16:19], v[28:31], v[76:79], 0\n\tds_read_b128 v[44:47], %1 offset:2512\n\t" \
    "s_waitcnt lgkmcnt(3)\n\tv_mfma_f32_16x16x32_f16 v[24:27], v[108:111], v[68:71], 0\n\tds_read_b128 v[32:35], %1 offset:2528\n\tv_mfma_f32_16x16x32_f16 v[64:67], v[108:111], v[76:79], 0\n\t" \
    "v_mfma_f32_16x16x32_f16 v[92:95], v[108:111], v[8:11], 0\n\tds_read2_b64 v[84:87], %4 offset0:0 offset1:1\n\tds_read2_b64 v[40:43], %4 offset0:2 offset1:3\n\t" \
    "v_mfma_f32_16x16x32_f16 v[116:119], v[100:103], v[60:63], v[116:119]\n\ts_nop 1\n\tv_cvt_pk_f16_f32 v36, v16, v24\n\tv_cvt_pk_f16_f32 v38, v17, v25\n\t" \
    "v_mfma_f32_16x16x32_f16 v[120:123], v[100:103], v[72:75], v[120:123]\n\tv_cvt_pk_f16_f32 v112, v18, v26\n\tv_cvt_pk_f16_f32 v114, v19, v27\n\ts_waitcnt lgkmcnt(2)\n\t" \
    "v_mfma_f32_16x16x32_f16 v[116:119], v[48:51], v[80:83], v[116:119]\n\tv_cvt_pk_f16_f32 v37, v64, v92\n\tv_cvt_pk_f16_f32 v39, v65, v93\n\tv_mfma_f32_16x16x32_f16 v[120:123], v[48:51], v[60:63], v[120:123]\n\t" \
    "v_cvt_pk_f16_f32 v113, v66, v94\n\tv_cvt_pk_f16_f32 v115, v67, v95\n\tv_mfma_f32_16x16x32_f16 v[12:15], v[44:47], v[76:79], 0\n\tv_mfma_f32_16x16x32_f16 v[104:107], v[32:35], v[68:71], 0\n\t" \
    "ds_read2_b64 v[52:55], %4 offset0:16 offset1:17\n\tv_mfma_f32_16x16x32_f16 v[72:75], v[32:35], v[76:79], 0\n\tds_read2_b64 v[100:103], %4 offset0:18 offset1:19\n\t" \
    "v_mfma_f32_16x16x32_f16 v[60:63], v[32:35], v[8:11], 0\n\tds_read_b128 v[16:19], %1 offset:3024\n\tds_read_b128 v[24:27], %1 offset:3040\n\ts_waitcnt lgkmcnt(4)\n\t" \
    "v_mfma_f32_16x16x32_f16 v[116:119], v[56:59], v[84:87], v[116:119]\n\tv_cvt_pk_f16_f32 v64, v12, v104\n\tv_cvt_pk_f16_f32 v66, v13, v105\n\tv_mfma_f32_16x16x32_f16 v[116:119], v[124:127], v[40:43], v[116:119]\n\t" \
    "v_cvt_pk_f16_f32 v92, v14, v106\n\tv_cvt_pk_f16_f32 v94, v15, v107\n\tv_mfma_f32_16x16x32_f16 v[48:51], v[28:31], v[88:91], 0\n\tv_cvt_pk_f16_f32 v65, v72, v60\n\t" \
    "v_cvt_pk_f16_f32 v67, v73, v61\n\tv_mfma_f32_16x16x32_f16 v[44:47], v[108:111], v[96:99], 0\n\tv_cvt_pk_f16_f32 v93, v74, v62\n\tv_cvt_pk_f16_f32 v95, v75, v63\n\t" \
    "ds_read2_b64 v[32:35], %4 offset0:32 offset1:33\n\tv_mfma_f32_16x16x32_f16 v[120:123], v[20:23], v[108:111], v[120:123]\n\tds_read2_b64 v[56:59], %4 offset0:34 offset1:35\n\t" \
    "s_waitcnt lgkmcnt(2)\n\tv_mfma_f32_16x16x32_f16 v[116:119], v[36:39], v[52:55], v[116:119]\n\tv_mfma_f32_16x16x32_f16 v[116:119], v[112:115], v[100:103], v[116:119]\n\t" \
    "v_cvt_pk_f16_f32 v12, v48, v44\n\tv_cvt_pk_f16_f32 v13, v49, v45\n\tv_mfma_f32_16x16x32_f16 v[104:107], v[16:19], v[76:79], 0\n\tv_cvt_pk_f16_f32 v14, v50, v46\n\t" \
    "v_cvt_pk_f16_f32 v15, v51, v47\n\tv_mfma_f32_16x16x32_f16 v[88:91], v[24:27], v[68:71], 0\n\tv_mfma_f32_16x16x32_f16 v[28:31], v[24:27], v[76:79], 0\n\t" \
    "v_mfma_f32_16x16x32_f16 v[124:127], v[24:27], v[8:11], 0\n\ts_waitcnt lgkmcnt(0)\n\tv_mfma_f32_16x16x32_f16 v[116:119], v[64:67], v[32:35], v[116:119]\n\t" \
    "s_nop 3\n\tv_cvt_pk_f16_f32 v96, v104, v88\n\tv_cvt_pk_f16_f32 v98, v105, v89\n\tv_mfma_f32_16x16x32_f16 v[116:119], v[92:95], v[56:59], v[116:119]\n\t" \
    "v_cvt_pk_f16_f32 v20, v106, v90\n\tv_cvt_pk_f16_f32 v22, v107, v91\n\tv_mfma_f32_16x16x32_f16 v[120:123], v[36:39], v[84:87], v[120:123]\n\tv_cvt_pk_f16_f32 v97, v28, v124\n\t" \
    "v_cvt_pk_f16_f32 v99, v29, v125\n\tv_mfma_f32_16x16x32_f16 v[120:123], v[112:115], v[40:43], v[120:123]\n\tv_cvt_pk_f16_f32 v21, v30, v126\n\tv_cvt_pk_f16_f32 v23, v31, v127\n\t" \
    "v_mfma_f32_16x16x32_f16 v[120:123], v[12:15], v[80:83], v[120:123]\n\tv_mul_f32 v116, %8, v116\n\tv_mul_f32 v117, %8, v117\n\tv_mfma_f32_16x16x32_f16 v[120:123], v[64:67], v[52:55], v[120:123]\n\t" \
    "v_mul_f32 v118, %8, v118\n\tv_mul_f32 v119, %8, v119\n\tv_mfma_f32_16x16x32_f16 v[120:123], v[92:95], v[100:103], v[120:123]\n\tds_write_b128 %2, v[116:119] offset:2048\n\t" \
    "ds_write_b8 %6, %6 offset:16\n\t" \
    CONV_P2 \
    "v_mfma_f32_16x16x32_f16 v[120:123], v[96:99], v[32:35], v[120:123]\n\tv_mfma_f32_16x16x32_f16 v[120:123], v[20:23], v[56:59], v[120:123]\n\ts_nop 7\n\t" \
    "v_mul_f32 v120, %8, v120\n\tv_mul_f32 v121, %8, v121\n\tv_mul_f32 v122, %8, v122\n\tv_mul_f32 v123, %8, v123\n\tds_write_b128 %2, v[120:123] offset:3072\n\t" \
    "ds_write_b8 %6, %6 offset:24\n\t"
#define CONV_ASM_1 \
    "v_add_u32 %7, -64, %0\n\tds_read_b128 v[8:11], %3 offset:0\n\tds_read_b128 v[12:15], %3 offset:2048\n\tds_read_b128 v[16:19], %3 offset:3072\n\tds_read2_b64 v[24:27], %4 offset0:32 offset1:33\n\t" \
    "ds_read2_b64 v[36:39], %4 offset0:34 offset1:35\n\tds_read2_b64 v[28:31], %4 offset0:16 offset1:17\n\tds_read2_b64 v[40:43], %4 offset0:18 offset1:19\n\t" \
    "ds_read2_b64 v[32:35], %4 offset0:0 offset1:1\n\tds_read2_b64 v[44:47], %4 offset0:2 offset1:3\n\tds_read2_b32 v[20:21], %7 offset0:0 offset1:1\n\tds_read2_b32 v[22:23], %7 offset0:2 offset1:3\n\t" \
    "ds_read2_b32 v[48:49], %0 offset0:0 offset1:1\n\tds_read2_b32 v[50:51], %0 offset0:2 offset1:3\n\tds_read2_b32 v[52:53], %0 offset0:16 offset1:17\n\tds_read2_b32 v[54:55], %0 offset0:18 offset1:19\n\t" \
    "ds_read2_b32 v[56:57], %0 offset0:32 offset1:33\n\tds_read2_b32 v[58:59], %0 offset0:34 offset1:35\n\tds_read2_b32 v[60:61], %0 offset0:48 offset1:49\n\t" \
    "ds_read2_b32 v[62:63], %0 offset0:50 offset1:51\n\tds_read2_b32 v[64:65], %0 offset0:64 offset1:65\n\tds_read2_b32 v[66:67], %0 offset0:66 offset1:67\n\t" \
    "ds_read2_b32 v[68:69], %0 offset0:80 offset1:81\n\tds_read2_b32 v[70:71], %0 offset0:82 offset1:83\n\tds_read2_b32 v[72:73], %0 offset0:96 offset1:97\n\t" \
    "ds_read2_b32 v[74:75], %0 offset0:98 offset1:99\n\tds_read2_b32 v[76:77], %0 offset0:112 offset1:113\n\tds_read2_b32 v[78:79], %0 offset0:114 offset1:115\n\t" \
    "ds_read2_b32 v[80:81], %0 offset0:128 offset1:129\n\tds_read2_b32 v[82:83], %0 offset0:130 offset1:131\n\tds_read2_b32 v[84:85], %0 offset0:144 offset1:145\n\t" \
    "ds_read2_b32 v[86:87], %0 offset0:146 offset1:147\n\tds_read2_b32 v[88:89], %0 offset0:160 offset1:161\n\tds_read2_b32 v[90:91], %0 offset0:162 offset1:163\n\t" \
    CONV_WAIT \
    "ds_read_b128 v[92:95], %1 offset:464\n\tds_read_b128 v[96:99], %1 offset:480\n\tds_read_b128 v[100:103], %1 offset:976\n\tds_read_b128 v[104:107], %1 offset:992\n\t" \
    "s_waitcnt lgkmcnt(3)\n\tv_mfma_f32_16x16x32_f16 v[108:111], v[92:95], v[8:11], 0\n\ts_waitcnt lgkmcnt(2)\n\tv_mfma_f32_16x16x32_f16 v[112:115], v[96:99], v[12:15], 0\n\t" \
    "v_mfma_f32_16x16x32_f16 v[116:119], v[96:99], v[8:11], 0\n\tv_mfma_f32_16x16x32_f16 v[120:123], v[96:99], v[16:19], 0\n\ts_waitcnt lgkmcnt(0)\n\tv_mfma_f32_16x16x32_f16 v[124:127], v[100:103], v[8:11], 0\n\t" \
    "s_nop 3\n\tv_cvt_pk_f16_f32 v92, v108, v112\n\tv_cvt_pk_f16_f32 v94, v109, v113\n\tv_mfma_f32_16x16x32_f16 v[100:103], v[104:107], v[12:15], 0\n\tv_cvt_pk_f16_f32 v12, v110, v114\n\t" \
    "v_cvt_pk_f16_f32 v14, v111, v115\n\tv_mfma_f32_16x16x32_f16 v[108:111], v[104:107], v[8:11], 0\n\tv_cvt_pk_f16_f32 v93, v116, v120\n\tv_cvt_pk_f16_f32 v95, v117, v121\n\t" \
    "v_mfma_f32_16x16x32_f16 v[112:115], v[104:107], v[16:19], 0\n\tv_cvt_pk_f16_f32 v13, v118, v122\n\tv_cvt_pk_f16_f32 v15, v119, v123\n\tv_mfma_f32_16x16x32_f16 v[8:11], v[20:23], v[96:99], 0\n\t" \
    "v_cvt_pk_f16_f32 v16, v124, v100\n\tv_cvt_pk_f16_f32 v18, v125, v101\n\tv_mfma_f32_16x16x32_f16 v[116:119], v[20:23], v[104:107], 0\n\tv_cvt_pk_f16_f32 v120, v126, v102\n\t" \
    "v_cvt_pk_f16_f32 v122, v127, v103\n\tv_mfma_f32_16x16x32_f16 v[8:11], v[92:95], v[24:27], v[8:11]\n\tv_cvt_pk_f16_f32 v17, v108, v112\n\tv_cvt_pk_f16_f32 v19, v109, v113\n\t" \
    "v_mfma_f32_16x16x32_f16 v[116:119], v[92:95], v[28:31], v[116:119]\n\tv_cvt_pk_f16_f32 v121, v110, v114\n\tv_cvt_pk_f16_f32 v123, v111, v115\n\tv_mfma_f32_16x16x32_f16 v[96:99], v[92:95], v[32:35], 0\n\t" \
    "v_mfma_f32_16x16x32_f16 v[8:11], v[12:15], v[36:39], v[8:11]\n\tv_mfma_f32_16x16x32_f16 v[116:119], v[12:15], v[40:43], v[116:119]\n\tds_read_b128 v[104:107], %1 offset:544\n\t" \
    "v_mfma_f32_16x16x32_f16 v[96:99], v[12:15], v[44:47], v[96:99]\n\tds_read_b128 v[124:127], %1 offset:608\n\tv_mfma_f32_16x16x32_f16 v[116:119], v[16:19], v[24:27], v[116:119]\n\t" \
    "ds_read_b128 v[100:103], %1 offset:672\n\tv_mfma_f32_16x16x32_f16 v[96:99], v[16:19], v[28:31], v[96:99]\n\tds_read_b128 v[108:111], %1 offset:736\n\tv_mfma_f32_16x16x32_f16 v[112:115], v[16:19], v[32:35], 0\n\t" \
    "ds_read_b128 v[92:95], %1 offset:800\n\tv_mfma_f32_16x16x32_f16 v[116:119], v[120:123], v[36:39], v[116:119]\n\tds_read_b128 v[24:27], %1 offset:864\n\t" \
    "v_mfma_f32_16x16x32_f16 v[96:99], v[120:123], v[40:43], v[96:99]\n\tds_read_b128 v[12:15], %1 offset:928\n\tv_mfma_f32_16x16x32_f16 v[112:115], v[120:123], v[44:47], v[112:115]\n\t" \
    "ds_read_b128 v[28:31], %1 offset:992\n\ts_waitcnt lgkmcnt(4)\n\tv_mfma_f32_16x16x32_f16 v[8:11], v[48:51], v[104:107], v[8:11]\n\tds_read_b128 v[32:35], %1 offset:1056\n\t" \
    "v_mfma_f32_16x16x32_f16 v[8:11], v[52:55], v[124:127], v[8:11]\n\tv_mfma_f32_16x16x32_f16 v[8:11], v[56:59], v[100:103], v[8:11]\n\tds_read_b128 v[36:39], %1 offset:1120\n\t" \
    "v_mfma_f32_16x16x32_f16 v[8:11], v[60:63], v[108:111], v[8:11]\n\ts_waitcnt lgkmcnt(2)\n\tv_mfma_f32_16x16x32_f16 v[8:11], v[64:67], v[92:95], v[8:11]\n\t" \
    "ds_read_b128 v[16:19], %1 offset:1184\n\tv_mfma_f32_16x16x32_f16 v[8:11], v[68:71], v[24:27], v[8:11]\n\tds_read2_b32 v[40:41], %0 offset0:176 offset1:177\n\t" \
    "ds_read2_b32 v[42:43], %0 offset0:178 offset1:179\n\tv_mfma_f32_16x16x32_f16 v[8:11], v[72:75], v[12:15], v[8:11]\n\tds_read_b128 v[44:47], %1 offset:1248\n\t" \
    "v_mfma_f32_16x16x32_f16 v[8:11], v[76:79], v[28:31], v[8:11]\n\tds_read2_b32 v[120:121], %0 offset0:192 offset1:193\n\tds_read2_b32 v[122:123], %0 offset0:194 offset1:195\n\t" \
    "s_waitcnt lgkmcnt(6)\n\tv_mfma_f32_16x16x32_f16 v[8:11], v[80:83], v[32:35], v[8:11]\n\tds_read_b128 v[104:107], %1 offset:1312\n\tv_mfma_f32_16x16x32_f16 v[116:119], v[48:51], v[32:35], v[116:119]\n\t" \
    "ds_read2_b32 v[124:125], %0 offset0:208 offset1:209\n\tds_read2_b32 v[126:127], %0 offset0:210 offset1:211\n\tv_mfma_f32_16x16x32_f16 v[8:11], v[84:87], v[36:39], v[8:11]\n\t" \
    "ds_read_b128 v[100:103], %1 offset:1376\n\tv_mfma_f32_16x16x32_f16 v[116:119], v[52:55], v[36:39], v[116:119]\n\tds_read2_b32 v[108:109], %0 offset0:224 offset1:225\n\t" \
    "ds_read2_b32 v[110:111], %0 offset0:226 offset1:227\n\ts_waitcnt lgkmcnt(8)\n\tv_mfma_f32_16x16x32_f16 v[8:11], v[88:91], v[16:19], v[8:11]\n\tds_read_b128 v[92:95], %1 offset:1440\n\t" \
    "v_mfma_f32_16x16x32_f16 v[116:119], v[56:59], v[16:19], v[116:119]\n\tds_read2_b32 v[24:25], %0 offset0:240 offset1:241\n\tds_read2_b32 v[26:27], %0 offset0:242 offset1:243\n\t" \
    "v_mfma_f32_16x16x32_f16 v[8:11], v[40:43], v[44:47], v[8:11]\n\tds_read_b128 v[12:15], %1 offset:1504\n\tv_mfma_f32_16x16x32_f16 v[116:119], v[60:63], v[44:47], v[116:119]\n\t" \
    "s_waitcnt lgkmcnt(6)\n\tv_mfma_f32_16x16x32_f16 v[8:11], v[120:123], v[104:107], v[8:11]\n\tv_mfma_f32_16x16x32_f16 v[116:119], v[64:67], v[104:107], v[116:119]\n\t" \
    "ds_read_b128 v[28:31], %1 offset:1568\n\tv_mfma_f32_16x16x32_f16 v[8:11], v[124:127], v[100:103], v[8:11]\n\tv_mfma_f32_16x16x32_f16 v[116:119], v[68:71], v[100:103], v[116:119]\n\t" \
    "ds_read_b128 v[32:35], %1 offset:1632\n\ts_waitcnt lgkmcnt(2)\n\tv_mfma_f32_16x16x32_f16 v[8:11], v[108:111], v[92:95], v[8:11]\n\tv_mfma_f32_16x16x32_f16 v[116:119], v[72:75], v[92:95], v[116:119]\n\t" \
    "ds_read_b128 v[36:39], %1 offset:1696\n\tv_mfma_f32_16x16x32_f16 v[8:11], v[24:27], v[12:15], v[8:11]\n\tv_mfma_f32_16x16x32_f16 v[116:119], v[76:79], v[12:15], v[116:119]\n\t" \
    "ds_read_b128 v[16:19], %1 offset:1760\n\tv_mfma_f32_16x16x32_f16 v[96:99], v[20:23], v[12:15], v[96:99]\n\tds_read_b128 v[44:47], %1 offset:1824\n\tds_read_b128 v[104:107], %1 offset:1888\n\t" \
    "s_waitcnt lgkmcnt(4)\n\tv_mfma_f32_16x16x32_f16 v[116:119], v[80:83], v[28:31], v[116:119]\n\ts_nop 0\n\tv_mul_f32 v8, %8, v8\n\tv_mul_f32 v9, %8, v9\n\t" \
    "v_mfma_f32_16x16x32_f16 v[96:99], v[48:51], v[28:31], v[96:99]\n\tv_mul_f32 v10, %8, v10\n\tv_mul_f32 v11, %8, v11\n\tv_mfma_f32_16x16x32_f16 v[116:119], v[84:87], v[32:35], v[116:119]\n\t" \
    "ds_write_b128 %2, v[8:11] offset:0\n\tds_write_b8 %6, %6 offset:0\n\t" \
    CONV_P0 \
    "v_mfma_f32_16x16x32_f16 v[96:99], v[52:55], v[32:35], v[96:99]\n\ts_waitcnt lgkmcnt(4)\n\tv_mfma_f32_16x16x32_f16 v[116:119], v[88:91], v[36:39], v[116:119]\n\t" \
    "ds_read_b128 v[100:103], %1 offset:1952\n\tv_mfma_f32_16x16x32_f16 v[96:99], v[56:59], v[36:39], v[96:99]\n\tv_mfma_f32_16x16x32_f16 v[116:119], v[40:43], v[16:19], v[116:119]\n\t" \
    "ds_read_b128 v[92:95], %1 offset:2016\n\tv_mfma_f32_16x16x32_f16 v[96:99], v[60:63], v[16:19], v[96:99]\n\ts_waitcnt lgkmcnt(4)\n\tv_mfma_f32_16x16x32_f16 v[116:119], v[120:123], v[44:47], v[116:119]\n\t" \
    "v_mfma_f32_16x16x32_f16 v[96:99], v[64:67], v[44:47], v[96:99]\n\tds_read_b128 v[12:15], %1 offset:2080\n\tv_mfma_f32_16x16x32_f16 v[116:119], v[124:127], v[104:107], v[116:119]\n\t" \
    "v_mfma_f32_16x16x32_f16 v[96:99], v[68:71], v[104:107], v[96:99]\n\tds_read_b128 v[8:11], %1 offset:2144\n\ts_waitcnt lgkmcnt(2)\n\tv_mfma_f32_16x16x32_f16 v[116:119], v[108:111], v[100:103], v[116:119]\n\t" \
    "v_mfma_f32_16x16x32_f16 v[96:99], v[72:75], v[100:103], v[96:99]\n\tds_read_b128 v[28:31], %1 offset:2208\n\tv_mfma_f32_16x16x32_f16 v[116:119], v[24:27], v[92:95], v[116:119]\n\t" \
    "v_mfma_f32_16x16x32_f16 v[96:99], v[76:79], v[92:95], v[96:99]\n\tds_read_b128 v[32:35], %1 offset:2272\n\tv_mfma_f32_16x16x32_f16 v[112:115], v[20:23], v[92:95], v[112:115]\n\t" \
    "ds_read_b128 v[36:39], %1 offset:2336\n\tds_read_b128 v[16:19], %1 offset:2400\n\ts_waitcnt lgkmcnt(4)\n\tv_mfma_f32_16x16x32_f16 v[96:99], v[80:83], v[12:15], v[96:99]\n\t" \
    "s_nop 0\n\tv_mul_f32 v116, %8, v116\n\tv_mul_f32 v117, %8, v117\n\tv_mfma_f32_16x16x32_f16 v[112:115], v[48:51], v[12:15], v[112:115]\n\tv_mul_f32 v118, %8, v118\n\t" \
    "v_mul_f32 v119, %8, v119\n\tv_mfma_f32_16x16x32_f16 v[96:99], v[84:87], v[8:11], v[96:99]\n\tds_write_b128 %2, v[116:119] offset:1024\n\tds_write_b8 %6, %6 offset:8\n\t" \
    CONV_P1 \
    "v_mfma_f32_16x16x32_f16 v[112:115], v[52:55], v[8:11], v[112:115]\n\ts_waitcnt lgkmcnt(4)\n\tv_mfma_f32_16x16x32_f16 v[96:99], v[88:91], v[28:31], v[96:99]\n\t" \
    "ds_read_b128 v[44:47], %1 offset:2464\n\tv_mfma_f32_16x16x32_f16 v[112:115], v[56:59], v[28:31], v[112:115]\n\tv_mfma_f32_16x16x32_f16 v[96:99], v[40:43], v[32:35], v[96:99]\n\t" \
    "ds_read_b128 v[104:107], %1 offset:2528\n\tv_mfma_f32_16x16x32_f16 v[112:115], v[60:63], v[32:35], v[112:115]\n\ts_waitcnt lgkmcnt(4)\n\tv_mfma_f32_16x16x32_f16 v[96:99], v[120:123], v[36:39], v[96:99]\n\t" \
    "ds_read_b128 v[100:103], %1 offset:2592\n\tv_mfma_f32_16x16x32_f16 v[112:115], v[64:67], v[36:39], v[112:115]\n\tds_read_b128 v[20:23], %1 offset:2656\n\t" \
    "v_mfma_f32_16x16x32_f16 v[96:99], v[124:127], v[16:19], v[96:99]\n\tds_read_b128 v[92:95], %1 offset:2720\n\tv_mfma_f32_16x16x32_f16 v[112:115], v[68:71], v[16:19], v[112:115]\n\t" \
    "ds_read_b128 v[48:51], %1 offset:464\n\tds_read_b128 v[52:55], %3 offset:1024\n\ts_waitcnt lgkmcnt(5)\n\tv_mfma_f32_16x16x32_f16 v[96:99], v[108:111], v[44:47], v[96:99]\n\t" \
    "ds_read_b128 v[116:119], %1 offset:480\n\tds_read_b128 v[12:15], %3 offset:4096\n\tv_mfma_f32_16x16x32_f16 v[112:115], v[72:75], v[44:47], v[112:115]\n\t" \
    "ds_read_b128 v[8:11], %1 offset:2784\n\tv_mfma_f32_16x16x32_f16 v[96:99], v[24:27], v[104:107], v[96:99]\n\tds_read_b128 v[56:59], %1 offset:1488\n\tds_read_b128 v[28:31], %3 offset:0\n\t" \
    "v_mfma_f32_16x16x32_f16 v[112:115], v[76:79], v[104:107], v[112:115]\n\tds_read_b128 v[60:63], %1 offset:1504\n\tds_read_b128 v[32:35], %3 offset:2048\n\t" \
    "s_waitcnt lgkmcnt(7)\n\tv_mfma_f32_16x16x32_f16 v[112:115], v[80:83], v[100:103], v[112:115]\n\tv_mfma_f32_16x16x32_f16 v[112:115], v[84:87], v[20:23], v[112:115]\n\t" \
    "ds_read_b128 v[64:67], %3 offset:3072\n\tv_mfma_f32_16x16x32_f16 v[112:115], v[88:91], v[92:95], v[112:115]\n\tds_read_b128 v[36:39], %1 offset:2848\n\t" \
    "v_mfma_f32_16x16x32_f16 v[68:71], v[48:51], v[52:55], 0\n\tds_read_b128 v[16:19], %1 offset:976\n\ts_waitcnt lgkmcnt(3)\n\tv_mfma_f32_16x16x32_f16 v[72:75], v[116:119], v[12:15], 0\n\t" \
    "ds_read_b128 v[44:47], %1 offset:992\n\tv_mfma_f32_16x16x32_f16 v[112:115], v[40:43], v[8:11], v[112:115]\n\tv_add_u32 %7, 1880, %0\n\tds_read2_b32 v[76:77], %7 offset0:0 offset1:1\n\t" \
    "ds_read2_b32 v[78:79], %7 offset0:2 offset1:3\n\tv_mfma_f32_16x16x32_f16 v[104:107], v[56:59], v[28:31], 0\n\tds_read_b128 v[80:83], %1 offset:2000\n\t" \
    "ds_read_b128 v[84:87], %1 offset:2016\n\tv_mfma_f32_16x16x32_f16 v[100:103], v[60:63], v[32:35], 0\n\tv_cvt_pk_f16_f32 v20, v68, v72\n\tv_cvt_pk_f16_f32 v21, v69, v73\n\t" \
    "v_mfma_f32_16x16x32_f16 v[88:91], v[60:63], v[28:31], 0\n\tv_cvt_pk_f16_f32 v22, v70, v74\n\tv_cvt_pk_f16_f32 v23, v71, v75\n\tds_read2_b32 v[92:93], %5 offset0:48 offset1:49\n\t" \
    "ds_read2_b32 v[94:95], %5 offset0:50 offset1:51\n\ts_waitcnt lgkmcnt(6)\n\tv_mfma_f32_16x16x32_f16 v[48:51], v[60:63], v[64:67], 0\n\tds_read2_b64 v[40:43], %4 offset0:32 offset1:33\n\t" \
    "ds_read2_b64 v[8:11], %4 offset0:34 offset1:35\n\tv_mfma_f32_16x16x32_f16 v[112:115], v[120:123], v[36:39], v[112:115]\n\tv_cvt_pk_f16_f32 v56, v104, v100\n\t" \
    "v_cvt_pk_f16_f32 v58, v105, v101\n\tv_mfma_f32_16x16x32_f16 v[68:71], v[16:19], v[52:55], 0\n\tv_cvt_pk_f16_f32 v72, v106, v102\n\tv_cvt_pk_f16_f32 v74, v107, v103\n\t" \
    "v_mfma_f32_16x16x32_f16 v[60:63], v[44:47], v[12:15], 0\n\tv_cvt_pk_f16_f32 v57, v88, v48\n\tv_cvt_pk_f16_f32 v59, v89, v49\n\ts_waitcnt lgkmcnt(2)\n\t" \
    "v_mfma_f32_16x16x32_f16 v[96:99], v[76:79], v[116:119], v[96:99]\n\tv_cvt_pk_f16_f32 v73, v90, v50\n\tv_cvt_pk_f16_f32 v75, v91, v51\n\tds_read_b128 v[120:123], %1 offset:2912\n\t" \
    "v_mfma_f32_16x16x32_f16 v[36:39], v[80:83], v[28:31], 0\n\tds_read2_b32 v[52:53], %5 offset0:32 offset1:33\n\tds_read2_b32 v[54:55], %5 offset0:34 offset1:35\n\t" \
    "v_mfma_f32_16x16x32_f16 v[16:19], v[84:87], v[32:35], 0\n\tv_cvt_pk_f16_f32 v104, v68, v60\n\tv_cvt_pk_f16_f32 v105, v69, v61\n\tv_mfma_f32_16x16x32_f16 v[96:99], v[20:23], v[92:95], v[96:99]\n\t" \
    "v_cvt_pk_f16_f32 v106, v70, v62\n\tv_cvt_pk_f16_f32 v107, v71, v63\n\tds_read_b128 v[100:103], %1 offset:2976\n\tv_mfma_f32_16x16x32_f16 v[116:119], v[84:87], v[28:31], 0\n\t" \
    "ds_read2_b64 v[12:15], %4 offset0:16 offset1:17\n\tds_read2_b64 v[88:91], %4 offset0:18 offset1:19\n\tv_mfma_f32_16x16x32_f16 v[48:51], v[84:87], v[64:67], 0\n\t" \
    "v_cvt_pk_f16_f32 v80, v36, v16\n\tv_cvt_pk_f16_f32 v82, v37, v17\n\ts_waitcnt lgkmcnt(5)\n\tv_mfma_f32_16x16x32_f16 v[96:99], v[56:59], v[40:43], v[96:99]\n\t" \
    "v_cvt_pk_f16_f32 v32, v38, v18\n\tv_cvt_pk_f16_f32 v34, v39, v19\n\tv_mfma_f32_16x16x32_f16 v[96:99], v[72:75], v[8:11], v[96:99]\n\tds_read_b128 v[68:71], %1 offset:3040\n\t" \
    "v_mfma_f32_16x16x32_f16 v[112:115], v[124:127], v[120:123], v[112:115]\n\tv_cvt_pk_f16_f32 v81, v116, v48\n\tv_cvt_pk_f16_f32 v83, v117, v49\n\tv_mfma_f32_16x16x32_f16 v[112:115], v[76:79], v[44:47], v[112:115]\n\t" \
    "v_cvt_pk_f16_f32 v33, v118, v50\n\tv_cvt_pk_f16_f32 v35, v119, v51\n\ts_waitcnt lgkmcnt(1)\n\tv_mfma_f32_16x16x32_f16 v[112:115], v[20:23], v[52:55], v[112:115]\n\t" \
    "v_mul_f32 v96, %8, v96\n\tv_mul_f32 v97, %8, v97\n\tv_mfma_f32_16x16x32_f16 v[112:115], v[108:111], v[100:103], v[112:115]\n\tv_mul_f32 v98, %8, v98\n\t" \
    "v_mul_f32 v99, %8, v99\n\tv_mfma_f32_16x16x32_f16 v[112:115], v[56:59], v[12:15], v[112:115]\n\tds_write_b128 %2, v[96:99] offset:2048\n\tds_write_b8 %6, %6 offset:16\n\t" \
    CONV_P2 \
    "v_mfma_f32_16x16x32_f16 v[112:115], v[72:75], v[88:91], v[112:115]\n\tv_mfma_f32_16x16x32_f16 v[112:115], v[104:107], v[92:95], v[112:115]\n\ts_waitcnt lgkmcnt(2)\n\t" \
    "v_mfma_f32_16x16x32_f16 v[112:115], v[24:27], v[68:71], v[112:115]\n\tv_mfma_f32_16x16x32_f16 v[112:115], v[80:83], v[40:43], v[112:115]\n\tv_mfma_f32_16x16x32_f16 v[112:115], v[32:35], v[8:11], v[112:115]\n\t" \
    "s_nop 7\n\tv_mul_f32 v112, %8, v112\n\tv_mul_f32 v113, %8, v113\n\tv_mul_f32 v114, %8, v114\n\tv_mul_f32 v115, %8, v115\n\tds_write_b128 %2, v[112:115] offset:3072\n\t" \
    "ds_write_b8 %6, %6 offset:24\n\t"
#define CONV_SPIN(lab, next, off, slp) lab ":\n\tv_and_b32 %7, -8, %6\n\tds_read_b32 %7, %7 offset:" off "\n\ts_waitcnt lgkmcnt(0)\n\tv_readfirstlane_b32 %9, %7\n\t" \
    "s_cmp_ge_u32 %9, 4\n\ts_cbranch_scc1 " next "f\n\ts_sleep " slp "\n\ts_branch " lab "b\n"
#define CONV_WAIT "s_setprio 1\n" CONV_SPIN("3", "1", "36", "6") CONV_SPIN("1", "2", "32", "1") "2:\n\ts_setprio 2\n\t"
#define CONV_P0 "s_nop 0\n\t"
#define CONV_P1 "s_setprio 1\n\t"
#define CONV_P2 "s_nop 0\n\t"
#define CONV_CLOBBERS \
    "v8", "v9", "v10", "v11", "v12", "v13", "v14", "v15", "v16", "v17", "v18", "v19", "v20", "v21", "v22", "v23", "v24", "v25", "v26", "v27", "v28", "v29", "v30", "v31", \
    "v32", "v33", "v34", "v35", "v36", "v37", "v38", "v39", "v40", "v41", "v42", "v43", "v44", "v45", "v46", "v47", "v48", "v49", "v50", "v51", "v52", "v53", "v54", "v55", \
    "v56", "v57", "v58", "v59", "v60", "v61", "v62", "v63", "v64", "v65", "v66", "v67", "v68", "v69", "v70", "v71", "v72", "v73", "v74", "v75", "v76", "v77", "v78", "v79", \
    "v80", "v81", "v82", "v83", "v84", "v85", "v86", "v87", "v88", "v89", "v90", "v91", "v92", "v93", "v94", "v95", "v96", "v97", "v98", "v99", "v100", "v101", "v102", \
    "v103", "v104", "v105", "v106", "v107", "v108", "v109", "v110", "v111", "v112", "v113", "v114", "v115", "v116", "v117", "v118", "v119", "v120", "v121", "v122", "v123", \
    "v124", "v125", "v126", "v127"
DEV void conv(int hf, u32 sm0, int cw, float yscale) {
    int lane;
    asm volatile("v_mbcnt_lo_u32_b32 %0, -1, 0\n\tv_mbcnt_hi_u32_b32 %0, -1, %0" : "=v"(lane));
    __builtin_assume((unsigned)lane < 64u);
    const int al = lane & 15, q = lane >> 4;
    const u32 chb = sm0 + L_CH + cw * CH_SZ, kpar = chb + ((al & 1) ? 0 : REC_B + 2);
    u32 ka = kpar + (R_KA + ZA - al - 16 + 8 * q) * 2 - 960, ub0 = chb + C_U + (16 * al + 8 * q) * 2;
    u32 yb = sm0 + L_STG + cw * STG_BUF + 64 * al + 16 * (q ^ ((al >> 1) & 3));
    u32 bxp = sm0 + L_BOX + lane * 16, k2b = chb + (R_K2 + 192 - 4 * al - 4 + 16 * q - 128) * 2, k3b = chb + (R_K3 + 128 - 2 * al - 2 + 8 * q) * 2 - 192;
    u32 fl = sm0 + L_CNT + 8 + cw, t0, t1;
    u32 ys = __builtin_amdgcn_readfirstlane(__float_as_uint(yscale));
    static_assert(UPAD == 256 && (R_KP + 15 - (R_KA + ZA - 16)) * 2 + 960 == 1880 && L_CNT % 16 == 0, "offsets in the streams");
    asm volatile("s_cmp_eq_u32 %10, 0\n\ts_cbranch_scc1 8f\n\t" CONV_ASM_1 "s_branch 9f\n8:\n\t" CONV_ASM_0 "\n9:"
                 : "+v"(ka), "+v"(ub0), "+v"(yb), "+v"(bxp), "+v"(k2b), "+v"(k3b), "+v"(fl), "=&v"(t0), "+s"(ys), "=&s"(t1) : "s"(hf) : "memory", "scc", CONV_CLOBBERS);
}

constexpr int G_KEY = 8 * SL * 8, G_SZ = G_KEY + 256 * NREP * 16;
constexpr size_t WS_EPOCH = 0, WS_GRAN = 4096, WS_HAND = WS_GRAN + G_SZ, WS_U = WS_HAND + (size_t)(GU_WORDS + DN_WORDS) * 4,
                 WS_END = WS_U + (size_t)SL * SD * 2;
typedef __attribute__((ext_vector_type(8))) u32 u8v;
typedef __amdgpu_buffer_rsrc_t rsrc_t;
DEV rsrc_t mk_rsrc(u32 lo, u32 hi, size_t off, u32 bytes) {
    return __builtin_amdgcn_make_buffer_rsrc((__attribute__((address_space(1))) void*)((((unsigned long long)hi << 32) | lo) + off), 0, bytes, 0x00020000);
}
#define BLD128(rs, vo, so, aux) __builtin_amdgcn_raw_buffer_load_b128(rs, vo, so, aux)
DEV u32 lds_addr(const void* p) { return (u32)(uintptr_t)(__attribute__((address_space(3))) const void*)p; }

extern "C" __global__ __launch_bounds__(1024, 4) void k_fused(
    const u16* x_, const u16* wr_, char* ws_, u16* out_,
    const u16* logA_, const u16* Aim_, const u16* Cre_, const u16* Cim_,
    const u16* logdt_, const u16* dskip_, const u16* wg_, const u16* wu_, const u16* wd_) {
    __shared__ __attribute__((aligned(16))) char smem[L_TOTAL];
    const int tid = threadIdx.x, lane = tid & 63;
    const int wave = __builtin_amdgcn_readfirstlane(tid >> 6);
    const int xcd = blockIdx.x & 7, slot = blockIdx.x >> 3;
    const int g = xcd * 16 + (slot >> 1), hf = slot & 1;
    STAMP_DECL;
    STAMP(0);
    u32* cnt = reinterpret_cast<u32*>(smem + L_CNT);
    const u32 sm0 = lds_addr(smem);
    if (wave == 0) {
        if (lane < 32) cnt[lane] = 0;
        __builtin_amdgcn_s_waitcnt(0xc07f);
    }
    MEM_FENCE(); SCHED_FENCE();
    u32 kw0, kw1;
    asm volatile("s_load_dword %0, %2, 0x0\n\ts_load_dword %1, %2, 0x40" : "=&s"(kw0), "=&s"(kw1) : "s"(__builtin_amdgcn_kernarg_segment_ptr()) : "memory");
#define ARGS_LOAD() u8v ka0, ka1, ka2; u2 ka3; \
    asm volatile("s_load_dwordx8 %0, %4, 0x0\n\ts_load_dwordx8 %1, %4, 0x20\n\ts_load_dwordx8 %2, %4, 0x40\n\ts_load_dwordx2 %3, %4, 0x60" \
                 : "=&s"(ka0), "=&s"(ka1), "=&s"(ka2), "=&s"(ka3) : "s"(__builtin_amdgcn_kernarg_segment_ptr()) : "memory"); SCHED_FENCE()
#define ARGS_WAIT() do { SCHED_FENCE(); asm volatile("s_waitcnt lgkmcnt(0)" : "+s"(ka0), "+s"(ka1), "+s"(ka2), "+s"(ka3) : "s"(kw0), "s"(kw1) : "memory"); SCHED_FENCE(); } while (0)
#define LANE_IDS() int lane; asm volatile("v_mbcnt_lo_u32_b32 %0, -1, 0\n\tv_mbcnt_hi_u32_b32 %0, -1, %0" : "=v"(lane)); __builtin_assume((unsigned)lane < 64u); \
    const int tid = wave * 64 + lane, al = lane & 15, q = lane >> 4
#define XRS mk_rsrc(ka0[0], ka0[1], 0, SL * SD * 2)
#define GRS mk_rsrc(ka0[4], ka0[5], WS_GRAN, G_SZ)
#define URS mk_rsrc(ka0[4], ka0[5], WS_U, SL * SD * 2)
#define HRS mk_rsrc(ka0[4], ka0[5], WS_HAND, (GU_WORDS + DN_WORDS) * 4)
#define ORS mk_rsrc(ka0[6], ka0[7], 0, SL * SD * 2)
    u32 code_pf = 0;
    if (wave == 8 && slot * 2048 < CODE_B) {
        unsigned long long pc;
        asm volatile("s_getpc_b64 %0" : "=s"(pc));
        code_pf = *(const __attribute__((address_space(1))) u32*)((pc & ~255ull) + (unsigned)min(slot * 2048 + lane * 32, CODE_B));
    }
    MEM_FENCE(); SCHED_FENCE();
    __builtin_amdgcn_s_barrier();
    MEM_FENCE(); SCHED_FENCE();
#define M0_ADD(n) "s_add_u32 m0, m0, " #n "\n\ts_nop 0\n\t"
    if (wave == 6) {
        unsigned long long pe;
        u32 ep;
        asm volatile("s_load_dwordx2 %0, %2, 0x10\n\ts_waitcnt lgkmcnt(0)\n\ts_load_dword %1, %0, %3\n\ts_waitcnt lgkmcnt(0)"
                     : "=&s"(pe), "=s"(ep) : "s"(__builtin_amdgcn_kernarg_segment_ptr()), "n"(WS_EPOCH) : "memory");
        const u32 tg = ep + 1u;
        STAMP(1);
        if (lane < 5) cnt[24 + lane] = lane == 4 ? tg : ((tg >> (6 * lane)) & 7u) | (((tg >> (6 * lane + 3)) & 7u) << 16);
        MEM_FENCE();
        lds_add1(cnt + 0);
    }
    MEM_FENCE(); SCHED_FENCE();
#define URGENT_DONE() do { MEM_FENCE(); SCHED_FENCE(); lds_add1(cnt + 17); MEM_FENCE(); SCHED_FENCE(); } while (0)
    const int b = xcd * 32 + slot;
    const unsigned long long all8 = 0x0706050403020100ull + ((sm0 + L_CNT + 8) & 0xffu) * 0x0101010101010101ull;
    typedef __attribute__((address_space(3))) u32* lptr32;
    typedef __attribute__((address_space(3))) u4* lptr128;
    auto stats = [&](const u4 (&v)[4], float& mb, float& den, float& rd) __attribute__((always_inline)) {
        f2 p[8];
#pragma unroll
        for (int i = 0; i < 8; ++i) { p[i].x = __uint_as_float(v[i >> 1][2 * (i & 1)]); p[i].y = __uint_as_float(v[i >> 1][2 * (i & 1) + 1]); }
        const f2 t = (((p[0] + p[1]) + (p[2] + p[3])) + ((p[4] + p[5]) + (p[6] + p[7]))) * (1.0f / SD);
        const float var = fmaxf(__builtin_fmaf(-t.x, t.x, t.y), 0.0f);
        mb = rbf(t.x);
        den = rbf(__builtin_amdgcn_sqrtf(rbf(rbf(var) + 1e-5f)));
        rd = __builtin_amdgcn_rcpf(den);
    };
    auto e_role = [&]() __attribute__((always_inline)) {
        ARGS_LOAD();
        __builtin_amdgcn_s_setprio(3);
        LANE_IDS();
        u32 vw = wave * 512 + (al >> 3) * 64 + q * 16;
        u32 vrow = (b * 8 + (lane >> 3)) * (SD * 2) + wave * 512 + (((lane & 7) ^ ((lane >> 3) & 2)) << 4);
        const u32 l_row = sm0 + L_CH + wave * CH_SZ + C_U + UPAD * 2;
        u32 a_x = l_row + (al & 7) * 128 + (((4 * (al >> 3) + q) ^ (al & 2)) << 4);
        PIN(vrow);
        u4 xa[4], wa[4];
        ARGS_WAIT();
        asm volatile("s_mov_b32 m0, %0\n\ts_nop 0\n\tglobal_load_lds_dwordx4 %1, %2\n\t" M0_ADD(896) "global_load_lds_dwordx4 %1, %2 offset:128\n\t"
                     M0_ADD(896) "global_load_lds_dwordx4 %1, %2 offset:256\n\t" M0_ADD(896) "global_load_lds_dwordx4 %1, %2 offset:384"
                     :: "s"(l_row), "v"(vrow), "s"(((unsigned long long)ka0[1] << 32) | ka0[0]) : "memory", "scc");
        MEM_FENCE(); SCHED_FENCE();
#pragma unroll
        for (int j = 0; j < 4; ++j) { wa[j] = u4{0x3f803f80u, 0x3f803f80u, 0x3f803f80u, 0x3f803f80u}; PIN(wa[j]); }
        PIN(vw); PIN(a_x);
        if (!(al & 4)) {
            const rsrc_t rrs = mk_rsrc(ka0[2], ka0[3], 0, SD * 2);
#pragma unroll
            for (int j = 0; j < 4; ++j) wa[j] = BLD128(rrs, vw + 128 * j, 0, 0);
        }
        URGENT_DONE();
        u4 sel[2];
#pragma unroll
        for (int h = 0; h < 2; ++h) {
#pragma unroll
            for (int i = 0; i < 4; ++i) sel[h][i] = (q == 2 * h + (al >> 3) && i == ((al & 7) >> 1)) ? (0x3f80u << (16 * (al & 1))) : 0u;
            PIN(sel[h]);
        }
        const int part = 2 * wave + (al >> 3);
        u32 a_p2 = sm0 + L_P12 + (al & 7) * P12_S + part * 8;
        u32 a_p1 = (q == 1) ? a_p2 : (q == (al >> 3) * 2) ? sm0 + L_P3 + (al & 7) * P3_S + part * 4 : sm0 + L_NUL + lane * 4;
        a_p2 = (q == (al >> 2)) ? a_p2 + 4 : sm0 + L_NUL + lane * 4;
        u32 a_pr = sm0 + L_P12 + (4 * (q & 1) + (al & 3)) * P12_S, a_cnt = sm0 + L_CNT;
        u32 vu[4], m7 = 0x00070007u;
#pragma unroll
        for (int j = 0; j < 4; ++j) { vu[j] = ((32 * wave + 8 * j + (lane >> 3)) * 256 + b) * 128 + (lane & 7) * 16; PIN(vu[j]); }
        u32 vp = g * 32768 + tid * 16, vs = (xcd * SL + tid) * 8;
        u32 a_u = sm0 + L_CH + (lane & 7) * CH_SZ + C_U + (UPAD + 64 * wave + 8 * (lane >> 3)) * 2;
        PIN(a_p1); PIN(a_p2); PIN(a_pr); PIN(a_cnt); PIN(m7); PIN(vp); PIN(vs); PIN(a_u);
        const rsrc_t urs = URS, grs = GRS;
        bool c1 = al & 1, c2_ = al & 2;
        lds_wait(cnt + 0, 1);
        const u32 tag = __builtin_amdgcn_readfirstlane(cnt[28]);
        u4 pat = *reinterpret_cast<const u4*>(cnt + 24);
        PIN(pat);
        MEM_FENCE(); SCHED_FENCE();
        {
            const f4 z4 = {0, 0, 0, 0};
            f4 d2 = z4, d1 = z4, xt[4][2];
            asm volatile("s_waitcnt vmcnt(0)\n\tds_read_b128 %0, %4\n\tds_read_b128 %1, %4 offset:1024\n\tds_read_b128 %2, %4 offset:2048\n\t"
                         "ds_read_b128 %3, %4 offset:3072\n\ts_waitcnt lgkmcnt(0)"
                         : "=&v"(xa[0]), "=&v"(xa[1]), "=&v"(xa[2]), "=&v"(xa[3]) : "v"(a_x) : "memory");
#pragma unroll
            for (int j = 0; j < 4; ++j) { d2 = mfma16b(xa[j], xa[j], d2); d1 = mfma16b(wa[j], xa[j], d1); }
            STAMP(1);
#pragma unroll
            for (int j = 0; j < 2; ++j) xt[j >> 1][j & 1] = mfma16b(xa[j >> 1], sel[j & 1], z4);
            SCHED_FENCE();
            const float e0 = c1 ? d2[1] : d2[0], e1 = c1 ? d2[3] : d2[2];
            *(lptr32)(uintptr_t)a_p2 = __float_as_uint(c2_ ? e1 : e0);
            *(lptr32)(uintptr_t)a_p1 = __float_as_uint(d1[0]);
            MEM_FENCE();
            lds_add1a(a_cnt + 4);
            __builtin_amdgcn_s_setprio(2);
            SCHED_FENCE();
#pragma unroll
            for (int j = 0; j < 2; ++j) asm volatile("" :: "v"(xt[j >> 1][j & 1]));
            SCHED_FENCE();
            u4 pv[4];
            for (;;) {
                u32 c;
                asm volatile("ds_read_b32 %0, %5 offset:4\n\tds_read_b128 %1, %6\n\tds_read_b128 %2, %6 offset:16\n\tds_read_b128 %3, %6 offset:32\n\t"
                             "ds_read_b128 %4, %6 offset:48\n\ts_waitcnt lgkmcnt(0)"
                             : "=&v"(c), "=&v"(pv[0]), "=&v"(pv[1]), "=&v"(pv[2]), "=&v"(pv[3]) : "v"(a_cnt), "v"(a_pr) : "memory");
                if (__builtin_amdgcn_readfirstlane(c) >= 4u) break;
            }
            STAMP(2);
#pragma unroll
            for (int j = 2; j < 8; ++j) xt[j >> 1][j & 1] = mfma16b(xa[j >> 1], sel[j & 1], z4);
            float mb, den, rd;
            stats(pv, mb, den, rd);
            const float a = LOG2E * rd, c = -mb * a;
            f2 a2[2], c2[2];
            a2[0].x = dpp_mov<0x00>(a); a2[0].y = dpp_mov<0x55>(a); a2[1].x = dpp_mov<0xaa>(a); a2[1].y = dpp_mov<0xff>(a);
            c2[0].x = dpp_mov<0x00>(c); c2[0].y = dpp_mov<0x55>(c); c2[1].x = dpp_mov<0xaa>(c); c2[1].y = dpp_mov<0xff>(c);
            STAMP(3);
#pragma unroll
            for (int jj = 0; jj < 4; jj += 2) {
                f2 v[8];
                u32 p[8];
#pragma unroll
                for (int i = 0; i < 8; ++i) {
                    const f4 t = xt[jj + (i >> 2)][(i >> 1) & 1];
                    v[i].x = t[2 * (i & 1)]; v[i].y = t[2 * (i & 1) + 1];
                    v[i] = __builtin_elementwise_fma(v[i], a2[i & 1], c2[i & 1]);
                }
                SCHED_FENCE();
#pragma unroll
                for (int i = 0; i < 8; ++i) p[i] = pkh(v[i]);
                SCHED_FENCE();
#pragma unroll
                for (int i = 0; i < 8; ++i) p[i] += 0x00040004u;
                SCHED_FENCE();
                u4 f[2];
#pragma unroll
                for (int k = 0; k < 2; ++k) {
                    const auto s0 = __builtin_amdgcn_permlane16_swap(p[4 * k], p[4 * k + 2], false, false);
                    const auto s1 = __builtin_amdgcn_permlane16_swap(p[4 * k + 1], p[4 * k + 3], false, false);
                    f[k].x = s0[0]; f[k].y = s1[0]; f[k].z = s0[1]; f[k].w = s1[1];
                }
                SCHED_FENCE();
#pragma unroll
                for (int k = 0; k < 2; ++k) {
#pragma unroll
                    for (int i = 0; i < 4; ++i) f[k][i] = bfi(m7, pat[i], f[k][i]);
                    __builtin_amdgcn_raw_buffer_store_b128(f[k], urs, vu[jj + k], 0, 16);
                }
                SCHED_FENCE();
            }
        }
        MEM_FENCE(); SCHED_FENCE();
        lds_add1a(a_cnt + 23 * 4);
        __builtin_amdgcn_s_setprio(3);
        MEM_FENCE(); SCHED_FENCE();
        STAMP(4);
        const int t01 = 2 * hf, t23 = 6 - 2 * hf;
        u32 a_g = sm0 + L_CH + ((lane >> 2) & 3) * CH_SZ + C_U + (UPAD + 64 * wave + 16 * (lane >> 4) + 4 * (lane & 3)) * 2;
        u32 a_g0 = a_g + t01 * 512, a_g1 = a_g + t23 * 512;
        u32 vp2 = hf ? 0x80000000u : vp;
        PIN(a_g0); PIN(a_g1); PIN(vp2);
        __builtin_amdgcn_s_sleep(U_DELAY);
        lds_wait(cnt + 31, 1);
        STAMP(5);
        u4 up[8];
        u2 sg[4];
        auto ld_stats = [&]() __attribute__((always_inline)) {
#pragma unroll
            for (int t = 0; t < 4; ++t) sg[t] = __builtin_amdgcn_raw_buffer_load_b64(grs, vs + (t & 1) * 2048, (t < 2 ? t01 : t23) * 2048, 16);
        };
#pragma unroll
        for (int p = 0; p < 8; ++p) up[p] = BLD128(urs, p < 6 ? vp : vp2, p * 4096, 16);
        MEM_FENCE(); SCHED_FENCE();
        lds_add1a(a_cnt + 44);
        MEM_FENCE(); SCHED_FENCE();
        {
            u32 bad = 0, bad6 = 0;
#pragma unroll
            for (int p = 0; p < 8; ++p) {
#pragma unroll
                for (int i = 0; i < 4; ++i) up[p][i] ^= pat[i];
                *(lptr128)(uintptr_t)(a_u + p * 512) = up[p];
                bad |= up[p].x | up[p].y; PIN(bad);
                bad |= up[p].z | up[p].w; PIN(bad);
                if (p == 5) bad6 = bad;
                SCHED_FENCE();
            }
            STAMP(13);
            if (__builtin_expect(__any(((hf ? bad6 : bad) & m7) != 0), 0)) {
                u32 miss = 0;
#pragma unroll
                for (int p = 0; p < 8; ++p) if (__any(((up[p].x | up[p].y | up[p].z | up[p].w) & m7) != 0)) miss |= 1u << p;
                if (hf) miss &= 0x3fu;
#pragma unroll 1
                for (int spin = 0; miss != 0 && spin < SPIN_MAX; ++spin) {
                    MEM_FENCE();
#pragma unroll
                    for (int p = 0; p < 8; ++p) if ((miss >> p) & 1u) up[p] = BLD128(urs, vp, p * 4096, 16);
#pragma unroll
                    for (int p = 0; p < 8; ++p) if ((miss >> p) & 1u) {
#pragma unroll
                        for (int i = 0; i < 4; ++i) up[p][i] ^= pat[i];
                        *(lptr128)(uintptr_t)(a_u + p * 512) = up[p];
                        if (!__any(((up[p].x | up[p].y | up[p].z | up[p].w) & m7) != 0)) miss &= ~(1u << p);
                    }
                }
            }
            STAMP(6);
            MEM_FENCE();
            lds_add1a(a_cnt + 40);
            MEM_FENCE(); SCHED_FENCE();
        }
        ld_stats();
        MEM_FENCE(); SCHED_FENCE();
        __builtin_amdgcn_s_setprio(0);
        u2 ut[4][2];
        asm volatile("ds_read_b64_tr_b16 %0, %8\n\tds_read_b64_tr_b16 %1, %8 offset:%10\n\tds_read_b64_tr_b16 %2, %8 offset:512\n\tds_read_b64_tr_b16 %3, %8 offset:512+%10\n\t"
                     "ds_read_b64_tr_b16 %4, %9\n\tds_read_b64_tr_b16 %5, %9 offset:%10\n\tds_read_b64_tr_b16 %6, %9 offset:512\n\tds_read_b64_tr_b16 %7, %9 offset:512+%10\n\t"
                     "s_waitcnt lgkmcnt(0)"
                     : "=&v"(ut[0][0]), "=&v"(ut[0][1]), "=&v"(ut[1][0]), "=&v"(ut[1][1]), "=&v"(ut[2][0]), "=&v"(ut[2][1]), "=&v"(ut[3][0]), "=&v"(ut[3][1])
                     : "v"(a_g0), "v"(a_g1), "n"(4 * CH_SZ) : "memory");
        STAMP(7);
        u32 gh[4][4];
        float xf[4][8];
        auto gate = [&](int t) __attribute__((always_inline)) {
            gate4(gh[t], ut[t][0].x, ut[t][0].y, ut[t][1].x, ut[t][1].y);
            MEM_FENCE(); SCHED_FENCE();
        };
        auto resid = [&](int t) __attribute__((always_inline)) {
            const float mt = bflo(sg[t].x), dt = bfhi(sg[t].x) * (1.0f / LOG2E);
#pragma unroll
            for (int c = 0; c < 8; ++c) {
                const u32 uw = ut[t][c >> 2][(c >> 1) & 1];
                float xx = __builtin_fmaf((float)__builtin_bit_cast(h2, uw)[c & 1], dt, mt);
                PIN(xx);
                xf[t][c] = xx;
            }
            MEM_FENCE(); SCHED_FENCE();
        };
        gate(0); gate(1); gate(2);
        if (__builtin_expect(__any(sg[0].y != tag || sg[1].y != tag || sg[2].y != tag || sg[3].y != tag), 0)) {
#pragma unroll 1
            for (int spin = 0; spin < SPIN_MAX; ++spin) {
                MEM_FENCE();
                ld_stats();
                if (!__any(sg[0].y != tag || sg[1].y != tag || sg[2].y != tag || sg[3].y != tag)) break;
                __builtin_amdgcn_s_sleep(G_SLEEP);
            }
        }
        resid(0); resid(1); resid(2);
        lds_wait(cnt + 13, 1);
        u32 vo[4];
        {
            const int i0 = 0xffff - (int)(__builtin_amdgcn_readfirstlane(cnt[12]) & 0xffffu);
            const int i1 = 0xffff - (int)(__builtin_amdgcn_readfirstlane(cnt[13]) & 0xffffu);
#pragma unroll
            for (int j = 0; j < 4; ++j) {
                const int l = 256 * ((j < 2 ? t01 : t23) + (j & 1)) + tid;
                vo[j] = (l == i0 || l == i1) ? 0x80000000u : l * (SD * 2) + g * 16;
                PIN(vo[j]);
            }
        }
        const char* yp = smem + L_STG + 64 * (tid >> 4) + 16 * (((tid >> 2) & 3) ^ ((tid >> 5) & 3)) + 4 * (tid & 3);
        const rsrc_t ors = ORS;
        STAMP(8);
        auto epi = [&](int t) __attribute__((always_inline)) {
            if (t >= E_UP) __builtin_amdgcn_s_setprio(3);
            while (__hip_atomic_load(reinterpret_cast<unsigned long long*>(cnt + 2 + 2 * t), __ATOMIC_RELAXED, __HIP_MEMORY_SCOPE_WORKGROUP) != all8) __builtin_amdgcn_s_sleep(1);
            __builtin_amdgcn_fence(__ATOMIC_ACQUIRE, "wavefront");
            __builtin_amdgcn_wave_barrier();
            if (t == 0) STAMP(9);
            if (t == 2) STAMP(14);
            if (t == 3) STAMP(11);
            float yv[8];
#pragma unroll
            for (int c = 0; c < 8; ++c) yv[c] = *reinterpret_cast<const float*>(yp + c * STG_BUF + 1024 * t);
            u4 o;
#pragma unroll
            for (int h = 0; h < 4; ++h) {
                const h2 gg = __builtin_bit_cast(h2, gh[t][h]);
                float o0 = __builtin_fmaf(yv[2 * h], (float)gg[0], xf[t][2 * h]);
                float o1 = __builtin_fmaf(yv[2 * h + 1], (float)gg[1], xf[t][2 * h + 1]);
                PIN(o0); PIN(o1);
                o[h] = pkbf(o0, o1);
            }
            if (t < PACE) { __builtin_amdgcn_s_waitcnt(0x0f70); MEM_FENCE(); SCHED_FENCE(); }
            __builtin_amdgcn_raw_buffer_store_b128(o, ors, vo[t], 0, 0);
            MEM_FENCE(); SCHED_FENCE();
            if (t == 1) STAMP(10);
            if (t == 3) STAMP(12);
        };
        epi(0);
        gate(3); resid(3);
        epi(1); epi(2); epi(3);
    };
    auto f_role = [&]() __attribute__((always_inline)) {
        constexpr int WE = W_A;
        static_assert(W_A >= 2 && W_A + W_B + W_E <= 16, "weight loads");
        ARGS_LOAD();
        __builtin_amdgcn_s_setprio(3);
        LANE_IDS();
        const int hw = wave - 4;
        const bool has_gu = slot < 28;
        const int s7 = (slot * 37) >> 8, mat = s7 & 1, quart = 2 * (xcd >> 2) + ((s7 >> 1) & 1), cblk = 7 * ((xcd + 3) & 3) + slot - 7 * s7, ksl = 4 * quart + hw;
        const int sup = (xcd & 3) * 8 + (slot >> 2), chk = 4 * sup + hw, dblk = (xcd < 4 ? 4 : 0) + (slot & 3);
        auto f_box = [&]() __attribute__((always_inline)) {
            const int off = (hw == 2) ? 4 : 8, bw = (hw == 0) ? 2 : (hw == 1) ? 4 : (hw == 2) ? 1 : 3;
            *reinterpret_cast<h8*>(smem + L_BOX + (hw == 3 ? 4 : hw) * 1024 + lane * 16) = box_frag(al, q, off, bw);
            if (hw == 2) *reinterpret_cast<h8*>(smem + L_BOX + 3072 + lane * 16) = box_frag(al, q, 12, 1);
            const u4 z = {0, 0, 0, 0};
#pragma unroll
            for (int i = 0; i < 3; ++i) *reinterpret_cast<u4*>(smem + L_Z + hw * IMG + min(i * 1024 + lane * 16, IMG - 16)) = z;
            MEM_FENCE();
            SCHED_FENCE();
        };
        u32 wvo = ((64 * ksl + q) * SF + 128 * cblk + 8 * al) * 2;
        u32 a_cnt = sm0 + L_CNT, v16 = (hw == 0) ? lane * 16 : 0x80000000u;
        PIN(wvo); PIN(a_cnt); PIN(v16);
        ARGS_WAIT();
        u4 wr[2], wv[16], wdv[8];
        {
            const rsrc_t rrs = mk_rsrc(ka0[2], ka0[3], 0, SD * 2);
            wr[0] = BLD128(rrs, v16, 0, 0); wr[1] = BLD128(rrs, v16, 1024, 0);
        }
        MEM_FENCE(); SCHED_FENCE();
        lds_wait(cnt + 17, 12);
        const rsrc_t wrs = mk_rsrc(mat ? ka2[6] : ka2[4], mat ? ka2[7] : ka2[5], 0, has_gu ? SD * SF * 2 : 0);
        auto load_w = [&](int j) __attribute__((always_inline)) {
            wv[j] = BLD128(wrs, wvo, 4 * j * (SF * 2), 2);
            MEM_FENCE(); SCHED_FENCE();

        };
        STAMP(8);
        load_w(0); load_w(1);
        f_box();
        lds_add1a(a_cnt + 22 * 4);
#pragma unroll
        for (int j = 2; j < WE; ++j) { load_w(j); if (j + 1 < WE) __builtin_amdgcn_s_sleep(W_PACE); }
        MEM_FENCE(); SCHED_FENCE();
        STAMP(10);
        const rsrc_t grs = GRS;
        u32 tag;
        {
            float sw = 0.f;
            u32 a_pr = sm0 + L_P12 + (lane & 7) * P12_S, a_p3 = sm0 + L_P3 + (lane & 7) * P3_S;
            u32 vpub = (hw == 0) ? (lane < NREP ? G_KEY + (lane * 256 + blockIdx.x) * 16 : 0x80000000u) : ((lane & 1) ? 0x80000000u : ((lane >> 3) * SL + b * 8 + (lane & 7)) * 8);
            u32 ck = 0xffff - (b * 8 + (lane & 7));
            PIN(a_pr); PIN(a_p3); PIN(vpub); PIN(ck);
            lds_wait(cnt + 0, 1);
            tag = __builtin_amdgcn_readfirstlane(cnt[28]);
            MEM_FENCE(); SCHED_FENCE();
            if (hw == 0) {
#pragma unroll
                for (int j = 0; j < 8; ++j) sw = dot2(wr[j >> 2][j & 3], 0x3f803f80u, sw);
                sw = wave_sum(sw);
            }
            MEM_FENCE(); SCHED_FENCE();
            if (hw < 2) {
                u4 pv[4];
                f4 s0, s1;
                lds_wait(cnt + 1, 3);
                for (;;) {
                    u32 c;
                    asm volatile("ds_read_b32 %0, %7 offset:4\n\tds_read_b128 %1, %8\n\tds_read_b128 %2, %8 offset:16\n\tds_read_b128 %3, %8 offset:32\n\t"
                                 "ds_read_b128 %4, %8 offset:48\n\tds_read_b128 %5, %9\n\tds_read_b128 %6, %9 offset:16\n\ts_waitcnt lgkmcnt(0)"
                                 : "=&v"(c), "=&v"(pv[0]), "=&v"(pv[1]), "=&v"(pv[2]), "=&v"(pv[3]), "=&v"(s0), "=&v"(s1) : "v"(a_cnt), "v"(a_pr), "v"(a_p3) : "memory");
                    const u32 cc = __builtin_amdgcn_readfirstlane(c);
                    if (cc >= 4u) break;
                    if (cc == 0u) __builtin_amdgcn_s_sleep(1);
                }
                float mb, den, rd;
                stats(pv, mb, den, rd);
                u4 f; f.y = tag; f.w = tag;
                if (hw == 0) {
                    const f4 sv = s0 + s1;
                    const u32 rb = __float_as_uint(rbf(__builtin_fmaf(-mb, sw, (sv[0] + sv[1]) + (sv[2] + sv[3])) * rd));
                    u32 m1 = ((rb ^ ((u32)((int)rb >> 31) | 0x80000000u)) & 0xffff0000u) | ck, m2 = 0;
                    top2_of8(m1, m2);
                    f.x = __builtin_amdgcn_readlane(m1, 7); f.z = __builtin_amdgcn_readlane(m2, 7);
                } else {
                    f.x = __builtin_amdgcn_perm(__float_as_uint(den), __float_as_uint(mb), 0x07060302u);
                    f.z = __builtin_amdgcn_mov_dpp(f.x, 0xf5, 0xf, 0xf, true);
                }
                __builtin_amdgcn_raw_buffer_store_b128(f, grs, vpub, 0, 16);
            }
            MEM_FENCE(); SCHED_FENCE();
        }
        STAMP(4);
#pragma unroll
        for (int j = WE; j < WE + W_B; ++j) { load_w(j); if (hw >= 2) __builtin_amdgcn_s_sleep(W_PACE2); }
        u32 vtag = tag, dvo = (q * SD + 128 * dblk + 8 * al) * 2;
        const u32 img = sm0 + L_XN + hw * IMG;
        u32 aoff = img + (al >> 2) * XN_S + 16 * q, a_red = sm0 + L_XN + q * XN_S + al * 16;
        u32 idx = ((al & 3) * 0x4444u) | (((al & 3) ^ 1) * 0x1111u);
        const u32 Q = 32 * cblk + (lane & 31), cj = __umul24(Q, 9363u) >> 16;
        u32 gu_off = has_gu ? (__umul24(cj, 121u) + Q + ((q >> 1) * 8 + quart * 2 + mat) * 8) * 16 : 0x80000000u;
        u32 aoffs = (chk * 128 + lane - ((lane & 7) == 7 ? 1 : 0)) * 16;
        u32 h_st = img + ((lane >> 3) & 1) * (2 * XN_S) + (4 * (lane & 7) + q) * 8;
        u32 dn_off = (GU_WORDS * 4) + ((sup * 8 + dblk) * 64 + lane) * 16;
        PIN(vtag); PIN(dvo); PIN(aoff); PIN(a_red); PIN(idx); PIN(gu_off); PIN(aoffs); PIN(h_st); PIN(dn_off);
        MEM_FENCE(); SCHED_FENCE();
#pragma unroll
        for (int j = WE + W_B; j < 16 - W_E; ++j) { load_w(j); __builtin_amdgcn_s_sleep(W_PACE2); }
        STAMP(5);
        int dso[7];
#pragma unroll
        for (int j = 0; j < 7; ++j) {
            const int Qd = 7 * chk + j;
            dso[j] = (128 * (Qd >> 5) + 8 * (Qd & 15) + 4 * ((Qd >> 4) & 1)) * (SD * 2);
            asm volatile("" : "+s"(dso[j]));
        }
        u32 k1, k2;
        if (hw == 3) {
            u32 vk = G_KEY + ((blockIdx.x % NREP) * 256 + lane) * 16;
            PIN(vk);
            lds_wait(cnt + 23, 4);
            __builtin_amdgcn_s_sleep(G_DELAY);
            u4 kg[4];
            poll_pieces<4, G_SLEEP>(kg, [&](int p) { return BLD128(grs, vk + 1024 * p, 0, 16); }, [&](u4 v_) { return v_.w != tag; });
            lds_add1a(a_cnt + 31 * 4);
            STAMP(2);
            u32 m1 = max(kg[0].x, kg[1].x), m2 = min(kg[0].x, kg[1].x);
            m2 = med3u(m1, m2, kg[2].x); m1 = max(m1, kg[2].x); m2 = med3u(m1, m2, kg[3].x); m1 = max(m1, kg[3].x);
            m2 = max(max(m2, max(kg[0].z, kg[1].z)), max(kg[2].z, kg[3].z));
            wave_top2(m1, m2);
            u2 m; m.x = m1; m.y = m2;
            asm volatile("s_mov_b32 exec_lo, 0\n\ts_brev_b32 exec_hi, 1\n\tds_write_b64 %0, %1 offset:48\n\ts_mov_b64 exec, -1" :: "v"(a_cnt), "v"(m) : "memory");
            k1 = __builtin_amdgcn_readlane(m1, 63); k2 = __builtin_amdgcn_readlane(m2, 63);
        } else {
            __builtin_amdgcn_s_sleep(G_DELAY);
            for (;;) {
                u2 m;
                asm volatile("ds_read_b64 %0, %1 offset:48\n\ts_waitcnt lgkmcnt(0)" : "=v"(m) : "v"(a_cnt) : "memory");
                k1 = __builtin_amdgcn_readfirstlane(m.x); k2 = __builtin_amdgcn_readfirstlane(m.y);
                if (k2) break;
                __builtin_amdgcn_s_sleep(1);
            }
        }
        MEM_FENCE(); SCHED_FENCE();
        const u32 ts = (0xffffu - ((lane & 32) ? k2 : k1)) & (SL - 1), osr = (xcd * SL + ts) * 8;
        const u32 xr = __builtin_amdgcn_raw_buffer_load_b32(XRS, ts * (SD * 2) + 128 * ksl + 4 * (lane & 31), 0, 0);
        u2 sr = __builtin_amdgcn_raw_buffer_load_b64(grs, osr, 0, 16);
        MEM_FENCE(); SCHED_FENCE();
        lds_wait(cnt + 11, 4);
#pragma unroll
        for (int j = 16 - W_E; j < 16; ++j) load_w(j);
        MEM_FENCE(); SCHED_FENCE();
        {
            u32 a_xn = img + (lane >> 5) * (2 * XN_S) + (lane & 31) * 16;
            PIN(a_xn);
            if (__builtin_expect(!__all(sr.y == tag), 0)) {
#pragma unroll 1
                for (int spin = 0; spin < SPIN_MAX && !__all(sr.y == tag); ++spin) { MEM_FENCE(); sr = __builtin_amdgcn_raw_buffer_load_b64(grs, osr, 0, 16); __builtin_amdgcn_s_waitcnt(0x0f70); }
            }
            const float mb2 = bflo(sr.x), rc2 = __builtin_amdgcn_rcpf(bfhi(sr.x));
            float e0 = bflo(xr) - mb2, e1 = bfhi(xr) - mb2;
            PIN(e0); PIN(e1);
            e0 = rbf(e0) * rc2; e1 = rbf(e1) * rc2;
            PIN(e0); PIN(e1);
            f2 o0; o0.x = rbf(e0); o0.y = rbf(e1);
#pragma unroll
            for (int h = 0; h < 2; ++h) {
                *(lptr32)(uintptr_t)(a_xn + h * (XN_S + 4)) = __float_as_uint(o0.x); *(lptr32)(uintptr_t)(a_xn + h * (XN_S + 4) + 8) = __float_as_uint(o0.y);
            }
            WAVE_SYNC();
            MEM_FENCE(); SCHED_FENCE();
            STAMP(3);
        }
        const u32 stf = __builtin_amdgcn_readlane(sr.x, 32 * (hw >> 1));
        auto reduce_send = [&](f4 a0, auto off_c, int ci, u32 voff) __attribute__((always_inline)) {
            constexpr int OFF = decltype(off_c)::value;
            *(__attribute__((address_space(3))) f4*)(uintptr_t)(a_red + hw * IMG + OFF) = a0;
            u32 old; f4 r[4];
            const u32 one = 1u;
            asm volatile("s_mov_b64 exec, 1\n\tds_add_rtn_u32 %0, %5, %6\n\ts_mov_b64 exec, -1\n\tds_read_b128 %1, %7 offset:%8\n\tds_read_b128 %2, %7 offset:%8+%9\n\t"
                         "ds_read_b128 %3, %7 offset:%8+2*%9\n\tds_read_b128 %4, %7 offset:%8+3*%9\n\ts_waitcnt lgkmcnt(0)"
                         : "=&v"(old), "=&v"(r[0]), "=&v"(r[1]), "=&v"(r[2]), "=&v"(r[3]) : "v"(a_cnt + 4 * ci), "v"(one), "v"(a_red), "n"(OFF), "n"(IMG) : "memory");
            if (__builtin_amdgcn_readfirstlane(old) == 3u) {
                f4 t;
#pragma unroll
                for (int i = 0; i < 4; ++i) { float t0 = r[0][i] + r[1][i], t1 = r[2][i] + r[3][i]; PIN(t0); PIN(t1); t0 += t1; PIN(t0); t[i] = t0; }
                __builtin_amdgcn_raw_buffer_store_b128(tag4(t, vtag), HRS, voff, 0, 16);
                MEM_FENCE(); SCHED_FENCE();
                lds_add1a(a_cnt + 4 * ci);
            }
        };
        auto load_wd = [&](int j0, int j1) __attribute__((always_inline)) {
            const rsrc_t drs = mk_rsrc(ka3[0], ka3[1], 0, SD * SF * 2);
            MEM_FENCE(); SCHED_FENCE();
#pragma unroll
            for (int j = j0; j < j1; ++j) {
                wdv[j] = BLD128(drs, dvo, dso[j], 2);
            }
            wdv[7] = u4{0, 0, 0, 0};
            MEM_FENCE(); SCHED_FENCE();
        };
        load_wd(0, WD_A);
        STAMP(6);
        const rsrc_t hrs = HRS;
        {
            f4 a0 = {0, 0, 0, 0};
            u4 Af[4], Ag[4];
#pragma unroll
            for (int t = 0; t < 4; ++t) Ag[t] = *(__attribute__((address_space(3))) const u4*)(uintptr_t)(aoff + 64 * t);
#pragma unroll
            for (int t = 0; t < 4; ++t) Af[t] = *(__attribute__((address_space(3))) const u4*)(uintptr_t)(aoff + 256 + 64 * t);
#pragma unroll
            for (int t = 0; t < 4; ++t) a0 = smfma(Ag[t], wv[2 * t], wv[2 * t + 1], a0, idx);
#pragma unroll
            for (int t = 0; t < 4; ++t) a0 = smfma(Af[t], wv[8 + 2 * t], wv[9 + 2 * t], a0, idx);
            STAMP(7);
            reduce_send(a0, std::integral_constant<int, 288>{}, 19, gu_off);
        }
        MEM_FENCE(); SCHED_FENCE();
        STAMP(9);
        lds_wait(cnt + 19, 5);
        load_wd(WD_A, 7);
        if (blockIdx.x == 0 && tid == 256)
            __hip_atomic_store((__attribute__((address_space(1))) u32*)((((unsigned long long)ka0[5] << 32) | ka0[4]) + WS_EPOCH), tag, __ATOMIC_RELAXED, __HIP_MEMORY_SCOPE_AGENT);
        const u32 keyf = hw < 2 ? k1 : k2;
        const int tokf = (0xffff - (int)(keyf & 0xffffu)) & (SL - 1), ftile = tokf >> 8;
        const bool mine = hf ? (ftile >= 2 && ftile <= 5) : (ftile < 2 || ftile > 5);
        const int chf = 4 * (hw & 1) + q;
        const u32 eoff = (tokf * SD + g * 8 + chf) * 2;
        const u32 xe = __builtin_amdgcn_raw_buffer_load_b16(XRS, mine ? eoff : 0x80000000u, 0, 0);
        MEM_FENCE(); SCHED_FENCE();
        __builtin_amdgcn_s_sleep(A_DELAY);
        STAMP(11);
        {
            u4 pv[2];
            poll_pieces<2, A_SLEEP>(pv, [&](int i) { return BLD128(hrs, aoffs + 1024 * i, 0, 16); }, [&](u4 v) { return tagof(v) != tag; });
            STAMP(12);
            int r[2];
#pragma unroll
            for (int i = 0; i < 2; ++i) {
                const u4 p = untag4(pv[i]);
                r[i] = __float_as_int(rbf(fold4(__uint_as_float(p[0]), __uint_as_float(p[2]), __uint_as_float(p[1]), __uint_as_float(p[3]))));
            }
            const float gv = __int_as_float(__builtin_amdgcn_update_dpp(r[0], r[1], 0x128, 0xf, 0xc, false));
            const float up = __int_as_float(__builtin_amdgcn_update_dpp(r[1], r[0], 0x128, 0xf, 0x3, false));
            const u32 hv = __float_as_uint(rbf(rbf(silu_fast(gv)) * up));
            *(lptr32)(uintptr_t)h_st = hv; *(lptr32)(uintptr_t)(h_st + XN_S + 4) = hv;
            WAVE_SYNC();
            u4 Ah[4];
#pragma unroll
            for (int t = 0; t < 4; ++t) Ah[t] = *(__attribute__((address_space(3))) const u4*)(uintptr_t)(aoff + 64 * t);
            f4 a0 = {0, 0, 0, 0};
#pragma unroll
            for (int t = 0; t < 4; ++t) a0 = smfma(Ah[t], wdv[2 * t], wdv[2 * t + 1], a0, idx);
            WAVE_SYNC();
            reduce_send(a0, std::integral_constant<int, 0>{}, 18, dn_off);
        }
        MEM_FENCE(); SCHED_FENCE();
        STAMP(13);
        if (mine) {
            u32 boff = lane < 32 ? (GU_WORDS * 4) + ((lane * 8 + (g >> 4)) * 64 + hw * 16 + (g & 15)) * 16 : 0x80000000u;
            const u32 rb = (keyf & 0x80000000u) ? (keyf & 0x7fff0000u) : (~keyf & 0xffff0000u);
            float rs = rbf(__builtin_amdgcn_rcpf(1.0f + __builtin_amdgcn_exp2f(-LOG2E * __uint_as_float(rb))));
            const int oi = hf ? ftile - 2 : (ftile < 2 ? ftile : ftile - 4);
            float xd = bflo(xe);
            PIN(xd);
            xd -= bflo(stf);
            PIN(xd);
            float xnv = rbf(rbf(xd) * __builtin_amdgcn_rcpf(bfhi(stf)));
            const int tt = tokf & 255;
            u32 ya = sm0 + L_STG + chf * STG_BUF + 1024 * oi + 64 * (tt >> 4) + 16 * (((tt >> 2) & 3) ^ ((tt >> 5) & 3)) + 4 * (tt & 3);
            u32 yc = a_cnt + 8 + 8 * oi + chf;
            u32 eo = (al == 0) ? eoff : 0x80000000u;
            PIN(boff); PIN(rs); PIN(xnv); PIN(ya); PIN(yc); PIN(eo);
            MEM_FENCE(); SCHED_FENCE();
            __builtin_amdgcn_s_sleep(B_DELAY);
            u4 pv = BLD128(hrs, boff, 0, 16);
#pragma unroll 1
            for (int spin = 0; spin < SPIN_MAX; ++spin) {
                const bool b0 = tagof(pv) != tag && lane < 32;
                if (!__any(b0)) break;
                MEM_FENCE();
                __builtin_amdgcn_s_sleep(B_SLEEP);
                if (b0) pv = BLD128(hrs, boff, 0, 16);
            }
            STAMP(14);
            float sv; u32 c;
            asm volatile("ds_read_u8 %0, %2\n\tds_read_b32 %1, %3" : "=&v"(c), "=&v"(sv) : "v"(yc), "v"(ya) : "memory");
            SCHED_FENCE();
            pv = untag4(pv);
            const float tot = row_sum(fold4(__uint_as_float(pv[0]), __uint_as_float(pv[2]), __uint_as_float(pv[1]), __uint_as_float(pv[3])));
            float gt = LOG2E * silu_fast(rbf(xnv + rbf(rbf(tot) * rs)));
            asm volatile("s_waitcnt lgkmcnt(0)" : "+v"(c), "+v"(sv), "+v"(gt) :: "memory");
            while (__any(c == 0u)) {
                __builtin_amdgcn_s_sleep(1);
                asm volatile("ds_read_u8 %0, %2\n\tds_read_b32 %1, %3\n\ts_waitcnt lgkmcnt(0)" : "=&v"(c), "=&v"(sv) : "v"(yc), "v"(ya) : "memory");
            }
            const float ov = __builtin_fmaf(sv, gt, bflo(xe));
            __builtin_amdgcn_raw_buffer_store_b16((u16)pkbf(ov, ov), ORS, eo, 0, 0);
        }
    };
    if (wave < 4) {
        e_role();
    } else if (wave < 8) {
        f_role();
    } else {
        ARGS_LOAD();
        const int cw = wave - 8;
        __builtin_amdgcn_s_setprio(2);
        const int d = g * 8 + cw;
        char* ch = smem + L_CH + cw * CH_SZ;
        u32 vp = d * (SN * 2) + (tid & 63) * 4, vd = d * 2;
        PIN(vp); PIN(vd);
        ARGS_WAIT();
        const u32 pDT = __builtin_amdgcn_raw_buffer_load_b16(mk_rsrc(ka2[0], ka2[1], 0, SD * 2), vd, 0, 0);
        const u32 pLA = __builtin_amdgcn_raw_buffer_load_b32(mk_rsrc(ka1[0], ka1[1], 0, SD * SN * 2), vp, 0, 2);
        const u32 pAI = __builtin_amdgcn_raw_buffer_load_b32(mk_rsrc(ka1[2], ka1[3], 0, SD * SN * 2), vp, 0, 2);
        const u32 pCR = __builtin_amdgcn_raw_buffer_load_b32(mk_rsrc(ka1[4], ka1[5], 0, SD * SN * 2), vp, 0, 2);
        const u32 pCI = __builtin_amdgcn_raw_buffer_load_b32(mk_rsrc(ka1[6], ka1[7], 0, SD * SN * 2), vp, 0, 2);
        const u32 pDS = __builtin_amdgcn_raw_buffer_load_b16(mk_rsrc(ka2[2], ka2[3], 0, SD * 2), vd, 0, 0);
        STAMP(4);
        URGENT_DONE();
        int lane = tid & 63;
        PIN(lane);
        const int al = lane & 15, q = lane >> 4;
        auto clear = [&](char* c, int n) {
            const u4 z = {0, 0, 0, 0};
#pragma unroll
            for (int i = 0; i < 4; ++i) *reinterpret_cast<u4*>(c + i * 1024 + lane * 16) = z;
            if (lane < n) *reinterpret_cast<u4*>(c + 4096 + lane * 16) = z;
        };
        auto put4 = [&](char* c, int idx, u32 lo, u32 hi) {
            u16* rec = reinterpret_cast<u16*>(c);
            u16* rec1 = reinterpret_cast<u16*>(c + REC_B + 2);
            u2 o; o.x = lo; o.y = hi;
            *reinterpret_cast<u2*>(rec + idx) = o;
            rec1[idx] = (u16)lo;
            *reinterpret_cast<u32*>(rec1 + idx + 1) = (lo >> 16) | (hi << 16);
            rec1[idx + 3] = (u16)(hi >> 16);
        };
        const u32 chb = sm0 + L_CH + cw * CH_SZ, nul = sm0 + L_NUL + lane * 4;
        u32 sc_x = R_KA + ZA - 3 - 128 - 256 * (q >> 1) - 4 * (q & 1) - 8 * al;
        u32 sc_2 = chb + (R_K2 + 190 - 32 * (q >> 1) - 64 * (q & 1) - 2 * al) * 2, sc_h = chb + (R_K3 + 127 - 16 - 32 * q - al) * 2, sc_l = lane ? sc_h + 32 : nul;
        const int pj = (lane < 18) ? (lane >= 9 ? lane - 9 : lane) : (lane >= 21 ? lane - 21 : lane - 18), pcopy = (lane < 18) ? (lane >= 9) : (lane >= 21);
        u32 sc_p = lane < 24 ? chb + ((lane < 18) ? (R_KP + 1055 - 1027 - pj) : (R_KA + ZA - 514 + pj)) * 2 + (pcopy ? REC_B + 2 : 0) : nul;
        int pcls = (lane < 18) ? (pj < 2 ? 0 : 1) : 2;
        const u32 stg = sm0 + L_STG + cw * STG_BUF + q * 16;
        u32 fr_b = stg + al * STG_ROW, fr_z = stg + 16 * STG_ROW;
        u32 fr_x = stg + (al < 8 ? al : 8 + (((al & 3) == 2) ? ((al - 8) >> 1) : (2 * ((al - 7) >> 2)))) * STG_ROW;
        u32 fr_y = stg + ((al & 10) == 0 ? 13 + (al >> 1) + (al & 1) - ((al >> 1) & 1) : (al >> 2)) * STG_ROW;
        asm volatile("s_mov_b32 m0, %0\n\ts_nop 0" :: "s"(sm0 + L_STG + cw * STG_BUF) : "memory");
        {
            hcx one; one.r = h2{(f16)1.0f, (f16)1.0f}; one.i = h2{(f16)0.0f, (f16)0.0f};
            stg_put<0>(one);
        }
        clear(ch, 40);
        PIN(sc_x); PIN(sc_2); PIN(sc_h); PIN(sc_l); PIN(sc_p); PIN(pcls); PIN(fr_b); PIN(fr_z); PIN(fr_x); PIN(fr_y);
        MEM_FENCE(); SCHED_FENCE();
        const float dt = rbf(exp_acc(bflo(pDT)));
        f2 ar, ai, cr, ci;
        {
            f2 la = bfup(pLA);
            f2 ex; ex.x = __builtin_amdgcn_exp2f(la.x * LOG2E); ex.y = __builtin_amdgcn_exp2f(la.y * LOG2E);
            ar = (-rbf2(ex) * dt) * LOG2E;
            ai = (bfup(pAI) * dt) * 0.15915494309189535f;
            cr = bfup(pCR) * dt;
            ci = bfup(pCI) * dt;
        }
        asm volatile("" :: "v"(code_pf));
        STAMP(1);
        const float bound = __builtin_fmaf(2.0f, wave_sum(fabsf(cr.x) + fabsf(cr.y) + fabsf(ci.x) + fabsf(ci.y)), fabsf(bflo(pDS)));
        int ke = 14 - ((int)((__float_as_uint(bound) >> 23) & 0xff) - 127);
        ke = __builtin_amdgcn_readfirstlane(min(max(ke, -100), 100));
        const float sig2 = __uint_as_float((u32)(127 + ke + 1) << 23);
        const float yscale = __uint_as_float((u32)(127 - ke) << 23) * (1.0f / (LOG2E * LOG2E));
        cplx G0; G0.r = cr * sig2; G0.i = -ci * sig2;
        float skip = (lane == 0) ? 0.5f * sig2 * bflo(pDS) : 0.0f;
        PIN(skip);
        typedef __attribute__((address_space(3))) u16* lptr16;
        {
            typedef __attribute__((address_space(3))) const u4* lq;
            const cplx E1 = cexp2(ar, ai, 1.0f), E2 = csq(E1), E4 = csq(E2), E8 = csq(E4);
            const hcx z1 = toh(E1), z2 = toh(E2), z4 = toh(E4), z8 = toh(E8);
            {
                const cplx E16 = csq(E8), E32 = csq(E16), E64 = csq(E32);
                hcx c[8];
                c[1] = z8; c[2] = toh(E16); c[4] = toh(E32);
                const hcx p64 = toh(E64), p128 = toh(csq(E64));
                c[3] = hmul(c[1], c[2]);
                stg_put<1>(c[1]); stg_put<2>(c[2]); stg_put<4>(c[4]); stg_put<8>(p64);
                c[5] = hmul(c[1], c[4]); c[6] = hmul(c[2], c[4]); c[7] = hmul(c[3], c[4]);
                stg_put<16>(p128); stg_put<3>(c[3]);
                const hcx t9 = hmul(c[1], p64), t10 = hmul(c[2], p64);
                stg_put<5>(c[5]); stg_put<6>(c[6]);
                const hcx t11 = hmul(c[3], p64), t12 = hmul(c[4], p64);
                stg_put<7>(c[7]); stg_put<9>(t9);
                const hcx t13 = hmul(c[5], p64), t14 = hmul(c[6], p64);
                stg_put<10>(t10); stg_put<11>(t11);
                const hcx t15 = hmul(c[7], p64);
                stg_put<12>(t12); stg_put<13>(t13); stg_put<14>(t14); stg_put<15>(t15);
            }
            WAVE_SYNC();
            u4 B0[8], B1[8], Af[8];
#pragma unroll
            for (int s = 0; s < 8; ++s) B0[s] = *(lq)(uintptr_t)(fr_b + s * 64);
#pragma unroll
            for (int s = 0; s < 8; ++s) B1[s] = *(lq)(uintptr_t)(fr_z + s * 64);
            WAVE_SYNC();
            STAMP(8);
            __builtin_amdgcn_s_sleep(K_S1);
            if (cw < 4) __builtin_amdgcn_s_setprio(1);
            hcx J[4];
            {
                const cplx F1 = cexp2(ar, ai, 256.0f), F2 = csq(F1);
                const hcx f1 = toh(F1), f2_ = toh(F2), f4_ = toh(csq(F2));
                const hcx g0 = toh(G0), g1 = hmul_conj(g0, z1), g2 = hmul_conj(g0, z2), g4 = hmul_conj(g0, z4), g8 = hmul_conj(g0, z8);
                stg_put<0>(g0); stg_put<1>(g1);
                const hcx g3 = hmul_conj(g1, z2), g5 = hmul_conj(g1, z4);
                stg_put<2>(g2); stg_put<4>(g4);
                const hcx g6 = hmul_conj(g2, z4), g7 = hmul_conj(g3, z4);
                stg_put<3>(g3); stg_put<5>(g5);
                const hcx h0 = hmul_conj(g0, f1), h2_ = hmul_conj(g2, f1);
                stg_put<6>(g6); stg_put<7>(g7);
                const hcx h4 = hmul_conj(g4, f1), h6 = hmul_conj(g6, f1);
                stg_put<8>(h0); stg_put<9>(h2_);
                const hcx j1 = hmul_conj(g8, f1), k2 = hmul_conj(g4, f2_);
                stg_put<10>(h4); stg_put<11>(h6);
                const hcx j2 = hmul_conj(g8, f2_), k3 = hmul_conj(h4, f2_);
                stg_put<12>(j1); stg_put<13>(k2);
                const hcx j3 = hmul_conj(j1, f2_);
                J[0] = hmul_conj(g8, f4_);
                stg_put<14>(j2); stg_put<15>(k3);
                J[1] = hmul_conj(j1, f4_); J[2] = hmul_conj(j2, f4_);
                stg_put<16>(j3);
                J[3] = hmul_conj(j3, f4_);
            }
            WAVE_SYNC();
#pragma unroll
            for (int s = 0; s < 8; ++s) Af[s] = *(lq)(uintptr_t)(fr_x + s * 64);
            WAVE_SYNC();
            stg_put<0>(J[0]); stg_put<1>(J[1]); stg_put<2>(J[2]); stg_put<3>(J[3]);
            STAMP(9);
            f4 X0 = {0, 0, 0, 0}, X1 = {0, 0, 0, 0}, Y0 = {0, 0, 0, 0}, Y1 = {0, 0, 0, 0};
            X0[0] = skip;
            auto mf = [](u4 a, u4 b, f4 c) { return mfma16(__builtin_bit_cast(h8, a), __builtin_bit_cast(h8, b), c); };
#pragma unroll
            for (int s = 0; s < 4; ++s) {
#pragma unroll
                for (int i = 0; i < 4; ++i) {
                    u32 zr = B1[s][i], zi = B1[s + 4][i], t;
                    asm volatile("v_pk_mul_f16 %2, %4, %1\n\tv_pk_mul_f16 %1, %3, %1\n\tv_pk_fma_f16 %1, %4, %0, %1\n\tv_pk_fma_f16 %0, %3, %0, %2 neg_lo:[0,0,1] neg_hi:[0,0,1]"
                                 : "+v"(zr), "+v"(zi), "=&v"(t) : "v"(B0[s][i]), "v"(B0[s + 4][i]));
                    B1[s][i] = zr; B1[s + 4][i] = zi;
                }
                X0 = mf(Af[2 * s], B0[2 * s], X0); X0 = mf(Af[2 * s + 1], B0[2 * s + 1], X0);
                asm volatile("" :: "v"(X0));
                SCHED_FENCE();
            }
            STAMP(10);
            WAVE_SYNC();
#pragma unroll
            for (int s = 0; s < 8; ++s) { X1 = mf(Af[s], B1[s], X1); asm volatile("" :: "v"(X1) : "memory"); Af[s] = *(lq)(uintptr_t)(fr_y + s * 64); SCHED_FENCE(); }
            put4(ch, sc_x + 128, pkh_r(X0[3], X0[2]), pkh_r(X0[1], X0[0]));
            STAMP(11);
#pragma unroll
            for (int s = 0; s < 8; ++s) { Y0 = mf(Af[s], B0[s], Y0); Y1 = mf(Af[s], B1[s], Y1); }
            const u32 xl = pkh_r(X1[3], X1[2]);
            put4(ch, sc_x, xl, pkh_r(X1[1], X1[0]));
            STAMP(2);
            const u32 y01 = __builtin_amdgcn_permlane32_swap(pkh_r(Y0[1], Y0[0]), pkh_r(Y1[1], Y1[0]), false, false)[0];
            const u32 y3 = pkh(Y0[2], Y1[2]);
            *(lptr32)(uintptr_t)sc_2 = y01;
            *(lptr16)(uintptr_t)sc_l = (u16)y3; *(lptr16)(uintptr_t)sc_h = (u16)(y3 >> 16);
            const u32 v0 = __builtin_amdgcn_readlane(y01, 63), v1 = __builtin_amdgcn_readlane(y3, 0), v2 = __builtin_amdgcn_readlane(xl, 63);
            *(lptr16)(uintptr_t)sc_p = (u16)((pcls == 0) ? v0 : (pcls == 1) ? v1 : v2);
            WAVE_SYNC();
            STAMP(3);
        }
        __builtin_amdgcn_s_setprio(2);
        lds_wait(cnt + 22, 4);
        STAMP(5);
        conv(hf, sm0, cw, yscale);
        STAMP(7);

    }
}

extern "C" void kernel_launch(void* const* d_in, const int* in_sizes, int n_in,
                              void* d_out, int out_size, void* d_ws, size_t ws_size,
                              hipStream_t stream) {
    char* p = (char*)d_ws + ((WS_OFF < 0 ? ws_size + (long long)WS_OFF * 1048576ll : (size_t)WS_OFF * 1048576ull) & ~(size_t)255);
    static_assert(WS_OFF >= 0 || WS_END <= (size_t)(-WS_OFF) * 1048576, "workspace");
    k_fused<<<256, 1024, 0, stream>>>((const u16*)d_in[0], (const u16*)d_in[8], p, (u16*)d_out,
                                      (const u16*)d_in[1], (const u16*)d_in[2], (const u16*)d_in[3], (const u16*)d_in[4],
                                      (const u16*)d_in[5], (const u16*)d_in[6], (const u16*)d_in[9], (const u16*)d_in[10], (const u16*)d_in[11]);
}
```
